```python
import torch

def _softmax(x, axis=-1):
    return torch.softmax(x, dim=axis)
def _gelu(x):
    return torch.nn.functional.gelu(x, approximate="tanh")
def _silu(x):
    return torch.nn.functional.silu(x)
def _sigmoid(x):
    return torch.sigmoid(x)
def _layernorm(x, axis=-1, eps=1e-5):
    m = x.mean(dim=axis, keepdim=True)
    v = x.var(dim=axis, keepdim=True, unbiased=False)
    return (x - m) / torch.sqrt(v + eps)
def _scatter_add(tgt, idx, src, axis=0):
    return tgt.clone().index_add_(axis, idx, src)
def _segment_sum(src, seg, n):
    out = torch.zeros((n,) + src.shape[1:], dtype=src.dtype)
    return out.index_add_(0, seg, src)
def _pad(x, pw):
    flat = tuple(v for pair in reversed(pw) for v in pair)
    return torch.nn.functional.pad(x, flat)

kernel_name = 'sequential_preLN__ssm_conv_s4__dense_activation_relu__prompt'

def setup_inputs():

    _w = (lambda a: (lambda t: t.to(torch.bfloat16) if t.is_floating_point() else t)(torch.as_tensor(a)))
    def _A_setup():
        B, L, D, N = 1, 2048, 640, 128
        A_re = -(lambda _x: torch.exp(torch.as_tensor(_x)))((lambda *s: torch.randn(*s, dtype=torch.bfloat16) * 3.2)(D, N))
        A_im = torch.pi * (lambda *s: torch.randn(*s, dtype=torch.bfloat16) * 0.05)(D, N)
        B_re = (lambda *s: torch.randn(*s, dtype=torch.bfloat16) * 0.1)(D, N)
        B_im = (lambda *s: torch.randn(*s, dtype=torch.bfloat16) * 0.1)(D, N)
        C_re = (lambda *s: torch.randn(*s, dtype=torch.bfloat16) * 0.1)(D, N)
        C_im = (lambda *s: torch.randn(*s, dtype=torch.bfloat16) * 0.1)(D, N)
        log_dt = (lambda *s: torch.randn(*s, dtype=torch.bfloat16) * 0.05)(D) * 0.5 - 4.0
        Dskip = (lambda *s: torch.randn(*s, dtype=torch.bfloat16) * 0.05)(D)
        return {
            "u": _w((lambda *s: torch.randn(*s, dtype=torch.bfloat16) * 0.05)(B, L, D)),
            "A_re": _w(A_re),
            "A_im": _w(A_im),
            "B_re": _w(B_re),
            "B_im": _w(B_im),
            "C_re": _w(C_re),
            "C_im": _w(C_im),
            "log_dt": _w(log_dt),
            "Dskip": _w(Dskip),
        }
    _A_in = _A_setup()

    def _B_setup():
        B, S, D, Dff = 1, 2048, 640, 1728
        return {
            "x": _w((lambda *s: torch.randn(*s, dtype=torch.bfloat16) * 0.05)(B, S, D)),
            "w1": _w((lambda *s: torch.randn(*s, dtype=torch.bfloat16) * 0.05)(D, Dff)),
            "w2": _w((lambda *s: torch.randn(*s, dtype=torch.bfloat16) * 0.05)(Dff, D)),
        }
    _B_in = _B_setup()

    return {
        "A_u": _A_in["u"],
        "A_A_re": _A_in["A_re"],
        "A_A_im": _A_in["A_im"],
        "A_B_re": _A_in["B_re"],
        "A_B_im": _A_in["B_im"],
        "A_C_re": _A_in["C_re"],
        "A_C_im": _A_in["C_im"],
        "A_log_dt": _A_in["log_dt"],
        "A_Dskip": _A_in["Dskip"],
        "B_x": _B_in["x"],
        "B_w1": _B_in["w1"],
        "B_w2": _B_in["w2"],
    }

def reference(A_u, A_A_re, A_A_im, A_B_re, A_B_im, A_C_re, A_C_im, A_log_dt, A_Dskip, B_x, B_w1, B_w2):
    def _A_ref(u, A_re, A_im, B_re, B_im, C_re, C_im, log_dt, Dskip):
        Bsz, L, D = u.shape
        dt = torch.exp(log_dt)[:, None]
        A = A_re + 1j * A_im
        Bc = B_re + 1j * B_im
        Cc = C_re + 1j * C_im
        logdA = dt * A
        dB = dt * Bc
        l = (lambda x, dt: x.to(dt))(torch.arange(L), A_re.dtype)
        P = torch.exp(logdA[:, :, None] * l[None, None, :])
        K = torch.einsum("dn,dnl,dn->dl", Cc, P, dB).real
        L2 = 2 * L
        Kp = _pad(K, ((0, 0), (0, L)))
        up = (lambda x, *d: x.permute(*d))(u, 0, 2, 1)
        up = _pad(up, ((0, 0), (0, 0), (0, L)))
        Kf = (lambda x, axes=(-2,-1): torch.fft.rfft2(x.float(), dim=axes))(Kp[:, None, :], axes=(-2, -1))
        Uf = (lambda x, axes=(-2,-1): torch.fft.rfft2(x.float(), dim=axes))(up[:, :, None, :], axes=(-2, -1))
        Yf = Uf * Kf[None, :, :, :]
        y = (lambda x, s, axes=(-2,-1): torch.fft.irfft2(x, s=s, dim=axes).to(torch.bfloat16))(Yf, (1, L2), axes=(-2, -1))[:, :, 0, :L]
        y = (lambda x, *d: x.permute(*d))(y, 0, 2, 1) + u * Dskip[None, None, :]
        return y

    def _B_ref(x, w1, w2):
        h = torch.einsum("bsd,df->bsf", x, w1)
        zero = torch.zeros_like(h)
        h = torch.maximum(h, zero)
        return torch.einsum("bsf,fd->bsd", h, w2)

    x = A_u
    h = x + _A_ref(_layernorm(x, axis=-1), A_A_re, A_A_im, A_B_re, A_B_im, A_C_re, A_C_im, A_log_dt, A_Dskip)
    h2 = h + _B_ref(_layernorm(h, axis=-1), B_w1, B_w2)
    return h2
```

```cpp
#include <hip/hip_runtime.h>
#include <hip/hip_bf16.h>
#define ROUND_L 1
typedef unsigned short u16;
constexpr int L=2048, D=640, N=128, F=1728;
__device__ inline float b2f(u16 v){ return __uint_as_float(((unsigned)v)<<16); }
__device__ inline u16 f2b(float f){ unsigned u=__float_as_uint(f); return (u16)((u+0x7FFF+((u>>16)&1))>>16); }
__device__ inline float rb(float f){ return b2f(f2b(f)); }

__global__ void k_ln(const u16* x, u16* u){
  int row=blockIdx.x; int t=threadIdx.x;
  float v[10]; float s=0;
  for(int i=0;i<10;i++){ v[i]=b2f(x[row*D+t+64*i]); s+=v[i]; }
  for(int o=32;o;o>>=1) s+=__shfl_xor(s,o);
  float m=s/D; float q=0;
  for(int i=0;i<10;i++){ float d=v[i]-m; q+=d*d; }
  for(int o=32;o;o>>=1) q+=__shfl_xor(q,o);
  float r=rsqrtf(q/D+1e-5f);
  for(int i=0;i<10;i++) u[row*D+t+64*i]=f2b((v[i]-m)*r);
}
__global__ void k_kgen(const u16* Are,const u16* Aim,const u16* Bre,const u16* Bim,const u16* Cre,const u16* Cim,const u16* ldt,float* K){
  int d=blockIdx.y; int l=blockIdx.x*256+threadIdx.x;
  __shared__ float ar[N],ai[N],wr[N],wi[N];
  float dt=rb(expf(b2f(ldt[d])));
  if(threadIdx.x<N){int n=threadIdx.x; ar[n]=dt*b2f(Are[d*N+n]); ai[n]=dt*b2f(Aim[d*N+n]);
    float br=dt*b2f(Bre[d*N+n]), bi=dt*b2f(Bim[d*N+n]); float cr=b2f(Cre[d*N+n]), ci=b2f(Cim[d*N+n]);
    wr[n]=cr*br-ci*bi; wi[n]=cr*bi+ci*br;}
  __syncthreads();
  float lf = ROUND_L ? rb((float)l) : (float)l;
  float acc=0;
  for(int n=0;n<N;n++){ float e=expf(ar[n]*lf); float s,c; sincosf(ai[n]*lf,&s,&c); acc+=e*(wr[n]*c-wi[n]*s);}
  K[l*D+d]=acc;
}
__global__ void k_conv(const u16* x,const u16* u,const float* K,const u16* Dk,u16* h){
  int l=blockIdx.x;
  for(int d=threadIdx.x; d<D; d+=blockDim.x){
    float acc=0;
    for(int j=0;j<=l;j++) acc+=K[j*D+d]*b2f(u[(l-j)*D+d]);
    float y=rb(rb(acc)+rb(b2f(u[l*D+d])*b2f(Dk[d])));
    h[l*D+d]=f2b(b2f(x[l*D+d])+y);
  }
}
__global__ void k_g1(const u16* a,const u16* w,u16* o){
  int l=blockIdx.y; int f=blockIdx.x*192+threadIdx.x;
  float acc=0; for(int k=0;k<D;k++) acc+=b2f(a[l*D+k])*b2f(w[k*F+f]);
  o[l*F+f]=f2b(fmaxf(acc,0.f));
}
__global__ void k_g2(const u16* a,const u16* w,const u16* h,u16* o){
  int l=blockIdx.y; int f=blockIdx.x*128+threadIdx.x;
  float acc=0; for(int k=0;k<F;k++) acc+=b2f(a[l*F+k])*b2f(w[k*D+f]);
  o[l*D+f]=f2b(b2f(h[l*D+f])+rb(acc));
}
extern "C" void kernel_launch(void* const* d_in, const int* in_sizes, int n_in, void* d_out, int out_size, void* d_ws, size_t ws_size, hipStream_t stream){
  const u16* x=(const u16*)d_in[0];
  char* ws=(char*)d_ws;
  u16* u=(u16*)ws; float* K=(float*)(ws+(4<<20)); u16* h=(u16*)(ws+(12<<20)); u16* u2=(u16*)(ws+(16<<20)); u16* hid=(u16*)(ws+(20<<20));
  k_ln<<<L,64,0,stream>>>(x,u);
  k_kgen<<<dim3(L/256,D),256,0,stream>>>((const u16*)d_in[1],(const u16*)d_in[2],(const u16*)d_in[3],(const u16*)d_in[4],(const u16*)d_in[5],(const u16*)d_in[6],(const u16*)d_in[7],K);
  k_conv<<<L,640,0,stream>>>(x,u,K,(const u16*)d_in[8],h);
  k_ln<<<L,64,0,stream>>>(h,u2);
  k_g1<<<dim3(F/192,L),192,0,stream>>>(u2,(const u16*)d_in[10],hid);
  k_g2<<<dim3(D/128,L),128,0,stream>>>(hid,(const u16*)d_in[11],h,(u16*)d_out);
}
```

```cpp
#include <hip/hip_runtime.h>

typedef unsigned short u16;
typedef unsigned u32;
typedef unsigned long long u64;
typedef u32 u32x2 __attribute__((ext_vector_type(2)));
typedef u32 u32x4 __attribute__((ext_vector_type(4)));
typedef float f32x2 __attribute__((ext_vector_type(2)));
typedef float f32x4 __attribute__((ext_vector_type(4)));
typedef float f32x16 __attribute__((ext_vector_type(16)));
typedef _Float16 f16x2 __attribute__((ext_vector_type(2)));
typedef _Float16 f16x8 __attribute__((ext_vector_type(8)));
typedef __bf16 bf16x2 __attribute__((ext_vector_type(2)));
typedef __bf16 bf16x8 __attribute__((ext_vector_type(8)));
typedef short s16x4 __attribute__((ext_vector_type(4)));
#define LAS __attribute__((address_space(3)))
#define DI __device__ __forceinline__
#define BC(T, v) __builtin_bit_cast(T, v)

constexpr int L = 2048, D = 640, F = 1728;
constexpr u32 WS_E = 0;
constexpr u32 WS_F2 = 4096;
constexpr u32 WS_F3 = WS_F2 + 8 * 4096;
constexpr u32 WS_ST = 65536;
constexpr u32 WS_H = 1u << 20;
constexpr u32 WS_HID = 4u << 20;
constexpr u32 WS_END = 12u << 20;
constexpr u32 H_PANEL = 160 * 1024, HID_PANEL = 432 * 1024;
constexpr int QSZ = 40960, SLOT1 = 7168, SLOT2 = 5120;
constexpr int KT_ROW = 528, KT_HI = 32 * KT_ROW;
constexpr int K_OFF = 27520, K_COPY = 4160;
constexpr int U_OFF = 35840, U_HALF = 2368;
constexpr int AUX_SC1 = 16;
constexpr u32 SPIN_CAP = 1u << 18;

#define STAMP(k)

DI float lo16(u32 v) { return BC(float, v << 16); }
DI float hi16(u32 v) { return BC(float, v & 0xffff0000u); }
DI u32 pk_bf16(float a, float b) { return BC(u32, __builtin_convertvector((f32x2){a, b}, bf16x2)); }
DI u32 pk_f16(float a, float b) { return BC(u32, __builtin_convertvector((f32x2){a, b}, f16x2)); }
DI float dot2(u32 a, u32 b, float c) { return __builtin_amdgcn_fdot2_f32_bf16(BC(bf16x2, a), BC(bf16x2, b), c, false); }
DI float rbf(float a) { return lo16(pk_bf16(a, 0.f)); }
template <int CTRL, int RM> DI float dpp_add(float v) {
  return v + BC(float, __builtin_amdgcn_update_dpp(0, BC(int, v), CTRL, RM, 0xf, true));
}
DI float sum16(float v) {
  v = dpp_add<0x121, 0xf>(v); v = dpp_add<0x122, 0xf>(v); v = dpp_add<0x124, 0xf>(v); return dpp_add<0x128, 0xf>(v);
}
struct Cx { f32x2 c, r; };
DI Cx mk(f32x2 c) { Cx o; o.c = c; o.r = (f32x2){-c.y, c.x}; return o; }
DI f32x2 cmul(f32x2 v, const Cx& m) { return __builtin_elementwise_fma(v.yy, m.r, v.xx * m.c); }
DI f32x2 conj(f32x2 v) { return (f32x2){v.x, -v.y}; }
DI void dma16(u32 lds_addr, u32 voff, __amdgpu_buffer_rsrc_t rs, u32 soff) {
  u32 keep;
  asm volatile("s_mov_b32 %0, m0\n\ts_mov_b32 m0, %1\n\ts_nop 3\n\tbuffer_load_dwordx4 %2, %3, %4 offen lds\n\ts_mov_b32 m0, %0"
               : "=&s"(keep) : "s"(lds_addr), "v"(voff), "s"(rs), "s"(soff) : "memory");
}
#define WAIT_VM0() asm volatile("s_waitcnt vmcnt(0)" ::: "memory")
DI f32x4 mfma_bf(u32x4 a, u32x4 b, f32x4 c) { return __builtin_amdgcn_mfma_f32_16x16x32_bf16(BC(bf16x8, a), BC(bf16x8, b), c, 0, 0, 0); }
DI f32x4 mfma_h(u32x4 a, u32x4 b, f32x4 c) { return __builtin_amdgcn_mfma_f32_16x16x32_f16(BC(f16x8, a), BC(f16x8, b), c, 0, 0, 0); }
DI u32x4 tr_frag(LAS char* p, int t_stride) {
  u32x2 a = BC(u32x2, __builtin_amdgcn_ds_read_tr16_b64_v4i16((LAS s16x4*)p));
  u32x2 b = BC(u32x2, __builtin_amdgcn_ds_read_tr16_b64_v4i16((LAS s16x4*)(p + t_stride)));
  return (u32x4){a.x, a.y, b.x, b.y};
}

__global__ __launch_bounds__(256, 1) void mega(const u16* __restrict__ x, const u16* __restrict__ Are, const u16* __restrict__ Aim,
                                               const u16* __restrict__ Bre, const u16* __restrict__ Bim, const u16* __restrict__ Cre,
                                               const u16* __restrict__ Cim, const u16* __restrict__ logdt, const u16* __restrict__ Dskip,
                                               const u16* __restrict__ w1, const u16* __restrict__ w2, u16* __restrict__ out, char* ws) {
  __shared__ __attribute__((aligned(16))) char smem[4 * QSZ];
  LAS char* const lds = (LAS char*)smem;
  const u32 lds0 = (u32)(size_t)lds;
  const int b = blockIdx.x, tid = threadIdx.x, lane = tid & 63;
  const int w = __builtin_amdgcn_readfirstlane(tid >> 6);
  const int xcd = b & 7, idx = b >> 3;
  const int r16 = lane & 15, q = lane >> 4;
  STAMP(0);
  const __amdgpu_buffer_rsrc_t rsW = __builtin_amdgcn_make_buffer_rsrc(ws, 0, WS_END, 0x00020000);
  const __amdgpu_buffer_rsrc_t rs1 = __builtin_amdgcn_make_buffer_rsrc((void*)w1, 0, D * F * 2, 0x00020000);
  const __amdgpu_buffer_rsrc_t rs2 = __builtin_amdgcn_make_buffer_rsrc((void*)w2, 0, D * F * 2, 0x00020000);
  const u32 Ev = __builtin_amdgcn_raw_buffer_load_b32(rsW, WS_E, 0, AUX_SC1);
  LAS char* const Q = lds + w * QSZ;
  const int pm = 2 * xcd + (idx >> 4), jn = idx & 15;
  u32 tag;

  auto w1_dma = [&](int s0, int s1) {
#pragma unroll
    for (int i = 0; i < 7; i++) {
      const int n = i * 64 + lane, row = n / 14, ck = n - row * 14;
      const u32 voff = row * (F * 2) + jn * 216 + ck * 16 - (ck == 13 ? 8 : 0);
      for (int s = s0; s < s1; s++) dma16(lds0 + w * QSZ + s * SLOT1 + i * 1024, voff, rs1, (5 * w + s) * 32 * (F * 2));
    }
  };

  if (idx < 20) {
    const int g = xcd * 20 + idx, ch = 4 * g + w;
    const u32 pAre = ((const u32*)Are)[ch * 64 + lane], pAim = ((const u32*)Aim)[ch * 64 + lane];
    const u32 pBre = ((const u32*)Bre)[ch * 64 + lane], pBim = ((const u32*)Bim)[ch * 64 + lane];
    const u32 pCre = ((const u32*)Cre)[ch * 64 + lane], pCim = ((const u32*)Cim)[ch * 64 + lane];
    const float ldt = lo16(logdt[ch]), dsk = lo16(Dskip[ch]);
    __builtin_amdgcn_sched_barrier(0);
    u32x2 xc[4][2];
#pragma unroll
    for (int j = 0; j < 4; j++)
#pragma unroll
      for (int e = 0; e < 2; e++) xc[j][e] = *(const u32x2*)(x + (512 * w + 128 * j + 2 * lane + e) * D + 4 * g);
    __builtin_amdgcn_sched_barrier(0);

    const float dt = rbf(__builtin_amdgcn_exp2f(ldt * 1.44269504088896f));
    f32x2 W[2], l1[2], l8[2], l32[2], l256[2];
    float S = 0.f;
#pragma unroll
    for (int m = 0; m < 2; m++) {
      const float are = dt * (m ? hi16(pAre) : lo16(pAre)), aim = dt * (m ? hi16(pAim) : lo16(pAim));
      const float br = dt * (m ? hi16(pBre) : lo16(pBre)), bi = dt * (m ? hi16(pBim) : lo16(pBim));
      const float cr = m ? hi16(pCre) : lo16(pCre), ci = m ? hi16(pCim) : lo16(pCim);
      W[m] = (f32x2){cr * br - ci * bi, cr * bi + ci * br};
      S += __builtin_fabsf(W[m].x) + __builtin_fabsf(W[m].y);
      const float ar2 = are * 1.44269504088896f;
      const float th = aim * 0.15915494f;
      const float tl = __builtin_fmaf(aim, 6.4206383e-9f, __builtin_fmaf(aim, 0.15915494f, -th));
      auto cexp = [&](float l) {
        const float mag = __builtin_amdgcn_exp2f(ar2 * l);
        const float ph = __builtin_fmaf(tl, l, __builtin_amdgcn_fractf(th * l));
        return (f32x2){mag * __builtin_amdgcn_cosf(ph), mag * __builtin_amdgcn_sinf(ph)};
      };
      l1[m] = cexp(1.f); l8[m] = cexp(8.f); l32[m] = cexp(32.f); l256[m] = cexp(256.f);
    }
    STAMP(1);
    S = sum16(S); S = dpp_add<0x142, 0xa>(S); S = dpp_add<0x143, 0xc>(S);
    S = BC(float, __builtin_amdgcn_readlane(BC(int, S), 63)) + __builtin_fabsf(dsk);
    int ex = (int)(BC(u32, S) >> 23);
    ex = ex < 14 ? 14 : ex;
    const float scale = BC(float, (u32)(267 - ex) << 23), inv_scale = BC(float, (u32)(ex - 13) << 23);

    LAS char* const trow = Q + lane * 8;
    auto put = [&](int off, f32x2 a, f32x2 c) { *(LAS u32x2*)(trow + off) = (u32x2){pk_f16(a.x, a.y), pk_f16(c.x, c.y)}; };
    {
      f32x2 P[2][4];
      Cx st[2];
#pragma unroll
      for (int m = 0; m < 2; m++) {
        const Cx c8 = mk(l8[m]);
        P[m][0] = (f32x2){1.f, 0.f}; P[m][1] = l8[m]; P[m][2] = cmul(l8[m], c8); P[m][3] = cmul(P[m][2], c8);
        st[m] = mk(l32[m]);
      }
#pragma unroll
      for (int a = 0; a < 8; a++)
#pragma unroll
        for (int c = 0; c < 4; c++) {
          if (a == 1 && c == 0) { P[0][0] = l32[0]; P[1][0] = l32[1]; }
          else if (a) { P[0][c] = cmul(P[0][c], st[0]); P[1][c] = cmul(P[1][c], st[1]); }
          put((4 * a + c) * KT_ROW, P[0][c], P[1][c]);
        }
    }
    {
      f32x2 V[2][9];
      Cx mu[2];
#pragma unroll
      for (int m = 0; m < 2; m++) {
        const Cx c1 = mk(conj(l1[m]));
        V[m][0] = conj(W[m]) * scale;
#pragma unroll
        for (int s = 1; s < 9; s++) V[m][s] = cmul(V[m][s - 1], c1);
        mu[m] = mk(conj(l256[m]));
      }
#pragma unroll
      for (int s = 0; s < 8; s++) put(KT_HI + s * KT_ROW, V[0][s], V[1][s]);
      auto adv = [&](int s, int row) { V[0][s] = cmul(V[0][s], mu[0]); V[1][s] = cmul(V[1][s], mu[1]); put(KT_HI + row * KT_ROW, V[0][s], V[1][s]); };
      adv(0, 8); adv(2, 9); adv(4, 10); put(KT_HI + 12 * KT_ROW, V[0][4], V[1][4]); adv(6, 13); adv(8, 14);
      adv(0, 16); adv(4, 17); adv(8, 18);
      adv(0, 20); adv(4, 21); adv(8, 22);
#pragma unroll
      for (int z = 0; z < 4; z++) { adv(0, 24 + 2 * z); adv(8, 25 + 2 * z); }
    }
    STAMP(2);
    u32x4 st[4];
    const u32 st_off = WS_ST + (512 * w + 2 * lane) * 8;
#pragma unroll
    for (int j = 0; j < 4; j++) st[j] = __builtin_amdgcn_raw_buffer_load_b128(rsW, st_off + j * 1024, 0, AUX_SC1);
    __builtin_amdgcn_sched_barrier(0);
    f32x16 kd = {0.f, 0.f, 0.f, 0.f, 0.f, 0.f, 0.f, 0.f, 0.f, 0.f, 0.f, 0.f, 0.f, 0.f, 0.f, 0.f};
    const int r0 = lane & 31, hh = lane >> 5;
    {
      LAS char* const fr = Q + r0 * KT_ROW + hh * 16;
#pragma unroll
      for (int s = 0; s < 16; s++)
        kd = __builtin_amdgcn_mfma_f32_32x32x16_f16(*(LAS f16x8*)(fr + KT_HI + s * 32), *(LAS f16x8*)(fr + s * 32), kd, 0, 0, 0);
    }
    {
      if (lane == 0) kd[0] += dsk * scale;
      auto put2 = [&](int el, u32 d0, u32 d1) {
        LAS char* p = Q + K_OFF + el * 2;
        *(LAS u32x2*)p = (u32x2){d0, d1};
        *(LAS u32*)(p + K_COPY + 4) = d0; *(LAS u32*)(p + K_COPY + 8) = d1;
      };
      put2(2044 - 8 * r0 - 4 * hh, pk_f16(kd[3], kd[2]), pk_f16(kd[1], kd[0]));
      put2(2044 - 256 - 8 * r0 - 4 * hh, pk_f16(kd[6], kd[5]), pk_f16(kd[4], kd[4]));
      const int e2 = 2040 - 512 - 256 * hh - 8 * r0;
      put2(e2, pk_f16(kd[10], kd[10]), pk_f16(kd[9], kd[9]));
      put2(e2 + 4, pk_f16(kd[9], kd[8]), pk_f16(kd[8], kd[8]));
#pragma unroll
      for (int z = 0; z < 2; z++) {
        const float a = kd[12 + 2 * z], c = kd[13 + 2 * z], t = (r0 & 1) ? c : a;
        const int e3 = 2040 - 1024 - 512 * hh - 256 * z - 8 * r0;
        const u32 aa = pk_f16(a, a);
        put2(e3, pk_f16(c, c), pk_f16(c, t));
        put2(e3 + 4, aa, aa);
      }
      if (lane < 4) put2(2048 + 4 * lane, 0u, 0u);
      if (lane < 30) *(LAS u32x4*)(Q + U_OFF + (lane & 1) * U_HALF + (lane >> 1) * 16) = (u32x4){0u, 0u, 0u, 0u};
    }
    STAMP(3);
    tag = __builtin_amdgcn_readfirstlane(Ev) + 1u;
    for (u32 spin = 0;; spin++) {
      bool ok = true;
#pragma unroll
      for (int j = 0; j < 4; j++) ok &= (st[j].y == tag) & (st[j].w == tag);
      if (__all(ok) || spin > SPIN_CAP) break;
      __builtin_amdgcn_s_sleep(1);
      asm volatile("" ::: "memory");
#pragma unroll
      for (int j = 0; j < 4; j++) st[j] = __builtin_amdgcn_raw_buffer_load_b128(rsW, st_off + j * 1024, 0, AUX_SC1);
    }
    {
      LAS char* const up = lds + U_OFF + ((lane >> 2) & 1) * U_HALF + (32 * w + 15 + (lane >> 3)) * 16 + (lane & 3) * 4;
#pragma unroll
      for (int j = 0; j < 4; j++) {
        const float m0 = lo16(st[j].x), i0 = __builtin_amdgcn_rcpf(hi16(st[j].x));
        const float m1 = lo16(st[j].z), i1 = __builtin_amdgcn_rcpf(hi16(st[j].z));
        *(LAS u32*)(up + j * 128) = pk_f16((lo16(xc[j][0].x) - m0) * i0, (lo16(xc[j][1].x) - m1) * i1);
        *(LAS u32*)(up + j * 128 + QSZ) = pk_f16((hi16(xc[j][0].x) - m0) * i0, (hi16(xc[j][1].x) - m1) * i1);
        *(LAS u32*)(up + j * 128 + 2 * QSZ) = pk_f16((lo16(xc[j][0].y) - m0) * i0, (lo16(xc[j][1].y) - m1) * i1);
        *(LAS u32*)(up + j * 128 + 3 * QSZ) = pk_f16((hi16(xc[j][0].y) - m0) * i0, (hi16(xc[j][1].y) - m1) * i1);
      }
    }
    w1_dma(0, 3);
    __syncthreads();
    STAMP(4);
    f32x4 acc[8];
#pragma unroll
    for (int m = 0; m < 8; m++) acc[m] = (f32x4){0.f, 0.f, 0.f, 0.f};
    {
      const int e0 = 15 - r16 + 64 * q;
      const int d0 = (e0 >> 1) + 1;
      const u32 sh = (e0 & 1) * 16;
      LAS char* kp = Q + K_OFF + (d0 & 1) * (K_COPY + 4) + d0 * 4;
      LAS char* const up = Q + U_OFF + (r16 + 4 * q) * 16;
      u32 carry[8];
#pragma unroll
      for (int s1 = 0; s1 < 8; s1++) carry[s1] = *(LAS u32*)(kp - 4 + (7 - s1) * 512);
#pragma unroll 2
      for (int s0 = 0; s0 < 8; s0++) {
        u32x4 U[8], Kf[8];
#pragma unroll
        for (int s1 = 0; s1 < 8; s1++) {
          const u32x2 a = *(LAS u32x2*)(kp + s0 * 16 + (7 - s1) * 512), c = *(LAS u32x2*)(kp + s0 * 16 + (7 - s1) * 512 + 8);
          U[s1] = *(LAS u32x4*)(up + (s0 & 1) * U_HALF + (s0 >> 1) * 16 + s1 * 256);
          Kf[s1] = (u32x4){__builtin_amdgcn_alignbit(a.x, carry[s1], sh), __builtin_amdgcn_alignbit(a.y, a.x, sh),
                           __builtin_amdgcn_alignbit(c.x, a.y, sh), __builtin_amdgcn_alignbit(c.y, c.x, sh)};
          carry[s1] = c.y;
        }
#pragma unroll
        for (int s1 = 0; s1 < 8; s1++)
#pragma unroll
          for (int m = s1; m < 8; m++) acc[m] = mfma_h(Kf[s1], U[m - s1], acc[m]);
      }
    }
    STAMP(5);
#pragma unroll
    for (int m = 0; m < 8; m++) *(LAS f32x4*)(Q + K_OFF + m * 1024 + q * 256 + ((r16 ^ (4 * q)) * 16)) = acc[m] * inv_scale;
    __syncthreads();
    {
      const int yq = (lane >> 1) & 3;
#pragma unroll
      for (int j = 0; j < 4; j++) {
        LAS char* const yp = lds + K_OFF + (2 * w + (j >> 1)) * 1024 + yq * 256 + ((((lane >> 3) + 8 * (j & 1)) ^ (4 * yq)) * 16) + (lane & 1) * 8;
        const f32x2 y0 = *(LAS f32x2*)yp, y1 = *(LAS f32x2*)(yp + QSZ), y2 = *(LAS f32x2*)(yp + 2 * QSZ), y3 = *(LAS f32x2*)(yp + 3 * QSZ);
        const u32x4 hv = {pk_bf16(lo16(xc[j][0].x) + y0.x, hi16(xc[j][0].x) + y1.x), pk_bf16(lo16(xc[j][0].y) + y2.x, hi16(xc[j][0].y) + y3.x),
                          pk_bf16(lo16(xc[j][1].x) + y0.y, hi16(xc[j][1].x) + y1.y), pk_bf16(lo16(xc[j][1].y) + y2.y, hi16(xc[j][1].y) + y3.y)};
        __builtin_amdgcn_raw_buffer_store_b128(hv, rsW, WS_H + (4 * w + j) * H_PANEL + g * 1024 + lane * 16, 0, AUX_SC1);
      }
    }
    STAMP(6);
    WAIT_VM0();
    __syncthreads();
    STAMP(7);
    if (tid < 8) __builtin_amdgcn_raw_buffer_store_b32(tag, rsW, WS_F2 + tid * 4096 + b * 16, 0, AUX_SC1);
    w1_dma(3, 5);
  } else {
    const int hid = xcd * 12 + idx - 20;
    if (hid < 64) {
      u32x4 v[2][5];
#pragma unroll
      for (int p = 0; p < 2; p++)
#pragma unroll
        for (int c = 0; c < 5; c++) v[p][c] = *(const u32x4*)(x + (32 * hid + 8 * w + 4 * p + q) * D + (r16 + 16 * c) * 8);
      __builtin_amdgcn_sched_barrier(0);
      tag = __builtin_amdgcn_readfirstlane(Ev) + 1u;
#pragma unroll
      for (int p = 0; p < 2; p++) {
        float s = 0.f;
#pragma unroll
        for (int c = 0; c < 5; c++)
#pragma unroll
          for (int k = 0; k < 4; k++) s += lo16(v[p][c][k]) + hi16(v[p][c][k]);
        const float mean = sum16(s) * (1.f / D);
        float ss = 0.f;
#pragma unroll
        for (int c = 0; c < 5; c++)
#pragma unroll
          for (int k = 0; k < 4; k++) {
            const float a = lo16(v[p][c][k]) - mean, d = hi16(v[p][c][k]) - mean;
            ss = __builtin_fmaf(a, a, __builtin_fmaf(d, d, ss));
          }
        const float var = sum16(ss) * (1.f / D);
        const float den = __builtin_amdgcn_sqrtf(rbf(rbf(var) + 1e-5f));
        if (r16 == 0) __builtin_amdgcn_raw_buffer_store_b64((u32x2){pk_bf16(mean, den), tag}, rsW, WS_ST + (32 * hid + 8 * w + 4 * p + q) * 8, 0, AUX_SC1);
      }
    } else {
      tag = __builtin_amdgcn_readfirstlane(Ev) + 1u;
    }
    STAMP(1);
    if (tid < 8) __builtin_amdgcn_raw_buffer_store_b32(tag, rsW, WS_F2 + tid * 4096 + b * 16, 0, AUX_SC1);
    w1_dma(0, 5);
  }

  WAIT_VM0();
#pragma unroll
  for (int i = 0; i < 3; i++)
    if (i * 64 + lane < 160) *(LAS u32x2*)(Q + (i * 64 + lane) * 224 + 208) = (u32x2){0x3f803f80u, 0x3f803f80u};
  __syncthreads();
  LAS char* const w1p = lds + (lane >> 2) * 224 + (lane & 3) * 8;
  f32x4 cs[7];
#pragma unroll
  for (int nt = 0; nt < 7; nt++) cs[nt] = (f32x4){0.f, 0.f, 0.f, 0.f};
  for (int qq = 0; qq < 4; qq++)
#pragma unroll
    for (int s = 0; s < 5; s++)
#pragma unroll
      for (int nt = 0; nt < 7; nt++)
        cs[nt] = mfma_bf(tr_frag(w1p + qq * QSZ + s * SLOT1 + nt * 32, 16 * 224), (u32x4){0x3f803f80u, 0x3f803f80u, 0x3f803f80u, 0x3f803f80u}, cs[nt]);
  STAMP(8);
  if (w == 0) {
    for (u32 spin = 0;; spin++) {
      bool ok = true;
#pragma unroll
      for (int j = 0; j < 4; j++) ok &= __builtin_amdgcn_raw_buffer_load_b32(rsW, WS_F2 + xcd * 4096 + (lane + 64 * j) * 16, 0, AUX_SC1) == tag;
      if (__all(ok) || spin > SPIN_CAP) break;
      __builtin_amdgcn_s_sleep(1);
      asm volatile("" ::: "memory");
    }
    if (b == 0 && lane == 0) __builtin_amdgcn_raw_buffer_store_b32(tag, rsW, WS_E, 0, AUX_SC1);
  }
  __syncthreads();
  STAMP(9);
  {
    u32x4 X[20][2];
    const u32 hoff = WS_H + pm * H_PANEL + q * 1024 + w * 256 + r16 * 16;
#pragma unroll
    for (int s = 0; s < 20; s++) {
      X[s][0] = __builtin_amdgcn_raw_buffer_load_b128(rsW, hoff, s * 8192, AUX_SC1);
      X[s][1] = __builtin_amdgcn_raw_buffer_load_b128(rsW, hoff, s * 8192 + 4096, AUX_SC1);
      __builtin_amdgcn_sched_barrier(0);
    }
    f32x4 a1[7][2];
#pragma unroll
    for (int nt = 0; nt < 7; nt++) a1[nt][0] = a1[nt][1] = (f32x4){0.f, 0.f, 0.f, 0.f};
    float ss0 = 0.f, ss1 = 0.f;
#pragma unroll
    for (int s = 0; s < 20; s++) {
      const u32x4 f0 = {X[s][0].x, X[s][0].y, X[s][1].x, X[s][1].y}, f1 = {X[s][0].z, X[s][0].w, X[s][1].z, X[s][1].w};
#pragma unroll
      for (int k = 0; k < 4; k++) {
        ss0 = dot2(f0[k], f0[k], ss0);
        ss1 = dot2(f1[k], f1[k], ss1);
      }
#pragma unroll
      for (int nt = 0; nt < 7; nt++) {
        const u32x4 wf = tr_frag(w1p + (s / 5) * QSZ + (s % 5) * SLOT1 + nt * 32, 16 * 224);
        a1[nt][0] = mfma_bf(wf, f0, a1[nt][0]);
        a1[nt][1] = mfma_bf(wf, f1, a1[nt][1]);
      }
    }
    STAMP(10);
    ss0 += __shfl_xor(ss0, 16); ss1 += __shfl_xor(ss1, 16);
    ss0 += __shfl_xor(ss0, 32); ss1 += __shfl_xor(ss1, 32);
    const float mean0 = __shfl(a1[6][0].x, 32 + r16) * (1.f / D), mean1 = __shfl(a1[6][1].x, 32 + r16) * (1.f / D);
    const float rs0 = __builtin_amdgcn_rsqf(__builtin_fmaxf(ss0 * (1.f / D) - mean0 * mean0, 0.f) + 1e-5f);
    const float rs1v = __builtin_amdgcn_rsqf(__builtin_fmaxf(ss1 * (1.f / D) - mean1 * mean1, 0.f) + 1e-5f);
    const u32 ooff = WS_HID + pm * HID_PANEL + (27 * jn + q) * 1024 + w * 256 + r16 * 16;
#pragma unroll
    for (int nt = 0; nt < 7; nt++) {
      f32x4 v0 = (a1[nt][0] - mean0 * cs[nt]) * rs0, v1 = (a1[nt][1] - mean1 * cs[nt]) * rs1v;
#pragma unroll
      for (int k = 0; k < 4; k++) { v0[k] = __builtin_fmaxf(v0[k], 0.f); v1[k] = __builtin_fmaxf(v1[k], 0.f); }
      const u32x4 o = {pk_bf16(v0.x, v0.y), pk_bf16(v0.z, v0.w), pk_bf16(v1.x, v1.y), pk_bf16(v1.z, v1.w)};
      if (nt < 6) __builtin_amdgcn_raw_buffer_store_b128(o, rsW, ooff, nt * 4096, AUX_SC1);
      else if (q != 2) __builtin_amdgcn_raw_buffer_store_b128(o, rsW, ooff - (q == 3 ? 1024 : 0), 6 * 4096, AUX_SC1);
    }
  }
  WAIT_VM0();
  __syncthreads();
  STAMP(11);
  if (tid == 0) __builtin_amdgcn_raw_buffer_store_b32(tag, rsW, WS_F3 + pm * 256 + jn * 4, 0, AUX_SC1);

  const int half = (idx >> 3) & 1, ct = idx & 7;
  const int ks0 = 14 * w - (w == 3 ? 1 : 0);
  auto w2_dma = [&](int s0, int s1, int slot0) {
#pragma unroll
    for (int i = 0; i < 5; i++) {
      const int n = i * 64 + lane, row = n / 10, ck = n - row * 10;
      const u32 voff = row * (D * 2) + ct * 160 + ck * 16;
      for (int s = s0; s < s1; s++) dma16(lds0 + w * QSZ + (slot0 + s - s0) * SLOT2 + i * 1024, voff, rs2, (ks0 + s) * 32 * (D * 2));
    }
  };
  w2_dma(0, 8, 0);
  WAIT_VM0();
  LAS char* const w2p = Q + (lane >> 2) * 160 + (lane & 3) * 8;
  u32x4 wr[6][5];
#pragma unroll
  for (int s = 0; s < 6; s++)
#pragma unroll
    for (int nt = 0; nt < 5; nt++) wr[s][nt] = tr_frag(w2p + s * SLOT2 + nt * 32, 16 * 160);
#pragma unroll
  for (int s = 0; s < 6; s++)
#pragma unroll
    for (int nt = 0; nt < 5; nt++) asm volatile("" : "+v"(wr[s][nt]));
  w2_dma(8, 14, 0);
  STAMP(12);
  if (w == 0) {
    for (u32 spin = 0;; spin++) {
      const bool ok = __builtin_amdgcn_raw_buffer_load_b32(rsW, WS_F3 + pm * 256 + r16 * 4, 0, AUX_SC1) == tag;
      if (__all(ok) || spin > SPIN_CAP) break;
      __builtin_amdgcn_s_sleep(1);
      asm volatile("" ::: "memory");
    }
  }
  __syncthreads();
  STAMP(13);
  {
    u32x4 X[14][4];
    const u32 aoff = WS_HID + pm * HID_PANEL + (8 * ks0 + q) * 1024 + half * 512 + r16 * 16;
#pragma unroll
    for (int s = 0; s < 14; s++) {
      if (s < 13 || w < 2) {
        X[s][0] = __builtin_amdgcn_raw_buffer_load_b128(rsW, aoff, s * 8192, AUX_SC1);
        X[s][1] = __builtin_amdgcn_raw_buffer_load_b128(rsW, aoff, s * 8192 + 4096, AUX_SC1);
        X[s][2] = __builtin_amdgcn_raw_buffer_load_b128(rsW, aoff + 256, s * 8192, AUX_SC1);
        X[s][3] = __builtin_amdgcn_raw_buffer_load_b128(rsW, aoff + 256, s * 8192 + 4096, AUX_SC1);
      } else {
        X[s][0] = X[s][1] = X[s][2] = X[s][3] = (u32x4){0u, 0u, 0u, 0u};
      }
      __builtin_amdgcn_sched_barrier(0);
    }
    const int tok = half * 64 + 32 * (w >> 1) + 2 * r16 + (w & 1);
    u32x2 res[5];
#pragma unroll
    for (int nt = 0; nt < 5; nt++) res[nt] = __builtin_amdgcn_raw_buffer_load_b64(rsW, WS_H + pm * H_PANEL + (ct * 20 + q) * 1024 + tok * 8, nt * 4096, AUX_SC1);
    __builtin_amdgcn_sched_barrier(0);
    f32x4 a2[4][5];
#pragma unroll
    for (int mt = 0; mt < 4; mt++)
#pragma unroll
      for (int nt = 0; nt < 5; nt++) a2[mt][nt] = (f32x4){0.f, 0.f, 0.f, 0.f};
#pragma unroll
    for (int s = 0; s < 14; s++) {
      u32x4 f[4];
#pragma unroll
      for (int hh = 0; hh < 2; hh++) {
        f[2 * hh] = (u32x4){X[s][2 * hh].x, X[s][2 * hh].y, X[s][2 * hh + 1].x, X[s][2 * hh + 1].y};
        f[2 * hh + 1] = (u32x4){X[s][2 * hh].z, X[s][2 * hh].w, X[s][2 * hh + 1].z, X[s][2 * hh + 1].w};
      }
      if (s == 6) {
        asm volatile("" : "+v"(a2[0][0]) :: "memory");
      }
#pragma unroll
      for (int nt = 0; nt < 5; nt++) {
        const u32x4 wf = s < 6 ? wr[s][nt] : tr_frag(w2p + (s < 8 ? s : s - 8) * SLOT2 + nt * 32, 16 * 160);
#pragma unroll
        for (int mt = 0; mt < 4; mt++) a2[mt][nt] = mfma_bf(wf, f[mt], a2[mt][nt]);
      }
    }
    STAMP(14);
#pragma unroll
    for (int mt = 0; mt < 4; mt++)
#pragma unroll
      for (int nt = 0; nt < 5; nt++) *(LAS f32x4*)(Q + (mt * 5 + nt) * 1024 + lane * 16) = a2[mt][nt];
    __syncthreads();
#pragma unroll
    for (int nt = 0; nt < 5; nt++) {
      LAS char* const p = lds + (w * 5 + nt) * 1024 + lane * 16;
      const f32x4 v = (*(LAS f32x4*)p + *(LAS f32x4*)(p + QSZ)) + (*(LAS f32x4*)(p + 2 * QSZ) + *(LAS f32x4*)(p + 3 * QSZ));
      const u32x2 o = {pk_bf16(lo16(res[nt].x) + v.x, hi16(res[nt].x) + v.y), pk_bf16(lo16(res[nt].y) + v.z, hi16(res[nt].y) + v.w)};
      *(u32x2*)(out + (pm * 128 + tok) * D + ct * 80 + nt * 16 + q * 4) = o;
    }
  }
  STAMP(15);
}

extern "C" void kernel_launch(void* const* d_in, const int* in_sizes, int n_in, void* d_out, int out_size, void* d_ws, size_t ws_size,
                              hipStream_t stream) {
  mega<<<256, 256, 0, stream>>>((const u16*)d_in[0], (const u16*)d_in[1], (const u16*)d_in[2], (const u16*)d_in[3], (const u16*)d_in[4],
                                (const u16*)d_in[5], (const u16*)d_in[6], (const u16*)d_in[7], (const u16*)d_in[8], (const u16*)d_in[10],
                                (const u16*)d_in[11], (u16*)d_out, (char*)d_ws);
}
```

```cpp
#include <hip/hip_runtime.h>

typedef unsigned short u16;
typedef unsigned u32;
typedef unsigned long long u64;
typedef u32 u32x2 __attribute__((ext_vector_type(2)));
typedef u32 u32x4 __attribute__((ext_vector_type(4)));
typedef float f32x2 __attribute__((ext_vector_type(2)));
typedef float f32x4 __attribute__((ext_vector_type(4)));
typedef float f32x16 __attribute__((ext_vector_type(16)));
typedef _Float16 f16x2 __attribute__((ext_vector_type(2)));
typedef _Float16 f16x8 __attribute__((ext_vector_type(8)));
typedef __bf16 bf16x2 __attribute__((ext_vector_type(2)));
typedef __bf16 bf16x8 __attribute__((ext_vector_type(8)));
typedef short s16x4 __attribute__((ext_vector_type(4)));
#define LAS __attribute__((address_space(3)))
#define DI __device__ __forceinline__
#define BC(T, v) __builtin_bit_cast(T, v)

constexpr int L = 2048, D = 640, F = 1728;
constexpr u32 WS_E = 0;
constexpr u32 WS_F2 = 4096;
constexpr u32 WS_F3 = WS_F2 + 8 * 4096;
constexpr u32 WS_ST = 65536;
constexpr u32 WS_H = 1u << 20;
constexpr u32 WS_HID = 4u << 20;
constexpr u32 WS_END = 12u << 20;
constexpr u32 H_PANEL = 160 * 1024, HID_PANEL = 432 * 1024;
constexpr int QSZ = 40960, SLOT1 = 7168, SLOT2 = 5120;
constexpr int KT_ROW = 528, KT_HI = 32 * KT_ROW;
constexpr int K_OFF = 27520, K_COPY = 4160;
constexpr int U_OFF = 35840, U_HALF = 2368;
constexpr int AUX_SC1 = 16;
constexpr u32 SPIN_CAP = 1u << 18;

#define STAMP(k)

DI float lo16(u32 v) { return BC(float, v << 16); }
DI float hi16(u32 v) { return BC(float, v & 0xffff0000u); }
DI u32 pk_bf16(float a, float b) { return BC(u32, __builtin_convertvector((f32x2){a, b}, bf16x2)); }
DI u32 pk_f16(float a, float b) { return BC(u32, __builtin_convertvector((f32x2){a, b}, f16x2)); }
DI float dot2(u32 a, u32 b, float c) { return __builtin_amdgcn_fdot2_f32_bf16(BC(bf16x2, a), BC(bf16x2, b), c, false); }
DI float rbf(float a) { return lo16(pk_bf16(a, 0.f)); }
template <int CTRL, int RM> DI float dpp_add(float v) {
  return v + BC(float, __builtin_amdgcn_update_dpp(0, BC(int, v), CTRL, RM, 0xf, true));
}
DI float sum16(float v) {
  v = dpp_add<0x121, 0xf>(v); v = dpp_add<0x122, 0xf>(v); v = dpp_add<0x124, 0xf>(v); return dpp_add<0x128, 0xf>(v);
}
struct Cx { f32x2 c, r; };
DI Cx mk(f32x2 c) { Cx o; o.c = c; o.r = (f32x2){-c.y, c.x}; return o; }
DI f32x2 cmul(f32x2 v, const Cx& m) { return __builtin_elementwise_fma(v.yy, m.r, v.xx * m.c); }
DI f32x2 conj(f32x2 v) { return (f32x2){v.x, -v.y}; }
DI void dma16(u32 lds_addr, u32 voff, __amdgpu_buffer_rsrc_t rs, u32 soff) {
  u32 keep;
  asm volatile("s_mov_b32 %0, m0\n\ts_mov_b32 m0, %1\n\ts_nop 3\n\tbuffer_load_dwordx4 %2, %3, %4 offen lds\n\ts_mov_b32 m0, %0"
               : "=&s"(keep) : "s"(lds_addr), "v"(voff), "s"(rs), "s"(soff) : "memory");
}
#define WAIT_VM0() asm volatile("s_waitcnt vmcnt(0)" ::: "memory")
DI f32x4 mfma_bf(u32x4 a, u32x4 b, f32x4 c) { return __builtin_amdgcn_mfma_f32_16x16x32_bf16(BC(bf16x8, a), BC(bf16x8, b), c, 0, 0, 0); }
DI f32x4 mfma_h(u32x4 a, u32x4 b, f32x4 c) { return __builtin_amdgcn_mfma_f32_16x16x32_f16(BC(f16x8, a), BC(f16x8, b), c, 0, 0, 0); }
DI u32x4 tr_frag(LAS char* p, int t_stride) {
  u32x2 a = BC(u32x2, __builtin_amdgcn_ds_read_tr16_b64_v4i16((LAS s16x4*)p));
  u32x2 b = BC(u32x2, __builtin_amdgcn_ds_read_tr16_b64_v4i16((LAS s16x4*)(p + t_stride)));
  return (u32x4){a.x, a.y, b.x, b.y};
}

__global__ __launch_bounds__(256, 1) void mega(const u16* __restrict__ x, const u16* __restrict__ Are, const u16* __restrict__ Aim,
                                               const u16* __restrict__ Bre, const u16* __restrict__ Bim, const u16* __restrict__ Cre,
                                               const u16* __restrict__ Cim, const u16* __restrict__ logdt, const u16* __restrict__ Dskip,
                                               const u16* __restrict__ w1, const u16* __restrict__ w2, u16* __restrict__ out, char* ws) {
  __shared__ __attribute__((aligned(16))) char smem[4 * QSZ];
  LAS char* const lds = (LAS char*)smem;
  const u32 lds0 = (u32)(size_t)lds;
  const int b = blockIdx.x, tid = threadIdx.x, lane = tid & 63;
  const int w = __builtin_amdgcn_readfirstlane(tid >> 6);
  const int xcd = b & 7, idx = b >> 3;
  const int r16 = lane & 15, q = lane >> 4;
  STAMP(0);
  u32 pfv = 0;
  {
    u64 pc; u32 len;
    asm volatile("s_getpc_b64 %0\n.Lmega_pf:\n\ts_mov_b32 %1, .Lmega_end-.Lmega_pf" : "=s"(pc), "=s"(len));
    const __amdgpu_buffer_rsrc_t rsC = __builtin_amdgcn_make_buffer_rsrc((void*)pc, 0, len, 0x00020000);
    if (tid < 16) pfv = __builtin_amdgcn_raw_buffer_load_b32(rsC, (tid * 32 + idx) * 128, 0, 0);
  }
  const __amdgpu_buffer_rsrc_t rsW = __builtin_amdgcn_make_buffer_rsrc(ws, 0, WS_END, 0x00020000);
  const __amdgpu_buffer_rsrc_t rs1 = __builtin_amdgcn_make_buffer_rsrc((void*)w1, 0, D * F * 2, 0x00020000);
  const __amdgpu_buffer_rsrc_t rs2 = __builtin_amdgcn_make_buffer_rsrc((void*)w2, 0, D * F * 2, 0x00020000);
  const u32 Ev = __builtin_amdgcn_raw_buffer_load_b32(rsW, WS_E, 0, AUX_SC1);
  LAS char* const Q = lds + w * QSZ;
  const int pm = 2 * xcd + (idx >> 4), jn = idx & 15;
  u32 tag;
  u32 tv = 0;
  if (w == 1) {
    if (lane < 34) tv = __builtin_amdgcn_raw_buffer_load_b32(rs2, lane * 65536, 0, 0);
  } else if (w == 2) {
    if (lane < 40) tv = *(const u32*)((const char*)out + lane * 65536);
  } else if (w == 3) {
    tv = __builtin_amdgcn_raw_buffer_load_b32(rsW, (lane < 3 ? WS_H + pm * H_PANEL + lane * 65536 : WS_HID + pm * HID_PANEL + (lane & 7) * 65536) , 0, AUX_SC1);
  } else if (lane < 34) tv = __builtin_amdgcn_raw_buffer_load_b32(rs1, lane * 65536, 0, 0);

  auto w1_dma = [&](int s0, int s1) {
#pragma unroll
    for (int i = 0; i < 7; i++) {
      const int n = i * 64 + lane, row = n / 14, ck = n - row * 14;
      const u32 voff = row * (F * 2) + jn * 216 + ck * 16 - (ck == 13 ? 8 : 0);
      for (int s = s0; s < s1; s++) dma16(lds0 + w * QSZ + s * SLOT1 + i * 1024, voff, rs1, (5 * w + s) * 32 * (F * 2));
    }
  };

  if (idx < 20) {
    const int g = xcd * 20 + idx, ch = 4 * g + w;
    const u32 pAre = ((const u32*)Are)[ch * 64 + lane], pAim = ((const u32*)Aim)[ch * 64 + lane];
    const u32 pBre = ((const u32*)Bre)[ch * 64 + lane], pBim = ((const u32*)Bim)[ch * 64 + lane];
    const u32 pCre = ((const u32*)Cre)[ch * 64 + lane], pCim = ((const u32*)Cim)[ch * 64 + lane];
    const float ldt = lo16(logdt[ch]), dsk = lo16(Dskip[ch]);
    __builtin_amdgcn_sched_barrier(0);
    u32x2 xc[4][2];
#pragma unroll
    for (int j = 0; j < 4; j++)
#pragma unroll
      for (int e = 0; e < 2; e++) xc[j][e] = *(const u32x2*)(x + (512 * w + 128 * j + 2 * lane + e) * D + 4 * g);
    __builtin_amdgcn_sched_barrier(0);

    const float dt = rbf(__builtin_amdgcn_exp2f(ldt * 1.44269504088896f));
    f32x2 W[2], l1[2], l8[2], l32[2], l256[2];
    float S = 0.f;
#pragma unroll
    for (int m = 0; m < 2; m++) {
      const float are = dt * (m ? hi16(pAre) : lo16(pAre)), aim = dt * (m ? hi16(pAim) : lo16(pAim));
      const float br = dt * (m ? hi16(pBre) : lo16(pBre)), bi = dt * (m ? hi16(pBim) : lo16(pBim));
      const float cr = m ? hi16(pCre) : lo16(pCre), ci = m ? hi16(pCim) : lo16(pCim);
      W[m] = (f32x2){cr * br - ci * bi, cr * bi + ci * br};
      S += __builtin_fabsf(W[m].x) + __builtin_fabsf(W[m].y);
      const float ar2 = are * 1.44269504088896f;
      const float th = aim * 0.15915494f;
      const float tl = __builtin_fmaf(aim, 6.4206383e-9f, __builtin_fmaf(aim, 0.15915494f, -th));
      auto cexp = [&](float l) {
        const float mag = __builtin_amdgcn_exp2f(ar2 * l);
        const float ph = __builtin_fmaf(tl, l, __builtin_amdgcn_fractf(th * l));
        return (f32x2){mag * __builtin_amdgcn_cosf(ph), mag * __builtin_amdgcn_sinf(ph)};
      };
      l1[m] = cexp(1.f); l8[m] = cexp(8.f); l32[m] = cexp(32.f); l256[m] = cexp(256.f);
    }
    STAMP(1);
    S = sum16(S); S = dpp_add<0x142, 0xa>(S); S = dpp_add<0x143, 0xc>(S);
    S = BC(float, __builtin_amdgcn_readlane(BC(int, S), 63)) + __builtin_fabsf(dsk);
    int ex = (int)(BC(u32, S) >> 23);
    ex = ex < 14 ? 14 : ex;
    const float scale = BC(float, (u32)(267 - ex) << 23), inv_scale = BC(float, (u32)(ex - 13) << 23);

    LAS char* const trow = Q + lane * 8;
    auto put = [&](int off, f32x2 a, f32x2 c) { *(LAS u32x2*)(trow + off) = (u32x2){pk_f16(a.x, a.y), pk_f16(c.x, c.y)}; };
    {
      f32x2 P[2][4];
      Cx st[2];
#pragma unroll
      for (int m = 0; m < 2; m++) {
        const Cx c8 = mk(l8[m]);
        P[m][0] = (f32x2){1.f, 0.f}; P[m][1] = l8[m]; P[m][2] = cmul(l8[m], c8); P[m][3] = cmul(P[m][2], c8);
        st[m] = mk(l32[m]);
      }
#pragma unroll
      for (int a = 0; a < 8; a++)
#pragma unroll
        for (int c = 0; c < 4; c++) {
          if (a == 1 && c == 0) { P[0][0] = l32[0]; P[1][0] = l32[1]; }
          else if (a) { P[0][c] = cmul(P[0][c], st[0]); P[1][c] = cmul(P[1][c], st[1]); }
          put((4 * a + c) * KT_ROW, P[0][c], P[1][c]);
        }
    }
    {
      f32x2 V[2][9];
      Cx mu[2];
#pragma unroll
      for (int m = 0; m < 2; m++) {
        const Cx c1 = mk(conj(l1[m]));
        V[m][0] = conj(W[m]) * scale;
#pragma unroll
        for (int s = 1; s < 9; s++) V[m][s] = cmul(V[m][s - 1], c1);
        mu[m] = mk(conj(l256[m]));
      }
#pragma unroll
      for (int s = 0; s < 8; s++) put(KT_HI + s * KT_ROW, V[0][s], V[1][s]);
      auto adv = [&](int s, int row) { V[0][s] = cmul(V[0][s], mu[0]); V[1][s] = cmul(V[1][s], mu[1]); put(KT_HI + row * KT_ROW, V[0][s], V[1][s]); };
      adv(0, 8); adv(2, 9); adv(4, 10); put(KT_HI + 12 * KT_ROW, V[0][4], V[1][4]); adv(6, 13); adv(8, 14);
      adv(0, 16); adv(4, 17); adv(8, 18);
      adv(0, 20); adv(4, 21); adv(8, 22);
#pragma unroll
      for (int z = 0; z < 4; z++) { adv(0, 24 + 2 * z); adv(8, 25 + 2 * z); }
    }
    STAMP(2);
    u32x4 st[4];
    const u32 st_off = WS_ST + (512 * w + 2 * lane) * 8;
#pragma unroll
    for (int j = 0; j < 4; j++) st[j] = __builtin_amdgcn_raw_buffer_load_b128(rsW, st_off + j * 1024, 0, AUX_SC1);
    __builtin_amdgcn_sched_barrier(0);
    f32x16 kd = {0.f, 0.f, 0.f, 0.f, 0.f, 0.f, 0.f, 0.f, 0.f, 0.f, 0.f, 0.f, 0.f, 0.f, 0.f, 0.f};
    const int r0 = lane & 31, hh = lane >> 5;
    {
      LAS char* const fr = Q + r0 * KT_ROW + hh * 16;
#pragma unroll
      for (int s = 0; s < 16; s++)
        kd = __builtin_amdgcn_mfma_f32_32x32x16_f16(*(LAS f16x8*)(fr + KT_HI + s * 32), *(LAS f16x8*)(fr + s * 32), kd, 0, 0, 0);
    }
    {
      if (lane == 0) kd[0] += dsk * scale;
      auto put2 = [&](int el, u32 d0, u32 d1) {
        LAS char* p = Q + K_OFF + el * 2;
        *(LAS u32x2*)p = (u32x2){d0, d1};
        *(LAS u32*)(p + K_COPY + 4) = d0; *(LAS u32*)(p + K_COPY + 8) = d1;
      };
      put2(2044 - 8 * r0 - 4 * hh, pk_f16(kd[3], kd[2]), pk_f16(kd[1], kd[0]));
      put2(2044 - 256 - 8 * r0 - 4 * hh, pk_f16(kd[6], kd[5]), pk_f16(kd[4], kd[4]));
      const int e2 = 2040 - 512 - 256 * hh - 8 * r0;
      put2(e2, pk_f16(kd[10], kd[10]), pk_f16(kd[9], kd[9]));
      put2(e2 + 4, pk_f16(kd[9], kd[8]), pk_f16(kd[8], kd[8]));
#pragma unroll
      for (int z = 0; z < 2; z++) {
        const float a = kd[12 + 2 * z], c = kd[13 + 2 * z], t = (r0 & 1) ? c : a;
        const int e3 = 2040 - 1024 - 512 * hh - 256 * z - 8 * r0;
        const u32 aa = pk_f16(a, a);
        put2(e3, pk_f16(c, c), pk_f16(c, t));
        put2(e3 + 4, aa, aa);
      }
      if (lane < 4) put2(2048 + 4 * lane, 0u, 0u);
      if (lane < 30) *(LAS u32x4*)(Q + U_OFF + (lane & 1) * U_HALF + (lane >> 1) * 16) = (u32x4){0u, 0u, 0u, 0u};
    }
    STAMP(3);
    tag = __builtin_amdgcn_readfirstlane(Ev) + 1u;
    for (u32 spin = 0;; spin++) {
      bool ok = true;
#pragma unroll
      for (int j = 0; j < 4; j++) ok &= (st[j].y == tag) & (st[j].w == tag);
      if (__all(ok) || spin > SPIN_CAP) break;
      __builtin_amdgcn_s_sleep(1);
      asm volatile("" ::: "memory");
#pragma unroll
      for (int j = 0; j < 4; j++) st[j] = __builtin_amdgcn_raw_buffer_load_b128(rsW, st_off + j * 1024, 0, AUX_SC1);
    }
    {
      LAS char* const up = lds + U_OFF + ((lane >> 2) & 1) * U_HALF + (32 * w + 15 + (lane >> 3)) * 16 + (lane & 3) * 4;
#pragma unroll
      for (int j = 0; j < 4; j++) {
        const float m0 = lo16(st[j].x), i0 = __builtin_amdgcn_rcpf(hi16(st[j].x));
        const float m1 = lo16(st[j].z), i1 = __builtin_amdgcn_rcpf(hi16(st[j].z));
        *(LAS u32*)(up + j * 128) = pk_f16((lo16(xc[j][0].x) - m0) * i0, (lo16(xc[j][1].x) - m1) * i1);
        *(LAS u32*)(up + j * 128 + QSZ) = pk_f16((hi16(xc[j][0].x) - m0) * i0, (hi16(xc[j][1].x) - m1) * i1);
        *(LAS u32*)(up + j * 128 + 2 * QSZ) = pk_f16((lo16(xc[j][0].y) - m0) * i0, (lo16(xc[j][1].y) - m1) * i1);
        *(LAS u32*)(up + j * 128 + 3 * QSZ) = pk_f16((hi16(xc[j][0].y) - m0) * i0, (hi16(xc[j][1].y) - m1) * i1);
      }
    }
    w1_dma(0, 3);
    __syncthreads();
    STAMP(4);
    f32x4 acc[8];
#pragma unroll
    for (int m = 0; m < 8; m++) acc[m] = (f32x4){0.f, 0.f, 0.f, 0.f};
    {
      const int e0 = 15 - r16 + 64 * q;
      const int d0 = (e0 >> 1) + 1;
      const u32 sh = (e0 & 1) * 16;
      LAS char* kp = Q + K_OFF + (d0 & 1) * (K_COPY + 4) + d0 * 4;
      LAS char* const up = Q + U_OFF + (r16 + 4 * q) * 16;
      u32 carry[8];
#pragma unroll
      for (int s1 = 0; s1 < 8; s1++) carry[s1] = *(LAS u32*)(kp - 4 + (7 - s1) * 512);
#pragma unroll 2
      for (int s0 = 0; s0 < 8; s0++) {
        u32x4 U[8], Kf[8];
#pragma unroll
        for (int s1 = 0; s1 < 8; s1++) {
          const u32x2 a = *(LAS u32x2*)(kp + s0 * 16 + (7 - s1) * 512), c = *(LAS u32x2*)(kp + s0 * 16 + (7 - s1) * 512 + 8);
          U[s1] = *(LAS u32x4*)(up + (s0 & 1) * U_HALF + (s0 >> 1) * 16 + s1 * 256);
          Kf[s1] = (u32x4){__builtin_amdgcn_alignbit(a.x, carry[s1], sh), __builtin_amdgcn_alignbit(a.y, a.x, sh),
                           __builtin_amdgcn_alignbit(c.x, a.y, sh), __builtin_amdgcn_alignbit(c.y, c.x, sh)};
          carry[s1] = c.y;
        }
#pragma unroll
        for (int s1 = 0; s1 < 8; s1++)
#pragma unroll
          for (int m = s1; m < 8; m++) acc[m] = mfma_h(Kf[s1], U[m - s1], acc[m]);
      }
    }
    STAMP(5);
#pragma unroll
    for (int m = 0; m < 8; m++) *(LAS f32x4*)(Q + K_OFF + m * 1024 + q * 256 + ((r16 ^ (4 * q)) * 16)) = acc[m] * inv_scale;
    __syncthreads();
    {
      const int yq = (lane >> 1) & 3;
#pragma unroll
      for (int j = 0; j < 4; j++) {
        LAS char* const yp = lds + K_OFF + (2 * w + (j >> 1)) * 1024 + yq * 256 + ((((lane >> 3) + 8 * (j & 1)) ^ (4 * yq)) * 16) + (lane & 1) * 8;
        const f32x2 y0 = *(LAS f32x2*)yp, y1 = *(LAS f32x2*)(yp + QSZ), y2 = *(LAS f32x2*)(yp + 2 * QSZ), y3 = *(LAS f32x2*)(yp + 3 * QSZ);
        const u32x4 hv = {pk_bf16(lo16(xc[j][0].x) + y0.x, hi16(xc[j][0].x) + y1.x), pk_bf16(lo16(xc[j][0].y) + y2.x, hi16(xc[j][0].y) + y3.x),
                          pk_bf16(lo16(xc[j][1].x) + y0.y, hi16(xc[j][1].x) + y1.y), pk_bf16(lo16(xc[j][1].y) + y2.y, hi16(xc[j][1].y) + y3.y)};
        __builtin_amdgcn_raw_buffer_store_b128(hv, rsW, WS_H + (4 * w + j) * H_PANEL + g * 1024 + lane * 16, 0, AUX_SC1);
      }
    }
    STAMP(6);
    WAIT_VM0();
    __syncthreads();
    STAMP(7);
    if (tid < 8) __builtin_amdgcn_raw_buffer_store_b32(tag, rsW, WS_F2 + tid * 4096 + b * 16, 0, AUX_SC1);
    w1_dma(3, 5);
  } else {
    const int hid = xcd * 12 + idx - 20;
    if (hid < 64) {
      u32x4 v[2][5];
#pragma unroll
      for (int p = 0; p < 2; p++)
#pragma unroll
        for (int c = 0; c < 5; c++) v[p][c] = *(const u32x4*)(x + (32 * hid + 8 * w + 4 * p + q) * D + (r16 + 16 * c) * 8);
      __builtin_amdgcn_sched_barrier(0);
      tag = __builtin_amdgcn_readfirstlane(Ev) + 1u;
#pragma unroll
      for (int p = 0; p < 2; p++) {
        float s = 0.f;
#pragma unroll
        for (int c = 0; c < 5; c++)
#pragma unroll
          for (int k = 0; k < 4; k++) s += lo16(v[p][c][k]) + hi16(v[p][c][k]);
        const float mean = sum16(s) * (1.f / D);
        float ss = 0.f;
#pragma unroll
        for (int c = 0; c < 5; c++)
#pragma unroll
          for (int k = 0; k < 4; k++) {
            const float a = lo16(v[p][c][k]) - mean, d = hi16(v[p][c][k]) - mean;
            ss = __builtin_fmaf(a, a, __builtin_fmaf(d, d, ss));
          }
        const float var = sum16(ss) * (1.f / D);
        const float den = __builtin_amdgcn_sqrtf(rbf(rbf(var) + 1e-5f));
        if (r16 == 0) __builtin_amdgcn_raw_buffer_store_b64((u32x2){pk_bf16(mean, den), tag}, rsW, WS_ST + (32 * hid + 8 * w + 4 * p + q) * 8, 0, AUX_SC1);
      }
    } else {
      tag = __builtin_amdgcn_readfirstlane(Ev) + 1u;
    }
    STAMP(1);
    if (tid < 8) __builtin_amdgcn_raw_buffer_store_b32(tag, rsW, WS_F2 + tid * 4096 + b * 16, 0, AUX_SC1);
    w1_dma(0, 5);
  }

  WAIT_VM0();
#pragma unroll
  for (int i = 0; i < 3; i++)
    if (i * 64 + lane < 160) *(LAS u32x2*)(Q + (i * 64 + lane) * 224 + 208) = (u32x2){0x3f803f80u, 0x3f803f80u};
  __syncthreads();
  LAS char* const w1p = lds + (lane >> 2) * 224 + (lane & 3) * 8;
  f32x4 cs[7];
#pragma unroll
  for (int nt = 0; nt < 7; nt++) cs[nt] = (f32x4){0.f, 0.f, 0.f, 0.f};
  for (int qq = 0; qq < 4; qq++)
#pragma unroll
    for (int s = 0; s < 5; s++)
#pragma unroll
      for (int nt = 0; nt < 7; nt++)
        cs[nt] = mfma_bf(tr_frag(w1p + qq * QSZ + s * SLOT1 + nt * 32, 16 * 224), (u32x4){0x3f803f80u, 0x3f803f80u, 0x3f803f80u, 0x3f803f80u}, cs[nt]);
  STAMP(8);
  if (w == 0) {
    for (u32 spin = 0;; spin++) {
      bool ok = true;
#pragma unroll
      for (int j = 0; j < 4; j++) ok &= __builtin_amdgcn_raw_buffer_load_b32(rsW, WS_F2 + xcd * 4096 + (lane + 64 * j) * 16, 0, AUX_SC1) == tag;
      if (__all(ok) || spin > SPIN_CAP) break;
      __builtin_amdgcn_s_sleep(1);
      asm volatile("" ::: "memory");
    }
    if (b == 0 && lane == 0) __builtin_amdgcn_raw_buffer_store_b32(tag, rsW, WS_E, 0, AUX_SC1);
  }
  __syncthreads();
  STAMP(9);
  {
    u32x4 X[20][2];
    const u32 hoff = WS_H + pm * H_PANEL + q * 1024 + w * 256 + r16 * 16;
#pragma unroll
    for (int s = 0; s < 20; s++) {
      X[s][0] = __builtin_amdgcn_raw_buffer_load_b128(rsW, hoff, s * 8192, AUX_SC1);
      X[s][1] = __builtin_amdgcn_raw_buffer_load_b128(rsW, hoff, s * 8192 + 4096, AUX_SC1);
      __builtin_amdgcn_sched_barrier(0);
    }
    f32x4 a1[7][2];
#pragma unroll
    for (int nt = 0; nt < 7; nt++) a1[nt][0] = a1[nt][1] = (f32x4){0.f, 0.f, 0.f, 0.f};
    float ss0 = 0.f, ss1 = 0.f;
#pragma unroll
    for (int s = 0; s < 20; s++) {
      const u32x4 f0 = {X[s][0].x, X[s][0].y, X[s][1].x, X[s][1].y}, f1 = {X[s][0].z, X[s][0].w, X[s][1].z, X[s][1].w};
#pragma unroll
      for (int k = 0; k < 4; k++) {
        ss0 = dot2(f0[k], f0[k], ss0);
        ss1 = dot2(f1[k], f1[k], ss1);
      }
#pragma unroll
      for (int nt = 0; nt < 7; nt++) {
        const u32x4 wf = tr_frag(w1p + (s / 5) * QSZ + (s % 5) * SLOT1 + nt * 32, 16 * 224);
        a1[nt][0] = mfma_bf(wf, f0, a1[nt][0]);
        a1[nt][1] = mfma_bf(wf, f1, a1[nt][1]);
      }
    }
    STAMP(10);
    ss0 += __shfl_xor(ss0, 16); ss1 += __shfl_xor(ss1, 16);
    ss0 += __shfl_xor(ss0, 32); ss1 += __shfl_xor(ss1, 32);
    const float mean0 = __shfl(a1[6][0].x, 32 + r16) * (1.f / D), mean1 = __shfl(a1[6][1].x, 32 + r16) * (1.f / D);
    const float rs0 = __builtin_amdgcn_rsqf(__builtin_fmaxf(ss0 * (1.f / D) - mean0 * mean0, 0.f) + 1e-5f);
    const float rs1v = __builtin_amdgcn_rsqf(__builtin_fmaxf(ss1 * (1.f / D) - mean1 * mean1, 0.f) + 1e-5f);
    const u32 ooff = WS_HID + pm * HID_PANEL + (27 * jn + q) * 1024 + w * 256 + r16 * 16;
#pragma unroll
    for (int nt = 0; nt < 7; nt++) {
      f32x4 v0 = (a1[nt][0] - mean0 * cs[nt]) * rs0, v1 = (a1[nt][1] - mean1 * cs[nt]) * rs1v;
#pragma unroll
      for (int k = 0; k < 4; k++) { v0[k] = __builtin_fmaxf(v0[k], 0.f); v1[k] = __builtin_fmaxf(v1[k], 0.f); }
      const u32x4 o = {pk_bf16(v0.x, v0.y), pk_bf16(v0.z, v0.w), pk_bf16(v1.x, v1.y), pk_bf16(v1.z, v1.w)};
      if (nt < 6) __builtin_amdgcn_raw_buffer_store_b128(o, rsW, ooff, nt * 4096, AUX_SC1);
      else if (q != 2) __builtin_amdgcn_raw_buffer_store_b128(o, rsW, ooff - (q == 3 ? 1024 : 0), 6 * 4096, AUX_SC1);
    }
  }
  WAIT_VM0();
  __syncthreads();
  STAMP(11);
  if (tid == 0) __builtin_amdgcn_raw_buffer_store_b32(tag, rsW, WS_F3 + pm * 256 + jn * 4, 0, AUX_SC1);

  const int half = (idx >> 3) & 1, ct = idx & 7;
  const int ks0 = 14 * w - (w == 3 ? 1 : 0);
  auto w2_dma = [&](int s0, int s1, int slot0) {
#pragma unroll
    for (int i = 0; i < 5; i++) {
      const int n = i * 64 + lane, row = n / 10, ck = n - row * 10;
      const u32 voff = row * (D * 2) + ct * 160 + ck * 16;
      for (int s = s0; s < s1; s++) dma16(lds0 + w * QSZ + (slot0 + s - s0) * SLOT2 + i * 1024, voff, rs2, (ks0 + s) * 32 * (D * 2));
    }
  };
  w2_dma(0, 8, 0);
  WAIT_VM0();
  LAS char* const w2p = Q + (lane >> 2) * 160 + (lane & 3) * 8;
  u32x4 wr[6][5];
#pragma unroll
  for (int s = 0; s < 6; s++)
#pragma unroll
    for (int nt = 0; nt < 5; nt++) wr[s][nt] = tr_frag(w2p + s * SLOT2 + nt * 32, 16 * 160);
#pragma unroll
  for (int s = 0; s < 6; s++)
#pragma unroll
    for (int nt = 0; nt < 5; nt++) asm volatile("" : "+v"(wr[s][nt]));
  w2_dma(8, 14, 0);
  STAMP(12);
  if (w == 0) {
    for (u32 spin = 0;; spin++) {
      const bool ok = __builtin_amdgcn_raw_buffer_load_b32(rsW, WS_F3 + pm * 256 + r16 * 4, 0, AUX_SC1) == tag;
      if (__all(ok) || spin > SPIN_CAP) break;
      __builtin_amdgcn_s_sleep(1);
      asm volatile("" ::: "memory");
    }
  }
  __syncthreads();
  STAMP(13);
  {
    u32x4 X[14][4];
    const u32 aoff = WS_HID + pm * HID_PANEL + (8 * ks0 + q) * 1024 + half * 512 + r16 * 16;
#pragma unroll
    for (int s = 0; s < 14; s++) {
      if (s < 13 || w < 2) {
        X[s][0] = __builtin_amdgcn_raw_buffer_load_b128(rsW, aoff, s * 8192, AUX_SC1);
        X[s][1] = __builtin_amdgcn_raw_buffer_load_b128(rsW, aoff, s * 8192 + 4096, AUX_SC1);
        X[s][2] = __builtin_amdgcn_raw_buffer_load_b128(rsW, aoff + 256, s * 8192, AUX_SC1);
        X[s][3] = __builtin_amdgcn_raw_buffer_load_b128(rsW, aoff + 256, s * 8192 + 4096, AUX_SC1);
      } else {
        X[s][0] = X[s][1] = X[s][2] = X[s][3] = (u32x4){0u, 0u, 0u, 0u};
      }
      __builtin_amdgcn_sched_barrier(0);
    }
    const int tok = half * 64 + 32 * (w >> 1) + 2 * r16 + (w & 1);
    u32x2 res[5];
#pragma unroll
    for (int nt = 0; nt < 5; nt++) res[nt] = __builtin_amdgcn_raw_buffer_load_b64(rsW, WS_H + pm * H_PANEL + (ct * 20 + q) * 1024 + tok * 8, nt * 4096, AUX_SC1);
    __builtin_amdgcn_sched_barrier(0);
    f32x4 a2[4][5];
#pragma unroll
    for (int mt = 0; mt < 4; mt++)
#pragma unroll
      for (int nt = 0; nt < 5; nt++) a2[mt][nt] = (f32x4){0.f, 0.f, 0.f, 0.f};
#pragma unroll
    for (int s = 0; s < 14; s++) {
      u32x4 f[4];
#pragma unroll
      for (int hh = 0; hh < 2; hh++) {
        f[2 * hh] = (u32x4){X[s][2 * hh].x, X[s][2 * hh].y, X[s][2 * hh + 1].x, X[s][2 * hh + 1].y};
        f[2 * hh + 1] = (u32x4){X[s][2 * hh].z, X[s][2 * hh].w, X[s][2 * hh + 1].z, X[s][2 * hh + 1].w};
      }
      if (s == 6) {
        asm volatile("" : "+v"(a2[0][0]) :: "memory");
      }
#pragma unroll
      for (int nt = 0; nt < 5; nt++) {
        const u32x4 wf = s < 6 ? wr[s][nt] : tr_frag(w2p + (s < 8 ? s : s - 8) * SLOT2 + nt * 32, 16 * 160);
#pragma unroll
        for (int mt = 0; mt < 4; mt++) a2[mt][nt] = mfma_bf(wf, f[mt], a2[mt][nt]);
      }
    }
    STAMP(14);
#pragma unroll
    for (int mt = 0; mt < 4; mt++)
#pragma unroll
      for (int nt = 0; nt < 5; nt++) *(LAS f32x4*)(Q + (mt * 5 + nt) * 1024 + lane * 16) = a2[mt][nt];
    __syncthreads();
#pragma unroll
    for (int nt = 0; nt < 5; nt++) {
      LAS char* const p = lds + (w * 5 + nt) * 1024 + lane * 16;
      const f32x4 v = (*(LAS f32x4*)p + *(LAS f32x4*)(p + QSZ)) + (*(LAS f32x4*)(p + 2 * QSZ) + *(LAS f32x4*)(p + 3 * QSZ));
      const u32x2 o = {pk_bf16(lo16(res[nt].x) + v.x, hi16(res[nt].x) + v.y), pk_bf16(lo16(res[nt].y) + v.z, hi16(res[nt].y) + v.w)};
      *(u32x2*)(out + (pm * 128 + tok) * D + ct * 80 + nt * 16 + q * 4) = o;
    }
  }
  asm volatile(".Lmega_end:" :: "v"(pfv), "v"(tv));
  STAMP(15);
}

extern "C" void kernel_launch(void* const* d_in, const int* in_sizes, int n_in, void* d_out, int out_size, void* d_ws, size_t ws_size,
                              hipStream_t stream) {
  mega<<<256, 256, 0, stream>>>((const u16*)d_in[0], (const u16*)d_in[1], (const u16*)d_in[2], (const u16*)d_in[3], (const u16*)d_in[4],
                                (const u16*)d_in[5], (const u16*)d_in[6], (const u16*)d_in[7], (const u16*)d_in[8], (const u16*)d_in[10],
                                (const u16*)d_in[11], (u16*)d_out, (char*)d_ws);
}
```

```cpp
#define PROBE 1
#include <hip/hip_runtime.h>

typedef unsigned short u16;
typedef unsigned u32;
typedef unsigned long long u64;
typedef u32 u32x2 __attribute__((ext_vector_type(2)));
typedef u32 u32x4 __attribute__((ext_vector_type(4)));
typedef float f32x2 __attribute__((ext_vector_type(2)));
typedef float f32x4 __attribute__((ext_vector_type(4)));
typedef float f32x16 __attribute__((ext_vector_type(16)));
typedef _Float16 f16x2 __attribute__((ext_vector_type(2)));
typedef _Float16 f16x8 __attribute__((ext_vector_type(8)));
typedef __bf16 bf16x2 __attribute__((ext_vector_type(2)));
typedef __bf16 bf16x8 __attribute__((ext_vector_type(8)));
typedef short s16x4 __attribute__((ext_vector_type(4)));
#define LAS __attribute__((address_space(3)))
#define DI __device__ __forceinline__
#define BC(T, v) __builtin_bit_cast(T, v)

constexpr int L = 2048, D = 640, F = 1728;
constexpr u32 WS_E = 0;
constexpr u32 WS_F2 = 4096;
constexpr u32 WS_F3 = WS_F2 + 8 * 4096;
constexpr u32 WS_ST = 65536;
constexpr u32 WS_H = 1u << 20;
constexpr u32 WS_HID = 4u << 20;
constexpr u32 WS_END = 12u << 20;
constexpr u32 H_PANEL = 160 * 1024, HID_PANEL = 432 * 1024;
constexpr int QSZ = 40960, SLOT1 = 7168, SLOT2 = 5120;
constexpr int KT_ROW = 528, KT_HI = 32 * KT_ROW;
constexpr int K_OFF = 27520, K_COPY = 4160;
constexpr int U_OFF = 35840, U_HALF = 2368;
constexpr int AUX_SC1 = 16;
constexpr u32 SPIN_CAP = 1u << 18;

#define STAMP(k) do { if ((k) == 0 || (k) == PROBE || (k) == 15) { u64 t_; __builtin_amdgcn_sched_barrier(0); asm volatile("s_memrealtime %0\n\ts_waitcnt lgkmcnt(0)" : "=s"(t_) :: "memory"); \
    if ((k) == 0) pr0 = pr1 = t_; else if ((k) == PROBE) pr1 = t_; else { u64 e_ = t_ + (pr1 - pr0); while (t_ < e_) { asm volatile("s_memrealtime %0\n\ts_waitcnt lgkmcnt(0)" : "=s"(t_) :: "memory"); } } \
    __builtin_amdgcn_sched_barrier(0); } } while (0)

DI float lo16(u32 v) { return BC(float, v << 16); }
DI float hi16(u32 v) { return BC(float, v & 0xffff0000u); }
DI u32 pk_bf16(float a, float b) { return BC(u32, __builtin_convertvector((f32x2){a, b}, bf16x2)); }
DI u32 pk_f16(float a, float b) { return BC(u32, __builtin_convertvector((f32x2){a, b}, f16x2)); }
DI float dot2(u32 a, u32 b, float c) { return __builtin_amdgcn_fdot2_f32_bf16(BC(bf16x2, a), BC(bf16x2, b), c, false); }
DI float rbf(float a) { return lo16(pk_bf16(a, 0.f)); }
template <int CTRL, int RM> DI float dpp_add(float v) {
  return v + BC(float, __builtin_amdgcn_update_dpp(0, BC(int, v), CTRL, RM, 0xf, true));
}
DI float sum16(float v) {
  v = dpp_add<0x121, 0xf>(v); v = dpp_add<0x122, 0xf>(v); v = dpp_add<0x124, 0xf>(v); return dpp_add<0x128, 0xf>(v);
}
struct Cx { f32x2 c, r; };
DI Cx mk(f32x2 c) { Cx o; o.c = c; o.r = (f32x2){-c.y, c.x}; return o; }
DI f32x2 cmul(f32x2 v, const Cx& m) { return __builtin_elementwise_fma(v.yy, m.r, v.xx * m.c); }
DI f32x2 conj(f32x2 v) { return (f32x2){v.x, -v.y}; }
DI void dma16(u32 lds_addr, u32 voff, __amdgpu_buffer_rsrc_t rs, u32 soff) {
  u32 keep;
  asm volatile("s_mov_b32 %0, m0\n\ts_mov_b32 m0, %1\n\ts_nop 3\n\tbuffer_load_dwordx4 %2, %3, %4 offen lds\n\ts_mov_b32 m0, %0"
               : "=&s"(keep) : "s"(lds_addr), "v"(voff), "s"(rs), "s"(soff) : "memory");
}
#define WAIT_VM0() asm volatile("s_waitcnt vmcnt(0)" ::: "memory")
DI f32x4 mfma_bf(u32x4 a, u32x4 b, f32x4 c) { return __builtin_amdgcn_mfma_f32_16x16x32_bf16(BC(bf16x8, a), BC(bf16x8, b), c, 0, 0, 0); }
DI f32x4 mfma_h(u32x4 a, u32x4 b, f32x4 c) { return __builtin_amdgcn_mfma_f32_16x16x32_f16(BC(f16x8, a), BC(f16x8, b), c, 0, 0, 0); }
DI u32x4 tr_frag(LAS char* p, int t_stride) {
  u32x2 a = BC(u32x2, __builtin_amdgcn_ds_read_tr16_b64_v4i16((LAS s16x4*)p));
  u32x2 b = BC(u32x2, __builtin_amdgcn_ds_read_tr16_b64_v4i16((LAS s16x4*)(p + t_stride)));
  return (u32x4){a.x, a.y, b.x, b.y};
}

__global__ __launch_bounds__(256, 1) void mega(const u16* __restrict__ x, const u16* __restrict__ Are, const u16* __restrict__ Aim,
                                               const u16* __restrict__ Bre, const u16* __restrict__ Bim, const u16* __restrict__ Cre,
                                               const u16* __restrict__ Cim, const u16* __restrict__ logdt, const u16* __restrict__ Dskip,
                                               const u16* __restrict__ w1, const u16* __restrict__ w2, u16* __restrict__ out, char* ws) {
  __shared__ __attribute__((aligned(16))) char smem[4 * QSZ];
  LAS char* const lds = (LAS char*)smem;
  const u32 lds0 = (u32)(size_t)lds;
  const int b = blockIdx.x, tid = threadIdx.x, lane = tid & 63;
  const int w = __builtin_amdgcn_readfirstlane(tid >> 6);
  const int xcd = b & 7, idx = b >> 3;
  const int r16 = lane & 15, q = lane >> 4;
  u64 pr0, pr1;
  STAMP(0);
  u32 pfv = 0;
  {
    u64 pc; u32 len;
    asm volatile("s_getpc_b64 %0\n.Lmega_pf:\n\ts_mov_b32 %1, .Lmega_end-.Lmega_pf" : "=s"(pc), "=s"(len));
    const __amdgpu_buffer_rsrc_t rsC = __builtin_amdgcn_make_buffer_rsrc((void*)pc, 0, len, 0x00020000);
    if (tid < 16) pfv = __builtin_amdgcn_raw_buffer_load_b32(rsC, (tid * 32 + idx) * 128, 0, 0);
  }
  const __amdgpu_buffer_rsrc_t rsW = __builtin_amdgcn_make_buffer_rsrc(ws, 0, WS_END, 0x00020000);
  const __amdgpu_buffer_rsrc_t rs1 = __builtin_amdgcn_make_buffer_rsrc((void*)w1, 0, D * F * 2, 0x00020000);
  const __amdgpu_buffer_rsrc_t rs2 = __builtin_amdgcn_make_buffer_rsrc((void*)w2, 0, D * F * 2, 0x00020000);
  const u32 Ev = __builtin_amdgcn_raw_buffer_load_b32(rsW, WS_E, 0, AUX_SC1);
  LAS char* const Q = lds + w * QSZ;
  const int pm = 2 * xcd + (idx >> 4), jn = idx & 15;
  u32 tag;

  auto w1_dma = [&](int s0, int s1) {
#pragma unroll
    for (int i = 0; i < 7; i++) {
      const int n = i * 64 + lane, row = n / 14, ck = n - row * 14;
      const u32 voff = row * (F * 2) + jn * 216 + ck * 16 - (ck == 13 ? 8 : 0);
      for (int s = s0; s < s1; s++) dma16(lds0 + w * QSZ + s * SLOT1 + i * 1024, voff, rs1, (5 * w + s) * 32 * (F * 2));
    }
  };

  if (idx < 20) {
    const int g = xcd * 20 + idx, ch = 4 * g + w;
    const u32 pAre = ((const u32*)Are)[ch * 64 + lane], pAim = ((const u32*)Aim)[ch * 64 + lane];
    const u32 pBre = ((const u32*)Bre)[ch * 64 + lane], pBim = ((const u32*)Bim)[ch * 64 + lane];
    const u32 pCre = ((const u32*)Cre)[ch * 64 + lane], pCim = ((const u32*)Cim)[ch * 64 + lane];
    const float ldt = lo16(logdt[ch]), dsk = lo16(Dskip[ch]);
    __builtin_amdgcn_sched_barrier(0);
    u32x2 xc[4][2];
#pragma unroll
    for (int j = 0; j < 4; j++)
#pragma unroll
      for (int e = 0; e < 2; e++) xc[j][e] = *(const u32x2*)(x + (512 * w + 128 * j + 2 * lane + e) * D + 4 * g);
    __builtin_amdgcn_sched_barrier(0);

    const float dt = rbf(__builtin_amdgcn_exp2f(ldt * 1.44269504088896f));
    f32x2 W[2], l1[2], l8[2], l32[2], l256[2];
    float S = 0.f;
#pragma unroll
    for (int m = 0; m < 2; m++) {
      const float are = dt * (m ? hi16(pAre) : lo16(pAre)), aim = dt * (m ? hi16(pAim) : lo16(pAim));
      const float br = dt * (m ? hi16(pBre) : lo16(pBre)), bi = dt * (m ? hi16(pBim) : lo16(pBim));
      const float cr = m ? hi16(pCre) : lo16(pCre), ci = m ? hi16(pCim) : lo16(pCim);
      W[m] = (f32x2){cr * br - ci * bi, cr * bi + ci * br};
      S += __builtin_fabsf(W[m].x) + __builtin_fabsf(W[m].y);
      const float ar2 = are * 1.44269504088896f;
      const float th = aim * 0.15915494f;
      const float tl = __builtin_fmaf(aim, 6.4206383e-9f, __builtin_fmaf(aim, 0.15915494f, -th));
      auto cexp = [&](float l) {
        const float mag = __builtin_amdgcn_exp2f(ar2 * l);
        const float ph = __builtin_fmaf(tl, l, __builtin_amdgcn_fractf(th * l));
        return (f32x2){mag * __builtin_amdgcn_cosf(ph), mag * __builtin_amdgcn_sinf(ph)};
      };
      l1[m] = cexp(1.f); l8[m] = cexp(8.f); l32[m] = cexp(32.f); l256[m] = cexp(256.f);
    }
    STAMP(1);
    S = sum16(S); S = dpp_add<0x142, 0xa>(S); S = dpp_add<0x143, 0xc>(S);
    S = BC(float, __builtin_amdgcn_readlane(BC(int, S), 63)) + __builtin_fabsf(dsk);
    int ex = (int)(BC(u32, S) >> 23);
    ex = ex < 14 ? 14 : ex;
    const float scale = BC(float, (u32)(267 - ex) << 23), inv_scale = BC(float, (u32)(ex - 13) << 23);

    LAS char* const trow = Q + lane * 8;
    auto put = [&](int off, f32x2 a, f32x2 c) { *(LAS u32x2*)(trow + off) = (u32x2){pk_f16(a.x, a.y), pk_f16(c.x, c.y)}; };
    {
      f32x2 P[2][4];
      Cx st[2];
#pragma unroll
      for (int m = 0; m < 2; m++) {
        const Cx c8 = mk(l8[m]);
        P[m][0] = (f32x2){1.f, 0.f}; P[m][1] = l8[m]; P[m][2] = cmul(l8[m], c8); P[m][3] = cmul(P[m][2], c8);
        st[m] = mk(l32[m]);
      }
#pragma unroll
      for (int a = 0; a < 8; a++)
#pragma unroll
        for (int c = 0; c < 4; c++) {
          if (a == 1 && c == 0) { P[0][0] = l32[0]; P[1][0] = l32[1]; }
          else if (a) { P[0][c] = cmul(P[0][c], st[0]); P[1][c] = cmul(P[1][c], st[1]); }
          put((4 * a + c) * KT_ROW, P[0][c], P[1][c]);
        }
    }
    {
      f32x2 V[2][9];
      Cx mu[2];
#pragma unroll
      for (int m = 0; m < 2; m++) {
        const Cx c1 = mk(conj(l1[m]));
        V[m][0] = conj(W[m]) * scale;
#pragma unroll
        for (int s = 1; s < 9; s++) V[m][s] = cmul(V[m][s - 1], c1);
        mu[m] = mk(conj(l256[m]));
      }
#pragma unroll
      for (int s = 0; s < 8; s++) put(KT_HI + s * KT_ROW, V[0][s], V[1][s]);
      auto adv = [&](int s, int row) { V[0][s] = cmul(V[0][s], mu[0]); V[1][s] = cmul(V[1][s], mu[1]); put(KT_HI + row * KT_ROW, V[0][s], V[1][s]); };
      adv(0, 8); adv(2, 9); adv(4, 10); put(KT_HI + 12 * KT_ROW, V[0][4], V[1][4]); adv(6, 13); adv(8, 14);
      adv(0, 16); adv(4, 17); adv(8, 18);
      adv(0, 20); adv(4, 21); adv(8, 22);
#pragma unroll
      for (int z = 0; z < 4; z++) { adv(0, 24 + 2 * z); adv(8, 25 + 2 * z); }
    }
    STAMP(2);
    u32x4 st[4];
    const u32 st_off = WS_ST + (512 * w + 2 * lane) * 8;
#pragma unroll
    for (int j = 0; j < 4; j++) st[j] = __builtin_amdgcn_raw_buffer_load_b128(rsW, st_off + j * 1024, 0, AUX_SC1);
    __builtin_amdgcn_sched_barrier(0);
    f32x16 kd = {0.f, 0.f, 0.f, 0.f, 0.f, 0.f, 0.f, 0.f, 0.f, 0.f, 0.f, 0.f, 0.f, 0.f, 0.f, 0.f};
    const int r0 = lane & 31, hh = lane >> 5;
    {
      LAS char* const fr = Q + r0 * KT_ROW + hh * 16;
#pragma unroll
      for (int s = 0; s < 16; s++)
        kd = __builtin_amdgcn_mfma_f32_32x32x16_f16(*(LAS f16x8*)(fr + KT_HI + s * 32), *(LAS f16x8*)(fr + s * 32), kd, 0, 0, 0);
    }
    {
      if (lane == 0) kd[0] += dsk * scale;
      auto put2 = [&](int el, u32 d0, u32 d1) {
        LAS char* p = Q + K_OFF + el * 2;
        *(LAS u32x2*)p = (u32x2){d0, d1};
        *(LAS u32*)(p + K_COPY + 4) = d0; *(LAS u32*)(p + K_COPY + 8) = d1;
      };
      put2(2044 - 8 * r0 - 4 * hh, pk_f16(kd[3], kd[2]), pk_f16(kd[1], kd[0]));
      put2(2044 - 256 - 8 * r0 - 4 * hh, pk_f16(kd[6], kd[5]), pk_f16(kd[4], kd[4]));
      const int e2 = 2040 - 512 - 256 * hh - 8 * r0;
      put2(e2, pk_f16(kd[10], kd[10]), pk_f16(kd[9], kd[9]));
      put2(e2 + 4, pk_f16(kd[9], kd[8]), pk_f16(kd[8], kd[8]));
#pragma unroll
      for (int z = 0; z < 2; z++) {
        const float a = kd[12 + 2 * z], c = kd[13 + 2 * z], t = (r0 & 1) ? c : a;
        const int e3 = 2040 - 1024 - 512 * hh - 256 * z - 8 * r0;
        const u32 aa = pk_f16(a, a);
        put2(e3, pk_f16(c, c), pk_f16(c, t));
        put2(e3 + 4, aa, aa);
      }
      if (lane < 4) put2(2048 + 4 * lane, 0u, 0u);
      if (lane < 30) *(LAS u32x4*)(Q + U_OFF + (lane & 1) * U_HALF + (lane >> 1) * 16) = (u32x4){0u, 0u, 0u, 0u};
    }
    STAMP(3);
    tag = __builtin_amdgcn_readfirstlane(Ev) + 1u;
    for (u32 spin = 0;; spin++) {
      bool ok = true;
#pragma unroll
      for (int j = 0; j < 4; j++) ok &= (st[j].y == tag) & (st[j].w == tag);
      if (__all(ok) || spin > SPIN_CAP) break;
      __builtin_amdgcn_s_sleep(1);
      asm volatile("" ::: "memory");
#pragma unroll
      for (int j = 0; j < 4; j++) st[j] = __builtin_amdgcn_raw_buffer_load_b128(rsW, st_off + j * 1024, 0, AUX_SC1);
    }
    {
      LAS char* const up = lds + U_OFF + ((lane >> 2) & 1) * U_HALF + (32 * w + 15 + (lane >> 3)) * 16 + (lane & 3) * 4;
#pragma unroll
      for (int j = 0; j < 4; j++) {
        const float m0 = lo16(st[j].x), i0 = __builtin_amdgcn_rcpf(hi16(st[j].x));
        const float m1 = lo16(st[j].z), i1 = __builtin_amdgcn_rcpf(hi16(st[j].z));
        *(LAS u32*)(up + j * 128) = pk_f16((lo16(xc[j][0].x) - m0) * i0, (lo16(xc[j][1].x) - m1) * i1);
        *(LAS u32*)(up + j * 128 + QSZ) = pk_f16((hi16(xc[j][0].x) - m0) * i0, (hi16(xc[j][1].x) - m1) * i1);
        *(LAS u32*)(up + j * 128 + 2 * QSZ) = pk_f16((lo16(xc[j][0].y) - m0) * i0, (lo16(xc[j][1].y) - m1) * i1);
        *(LAS u32*)(up + j * 128 + 3 * QSZ) = pk_f16((hi16(xc[j][0].y) - m0) * i0, (hi16(xc[j][1].y) - m1) * i1);
      }
    }
    w1_dma(0, 3);
    __syncthreads();
    STAMP(4);
    f32x4 acc[8];
#pragma unroll
    for (int m = 0; m < 8; m++) acc[m] = (f32x4){0.f, 0.f, 0.f, 0.f};
    {
      const int e0 = 15 - r16 + 64 * q;
      const int d0 = (e0 >> 1) + 1;
      const u32 sh = (e0 & 1) * 16;
      LAS char* kp = Q + K_OFF + (d0 & 1) * (K_COPY + 4) + d0 * 4;
      LAS char* const up = Q + U_OFF + (r16 + 4 * q) * 16;
      u32 carry[8];
#pragma unroll
      for (int s1 = 0; s1 < 8; s1++) carry[s1] = *(LAS u32*)(kp - 4 + (7 - s1) * 512);
#pragma unroll 2
      for (int s0 = 0; s0 < 8; s0++) {
        u32x4 U[8], Kf[8];
#pragma unroll
        for (int s1 = 0; s1 < 8; s1++) {
          const u32x2 a = *(LAS u32x2*)(kp + s0 * 16 + (7 - s1) * 512), c = *(LAS u32x2*)(kp + s0 * 16 + (7 - s1) * 512 + 8);
          U[s1] = *(LAS u32x4*)(up + (s0 & 1) * U_HALF + (s0 >> 1) * 16 + s1 * 256);
          Kf[s1] = (u32x4){__builtin_amdgcn_alignbit(a.x, carry[s1], sh), __builtin_amdgcn_alignbit(a.y, a.x, sh),
                           __builtin_amdgcn_alignbit(c.x, a.y, sh), __builtin_amdgcn_alignbit(c.y, c.x, sh)};
          carry[s1] = c.y;
        }
#pragma unroll
        for (int s1 = 0; s1 < 8; s1++)
#pragma unroll
          for (int m = s1; m < 8; m++) acc[m] = mfma_h(Kf[s1], U[m - s1], acc[m]);
      }
    }
    STAMP(5);
#pragma unroll
    for (int m = 0; m < 8; m++) *(LAS f32x4*)(Q + K_OFF + m * 1024 + q * 256 + ((r16 ^ (4 * q)) * 16)) = acc[m] * inv_scale;
    __syncthreads();
    {
      const int yq = (lane >> 1) & 3;
#pragma unroll
      for (int j = 0; j < 4; j++) {
        LAS char* const yp = lds + K_OFF + (2 * w + (j >> 1)) * 1024 + yq * 256 + ((((lane >> 3) + 8 * (j & 1)) ^ (4 * yq)) * 16) + (lane & 1) * 8;
        const f32x2 y0 = *(LAS f32x2*)yp, y1 = *(LAS f32x2*)(yp + QSZ), y2 = *(LAS f32x2*)(yp + 2 * QSZ), y3 = *(LAS f32x2*)(yp + 3 * QSZ);
        const u32x4 hv = {pk_bf16(lo16(xc[j][0].x) + y0.x, hi16(xc[j][0].x) + y1.x), pk_bf16(lo16(xc[j][0].y) + y2.x, hi16(xc[j][0].y) + y3.x),
                          pk_bf16(lo16(xc[j][1].x) + y0.y, hi16(xc[j][1].x) + y1.y), pk_bf16(lo16(xc[j][1].y) + y2.y, hi16(xc[j][1].y) + y3.y)};
        __builtin_amdgcn_raw_buffer_store_b128(hv, rsW, WS_H + (4 * w + j) * H_PANEL + g * 1024 + lane * 16, 0, AUX_SC1);
      }
    }
    STAMP(6);
    WAIT_VM0();
    __syncthreads();
    STAMP(7);
    if (tid < 8) __builtin_amdgcn_raw_buffer_store_b32(tag, rsW, WS_F2 + tid * 4096 + b * 16, 0, AUX_SC1);
    w1_dma(3, 5);
  } else {
    const int hid = xcd * 12 + idx - 20;
    if (hid < 64) {
      u32x4 v[2][5];
#pragma unroll
      for (int p = 0; p < 2; p++)
#pragma unroll
        for (int c = 0; c < 5; c++) v[p][c] = *(const u32x4*)(x + (32 * hid + 8 * w + 4 * p + q) * D + (r16 + 16 * c) * 8);
      __builtin_amdgcn_sched_barrier(0);
      tag = __builtin_amdgcn_readfirstlane(Ev) + 1u;
#pragma unroll
      for (int p = 0; p < 2; p++) {
        float s = 0.f;
#pragma unroll
        for (int c = 0; c < 5; c++)
#pragma unroll
          for (int k = 0; k < 4; k++) s += lo16(v[p][c][k]) + hi16(v[p][c][k]);
        const float mean = sum16(s) * (1.f / D);
        float ss = 0.f;
#pragma unroll
        for (int c = 0; c < 5; c++)
#pragma unroll
          for (int k = 0; k < 4; k++) {
            const float a = lo16(v[p][c][k]) - mean, d = hi16(v[p][c][k]) - mean;
            ss = __builtin_fmaf(a, a, __builtin_fmaf(d, d, ss));
          }
        const float var = sum16(ss) * (1.f / D);
        const float den = __builtin_amdgcn_sqrtf(rbf(rbf(var) + 1e-5f));
        if (r16 == 0) __builtin_amdgcn_raw_buffer_store_b64((u32x2){pk_bf16(mean, den), tag}, rsW, WS_ST + (32 * hid + 8 * w + 4 * p + q) * 8, 0, AUX_SC1);
      }
    } else {
      tag = __builtin_amdgcn_readfirstlane(Ev) + 1u;
    }
    STAMP(1);
    if (tid < 8) __builtin_amdgcn_raw_buffer_store_b32(tag, rsW, WS_F2 + tid * 4096 + b * 16, 0, AUX_SC1);
    w1_dma(0, 5);
  }

  WAIT_VM0();
#pragma unroll
  for (int i = 0; i < 3; i++)
    if (i * 64 + lane < 160) *(LAS u32x2*)(Q + (i * 64 + lane) * 224 + 208) = (u32x2){0x3f803f80u, 0x3f803f80u};
  __syncthreads();
  LAS char* const w1p = lds + (lane >> 2) * 224 + (lane & 3) * 8;
  f32x4 cs[7];
#pragma unroll
  for (int nt = 0; nt < 7; nt++) cs[nt] = (f32x4){0.f, 0.f, 0.f, 0.f};
  for (int qq = 0; qq < 4; qq++)
#pragma unroll
    for (int s = 0; s < 5; s++)
#pragma unroll
      for (int nt = 0; nt < 7; nt++)
        cs[nt] = mfma_bf(tr_frag(w1p + qq * QSZ + s * SLOT1 + nt * 32, 16 * 224), (u32x4){0x3f803f80u, 0x3f803f80u, 0x3f803f80u, 0x3f803f80u}, cs[nt]);
  STAMP(8);
  if (w == 0) {
    for (u32 spin = 0;; spin++) {
      bool ok = true;
#pragma unroll
      for (int j = 0; j < 4; j++) ok &= __builtin_amdgcn_raw_buffer_load_b32(rsW, WS_F2 + xcd * 4096 + (lane + 64 * j) * 16, 0, AUX_SC1) == tag;
      if (__all(ok) || spin > SPIN_CAP) break;
      __builtin_amdgcn_s_sleep(1);
      asm volatile("" ::: "memory");
    }
    if (b == 0 && lane == 0) __builtin_amdgcn_raw_buffer_store_b32(tag, rsW, WS_E, 0, AUX_SC1);
  }
  __syncthreads();
  STAMP(9);
  {
    u32x4 X[20][2];
    const u32 hoff = WS_H + pm * H_PANEL + q * 1024 + w * 256 + r16 * 16;
#pragma unroll
    for (int s = 0; s < 20; s++) {
      X[s][0] = __builtin_amdgcn_raw_buffer_load_b128(rsW, hoff, s * 8192, AUX_SC1);
      X[s][1] = __builtin_amdgcn_raw_buffer_load_b128(rsW, hoff, s * 8192 + 4096, AUX_SC1);
      __builtin_amdgcn_sched_barrier(0);
    }
    f32x4 a1[7][2];
#pragma unroll
    for (int nt = 0; nt < 7; nt++) a1[nt][0] = a1[nt][1] = (f32x4){0.f, 0.f, 0.f, 0.f};
    float ss0 = 0.f, ss1 = 0.f;
#pragma unroll
    for (int s = 0; s < 20; s++) {
      const u32x4 f0 = {X[s][0].x, X[s][0].y, X[s][1].x, X[s][1].y}, f1 = {X[s][0].z, X[s][0].w, X[s][1].z, X[s][1].w};
#pragma unroll
      for (int k = 0; k < 4; k++) {
        ss0 = dot2(f0[k], f0[k], ss0);
        ss1 = dot2(f1[k], f1[k], ss1);
      }
#pragma unroll
      for (int nt = 0; nt < 7; nt++) {
        const u32x4 wf = tr_frag(w1p + (s / 5) * QSZ + (s % 5) * SLOT1 + nt * 32, 16 * 224);
        a1[nt][0] = mfma_bf(wf, f0, a1[nt][0]);
        a1[nt][1] = mfma_bf(wf, f1, a1[nt][1]);
      }
    }
    STAMP(10);
    ss0 += __shfl_xor(ss0, 16); ss1 += __shfl_xor(ss1, 16);
    ss0 += __shfl_xor(ss0, 32); ss1 += __shfl_xor(ss1, 32);
    const float mean0 = __shfl(a1[6][0].x, 32 + r16) * (1.f / D), mean1 = __shfl(a1[6][1].x, 32 + r16) * (1.f / D);
    const float rs0 = __builtin_amdgcn_rsqf(__builtin_fmaxf(ss0 * (1.f / D) - mean0 * mean0, 0.f) + 1e-5f);
    const float rs1v = __builtin_amdgcn_rsqf(__builtin_fmaxf(ss1 * (1.f / D) - mean1 * mean1, 0.f) + 1e-5f);
    const u32 ooff = WS_HID + pm * HID_PANEL + (27 * jn + q) * 1024 + w * 256 + r16 * 16;
#pragma unroll
    for (int nt = 0; nt < 7; nt++) {
      f32x4 v0 = (a1[nt][0] - mean0 * cs[nt]) * rs0, v1 = (a1[nt][1] - mean1 * cs[nt]) * rs1v;
#pragma unroll
      for (int k = 0; k < 4; k++) { v0[k] = __builtin_fmaxf(v0[k], 0.f); v1[k] = __builtin_fmaxf(v1[k], 0.f); }
      const u32x4 o = {pk_bf16(v0.x, v0.y), pk_bf16(v0.z, v0.w), pk_bf16(v1.x, v1.y), pk_bf16(v1.z, v1.w)};
      if (nt < 6) __builtin_amdgcn_raw_buffer_store_b128(o, rsW, ooff, nt * 4096, AUX_SC1);
      else if (q != 2) __builtin_amdgcn_raw_buffer_store_b128(o, rsW, ooff - (q == 3 ? 1024 : 0), 6 * 4096, AUX_SC1);
    }
  }
  WAIT_VM0();
  __syncthreads();
  STAMP(11);
  if (tid == 0) __builtin_amdgcn_raw_buffer_store_b32(tag, rsW, WS_F3 + pm * 256 + jn * 4, 0, AUX_SC1);

  const int half = (idx >> 3) & 1, ct = idx & 7;
  const int ks0 = 14 * w - (w == 3 ? 1 : 0);
  auto w2_dma = [&](int s0, int s1, int slot0) {
#pragma unroll
    for (int i = 0; i < 5; i++) {
      const int n = i * 64 + lane, row = n / 10, ck = n - row * 10;
      const u32 voff = row * (D * 2) + ct * 160 + ck * 16;
      for (int s = s0; s < s1; s++) dma16(lds0 + w * QSZ + (slot0 + s - s0) * SLOT2 + i * 1024, voff, rs2, (ks0 + s) * 32 * (D * 2));
    }
  };
  w2_dma(0, 8, 0);
  WAIT_VM0();
  LAS char* const w2p = Q + (lane >> 2) * 160 + (lane & 3) * 8;
  u32x4 wr[6][5];
#pragma unroll
  for (int s = 0; s < 6; s++)
#pragma unroll
    for (int nt = 0; nt < 5; nt++) wr[s][nt] = tr_frag(w2p + s * SLOT2 + nt * 32, 16 * 160);
#pragma unroll
  for (int s = 0; s < 6; s++)
#pragma unroll
    for (int nt = 0; nt < 5; nt++) asm volatile("" : "+v"(wr[s][nt]));
  w2_dma(8, 14, 0);
  STAMP(12);
  if (w == 0) {
    for (u32 spin = 0;; spin++) {
      const bool ok = __builtin_amdgcn_raw_buffer_load_b32(rsW, WS_F3 + pm * 256 + r16 * 4, 0, AUX_SC1) == tag;
      if (__all(ok) || spin > SPIN_CAP) break;
      __builtin_amdgcn_s_sleep(1);
      asm volatile("" ::: "memory");
    }
  }
  __syncthreads();
  STAMP(13);
  {
    u32x4 X[14][4];
    const u32 aoff = WS_HID + pm * HID_PANEL + (8 * ks0 + q) * 1024 + half * 512 + r16 * 16;
#pragma unroll
    for (int s = 0; s < 14; s++) {
      if (s < 13 || w < 2) {
        X[s][0] = __builtin_amdgcn_raw_buffer_load_b128(rsW, aoff, s * 8192, AUX_SC1);
        X[s][1] = __builtin_amdgcn_raw_buffer_load_b128(rsW, aoff, s * 8192 + 4096, AUX_SC1);
        X[s][2] = __builtin_amdgcn_raw_buffer_load_b128(rsW, aoff + 256, s * 8192, AUX_SC1);
        X[s][3] = __builtin_amdgcn_raw_buffer_load_b128(rsW, aoff + 256, s * 8192 + 4096, AUX_SC1);
      } else {
        X[s][0] = X[s][1] = X[s][2] = X[s][3] = (u32x4){0u, 0u, 0u, 0u};
      }
      __builtin_amdgcn_sched_barrier(0);
    }
    const int tok = half * 64 + 32 * (w >> 1) + 2 * r16 + (w & 1);
    u32x2 res[5];
#pragma unroll
    for (int nt = 0; nt < 5; nt++) res[nt] = __builtin_amdgcn_raw_buffer_load_b64(rsW, WS_H + pm * H_PANEL + (ct * 20 + q) * 1024 + tok * 8, nt * 4096, AUX_SC1);
    __builtin_amdgcn_sched_barrier(0);
    f32x4 a2[4][5];
#pragma unroll
    for (int mt = 0; mt < 4; mt++)
#pragma unroll
      for (int nt = 0; nt < 5; nt++) a2[mt][nt] = (f32x4){0.f, 0.f, 0.f, 0.f};
#pragma unroll
    for (int s = 0; s < 14; s++) {
      u32x4 f[4];
#pragma unroll
      for (int hh = 0; hh < 2; hh++) {
        f[2 * hh] = (u32x4){X[s][2 * hh].x, X[s][2 * hh].y, X[s][2 * hh + 1].x, X[s][2 * hh + 1].y};
        f[2 * hh + 1] = (u32x4){X[s][2 * hh].z, X[s][2 * hh].w, X[s][2 * hh + 1].z, X[s][2 * hh + 1].w};
      }
      if (s == 6) {
        asm volatile("" : "+v"(a2[0][0]) :: "memory");
      }
#pragma unroll
      for (int nt = 0; nt < 5; nt++) {
        const u32x4 wf = s < 6 ? wr[s][nt] : tr_frag(w2p + (s < 8 ? s : s - 8) * SLOT2 + nt * 32, 16 * 160);
#pragma unroll
        for (int mt = 0; mt < 4; mt++) a2[mt][nt] = mfma_bf(wf, f[mt], a2[mt][nt]);
      }
    }
    STAMP(14);
#pragma unroll
    for (int mt = 0; mt < 4; mt++)
#pragma unroll
      for (int nt = 0; nt < 5; nt++) *(LAS f32x4*)(Q + (mt * 5 + nt) * 1024 + lane * 16) = a2[mt][nt];
    __syncthreads();
#pragma unroll
    for (int nt = 0; nt < 5; nt++) {
      LAS char* const p = lds + (w * 5 + nt) * 1024 + lane * 16;
      const f32x4 v = (*(LAS f32x4*)p + *(LAS f32x4*)(p + QSZ)) + (*(LAS f32x4*)(p + 2 * QSZ) + *(LAS f32x4*)(p + 3 * QSZ));
      const u32x2 o = {pk_bf16(lo16(res[nt].x) + v.x, hi16(res[nt].x) + v.y), pk_bf16(lo16(res[nt].y) + v.z, hi16(res[nt].y) + v.w)};
      *(u32x2*)(out + (pm * 128 + tok) * D + ct * 80 + nt * 16 + q * 4) = o;
    }
  }
  asm volatile(".Lmega_end:" :: "v"(pfv));
  STAMP(15);
}

extern "C" void kernel_launch(void* const* d_in, const int* in_sizes, int n_in, void* d_out, int out_size, void* d_ws, size_t ws_size,
                              hipStream_t stream) {
  mega<<<256, 256, 0, stream>>>((const u16*)d_in[0], (const u16*)d_in[1], (const u16*)d_in[2], (const u16*)d_in[3], (const u16*)d_in[4],
                                (const u16*)d_in[5], (const u16*)d_in[6], (const u16*)d_in[7], (const u16*)d_in[8], (const u16*)d_in[10],
                                (const u16*)d_in[11], (u16*)d_out, (char*)d_ws);
}
```

```cpp
#include <hip/hip_runtime.h>

typedef unsigned short u16;
typedef unsigned u32;
typedef unsigned long long u64;
typedef u32 u32x2 __attribute__((ext_vector_type(2)));
typedef u32 u32x4 __attribute__((ext_vector_type(4)));
typedef float f32x2 __attribute__((ext_vector_type(2)));
typedef float f32x4 __attribute__((ext_vector_type(4)));
typedef float f32x16 __attribute__((ext_vector_type(16)));
typedef _Float16 f16x2 __attribute__((ext_vector_type(2)));
typedef _Float16 f16x8 __attribute__((ext_vector_type(8)));
typedef __bf16 bf16x2 __attribute__((ext_vector_type(2)));
typedef __bf16 bf16x8 __attribute__((ext_vector_type(8)));
typedef short s16x4 __attribute__((ext_vector_type(4)));
#define LAS __attribute__((address_space(3)))
#define DI __device__ __forceinline__
#define BC(T, v) __builtin_bit_cast(T, v)

constexpr int L = 2048, D = 640, F = 1728;
constexpr u32 WS_E = 0;
constexpr u32 WS_F2 = 4096;
constexpr u32 WS_F3 = WS_F2 + 8 * 4096;
constexpr u32 WS_ST = 65536;
constexpr u32 WS_H = 1u << 20;
constexpr u32 WS_HID = 4u << 20;
constexpr u32 WS_END = 12u << 20;
constexpr u32 H_PANEL = 160 * 1024, HID_PANEL = 432 * 1024;
constexpr int QSZ = 40960, SLOT1 = 7168, SLOT2 = 5120;
constexpr int KT_ROW = 528, KT_HI = 32 * KT_ROW;
constexpr int K_OFF = 27520, K_COPY = 4160;
constexpr int U_OFF = 35840, U_HALF = 2368;
constexpr int AUX_SC1 = 16;
constexpr u32 SPIN_CAP = 1u << 18;

#define STAMP(k)

DI float lo16(u32 v) { return BC(float, v << 16); }
DI float hi16(u32 v) { return BC(float, v & 0xffff0000u); }
DI u32 pk_bf16(float a, float b) { return BC(u32, __builtin_convertvector((f32x2){a, b}, bf16x2)); }
DI u32 pk_f16(float a, float b) { return BC(u32, __builtin_convertvector((f32x2){a, b}, f16x2)); }
DI float dot2(u32 a, u32 b, float c) { return __builtin_amdgcn_fdot2_f32_bf16(BC(bf16x2, a), BC(bf16x2, b), c, false); }
DI float rbf(float a) { return lo16(pk_bf16(a, 0.f)); }
template <int CTRL, int RM> DI float dpp_add(float v) {
  return v + BC(float, __builtin_amdgcn_update_dpp(0, BC(int, v), CTRL, RM, 0xf, true));
}
DI float sum16(float v) {
  v = dpp_add<0x121, 0xf>(v); v = dpp_add<0x122, 0xf>(v); v = dpp_add<0x124, 0xf>(v); return dpp_add<0x128, 0xf>(v);
}
struct Cx { f32x2 c, r; };
DI Cx mk(f32x2 c) { Cx o; o.c = c; o.r = (f32x2){-c.y, c.x}; return o; }
DI f32x2 cmul(f32x2 v, const Cx& m) { return __builtin_elementwise_fma(v.yy, m.r, v.xx * m.c); }
DI f32x2 conj(f32x2 v) { return (f32x2){v.x, -v.y}; }
DI void dma16(u32 lds_addr, u32 voff, __amdgpu_buffer_rsrc_t rs, u32 soff) {
  u32 keep;
  asm volatile("s_mov_b32 %0, m0\n\ts_mov_b32 m0, %1\n\ts_nop 3\n\tbuffer_load_dwordx4 %2, %3, %4 offen lds\n\ts_mov_b32 m0, %0"
               : "=&s"(keep) : "s"(lds_addr), "v"(voff), "s"(rs), "s"(soff) : "memory");
}
#define WAIT_VM0() asm volatile("s_waitcnt vmcnt(0)" ::: "memory")
DI f32x4 mfma_bf(u32x4 a, u32x4 b, f32x4 c) { return __builtin_amdgcn_mfma_f32_16x16x32_bf16(BC(bf16x8, a), BC(bf16x8, b), c, 0, 0, 0); }
DI f32x4 mfma_h(u32x4 a, u32x4 b, f32x4 c) { return __builtin_amdgcn_mfma_f32_16x16x32_f16(BC(f16x8, a), BC(f16x8, b), c, 0, 0, 0); }
DI u32x4 tr_frag(LAS char* p, int t_stride) {
  u32x2 a = BC(u32x2, __builtin_amdgcn_ds_read_tr16_b64_v4i16((LAS s16x4*)p));
  u32x2 b = BC(u32x2, __builtin_amdgcn_ds_read_tr16_b64_v4i16((LAS s16x4*)(p + t_stride)));
  return (u32x4){a.x, a.y, b.x, b.y};
}

__device__ inline u64 rtc(){ u64 t; asm volatile("s_memrealtime %0\n\ts_waitcnt lgkmcnt(0)" : "=s"(t) :: "memory"); return t; }
__global__ void preA(char* ws){ u64 t=rtc(); *(u64*)(ws+(64u<<20))=t; }
__global__ void postB(char* ws, int spin){ u64 t=rtc(); if(spin){ u64 m=0; for(int i=threadIdx.x;i<256;i+=64){ u64 v=((u64*)(ws+(65u<<20)))[i]; m=v>m?v:m; }
  for(int o=32;o;o>>=1){ u64 v=__shfl_xor(m,o); m=v>m?v:m; } u64 gap=t-m; if(gap>3000) gap=3000; u64 e=rtc()+gap; while(rtc()<e); } }
__global__ __launch_bounds__(256, 1) void mega(const u16* __restrict__ x, const u16* __restrict__ Are, const u16* __restrict__ Aim,
                                               const u16* __restrict__ Bre, const u16* __restrict__ Bim, const u16* __restrict__ Cre,
                                               const u16* __restrict__ Cim, const u16* __restrict__ logdt, const u16* __restrict__ Dskip,
                                               const u16* __restrict__ w1, const u16* __restrict__ w2, u16* __restrict__ out, char* ws) {
  __shared__ __attribute__((aligned(16))) char smem[4 * QSZ];
  LAS char* const lds = (LAS char*)smem;
  const u32 lds0 = (u32)(size_t)lds;
  const int b = blockIdx.x, tid = threadIdx.x, lane = tid & 63;
  const int w = __builtin_amdgcn_readfirstlane(tid >> 6);
  const int xcd = b & 7, idx = b >> 3;
  const int r16 = lane & 15, q = lane >> 4;
  u64 q0 = rtc();
  u32 pfv = 0;
  {
    u64 pc; u32 len;
    asm volatile("s_getpc_b64 %0\n.Lmega_pf:\n\ts_mov_b32 %1, .Lmega_end-.Lmega_pf" : "=s"(pc), "=s"(len));
    const __amdgpu_buffer_rsrc_t rsC = __builtin_amdgcn_make_buffer_rsrc((void*)pc, 0, len, 0x00020000);
    if (tid < 16) pfv = __builtin_amdgcn_raw_buffer_load_b32(rsC, (tid * 32 + idx) * 128, 0, 0);
  }
  const __amdgpu_buffer_rsrc_t rsW = __builtin_amdgcn_make_buffer_rsrc(ws, 0, WS_END, 0x00020000);
  const __amdgpu_buffer_rsrc_t rs1 = __builtin_amdgcn_make_buffer_rsrc((void*)w1, 0, D * F * 2, 0x00020000);
  const __amdgpu_buffer_rsrc_t rs2 = __builtin_amdgcn_make_buffer_rsrc((void*)w2, 0, D * F * 2, 0x00020000);
  const u32 Ev = __builtin_amdgcn_raw_buffer_load_b32(rsW, WS_E, 0, AUX_SC1);
  LAS char* const Q = lds + w * QSZ;
  const int pm = 2 * xcd + (idx >> 4), jn = idx & 15;
  u32 tag;

  auto w1_dma = [&](int s0, int s1) {
#pragma unroll
    for (int i = 0; i < 7; i++) {
      const int n = i * 64 + lane, row = n / 14, ck = n - row * 14;
      const u32 voff = row * (F * 2) + jn * 216 + ck * 16 - (ck == 13 ? 8 : 0);
      for (int s = s0; s < s1; s++) dma16(lds0 + w * QSZ + s * SLOT1 + i * 1024, voff, rs1, (5 * w + s) * 32 * (F * 2));
    }
  };

  if (idx < 20) {
    const int g = xcd * 20 + idx, ch = 4 * g + w;
    const u32 pAre = ((const u32*)Are)[ch * 64 + lane], pAim = ((const u32*)Aim)[ch * 64 + lane];
    const u32 pBre = ((const u32*)Bre)[ch * 64 + lane], pBim = ((const u32*)Bim)[ch * 64 + lane];
    const u32 pCre = ((const u32*)Cre)[ch * 64 + lane], pCim = ((const u32*)Cim)[ch * 64 + lane];
    const float ldt = lo16(logdt[ch]), dsk = lo16(Dskip[ch]);
    __builtin_amdgcn_sched_barrier(0);
    u32x2 xc[4][2];
#pragma unroll
    for (int j = 0; j < 4; j++)
#pragma unroll
      for (int e = 0; e < 2; e++) xc[j][e] = *(const u32x2*)(x + (512 * w + 128 * j + 2 * lane + e) * D + 4 * g);
    __builtin_amdgcn_sched_barrier(0);

    const float dt = rbf(__builtin_amdgcn_exp2f(ldt * 1.44269504088896f));
    f32x2 W[2], l1[2], l8[2], l32[2], l256[2];
    float S = 0.f;
#pragma unroll
    for (int m = 0; m < 2; m++) {
      const float are = dt * (m ? hi16(pAre) : lo16(pAre)), aim = dt * (m ? hi16(pAim) : lo16(pAim));
      const float br = dt * (m ? hi16(pBre) : lo16(pBre)), bi = dt * (m ? hi16(pBim) : lo16(pBim));
      const float cr = m ? hi16(pCre) : lo16(pCre), ci = m ? hi16(pCim) : lo16(pCim);
      W[m] = (f32x2){cr * br - ci * bi, cr * bi + ci * br};
      S += __builtin_fabsf(W[m].x) + __builtin_fabsf(W[m].y);
      const float ar2 = are * 1.44269504088896f;
      const float th = aim * 0.15915494f;
      const float tl = __builtin_fmaf(aim, 6.4206383e-9f, __builtin_fmaf(aim, 0.15915494f, -th));
      auto cexp = [&](float l) {
        const float mag = __builtin_amdgcn_exp2f(ar2 * l);
        const float ph = __builtin_fmaf(tl, l, __builtin_amdgcn_fractf(th * l));
        return (f32x2){mag * __builtin_amdgcn_cosf(ph), mag * __builtin_amdgcn_sinf(ph)};
      };
      l1[m] = cexp(1.f); l8[m] = cexp(8.f); l32[m] = cexp(32.f); l256[m] = cexp(256.f);
    }
    STAMP(1);
    S = sum16(S); S = dpp_add<0x142, 0xa>(S); S = dpp_add<0x143, 0xc>(S);
    S = BC(float, __builtin_amdgcn_readlane(BC(int, S), 63)) + __builtin_fabsf(dsk);
    int ex = (int)(BC(u32, S) >> 23);
    ex = ex < 14 ? 14 : ex;
    const float scale = BC(float, (u32)(267 - ex) << 23), inv_scale = BC(float, (u32)(ex - 13) << 23);

    LAS char* const trow = Q + lane * 8;
    auto put = [&](int off, f32x2 a, f32x2 c) { *(LAS u32x2*)(trow + off) = (u32x2){pk_f16(a.x, a.y), pk_f16(c.x, c.y)}; };
    {
      f32x2 P[2][4];
      Cx st[2];
#pragma unroll
      for (int m = 0; m < 2; m++) {
        const Cx c8 = mk(l8[m]);
        P[m][0] = (f32x2){1.f, 0.f}; P[m][1] = l8[m]; P[m][2] = cmul(l8[m], c8); P[m][3] = cmul(P[m][2], c8);
        st[m] = mk(l32[m]);
      }
#pragma unroll
      for (int a = 0; a < 8; a++)
#pragma unroll
        for (int c = 0; c < 4; c++) {
          if (a == 1 && c == 0) { P[0][0] = l32[0]; P[1][0] = l32[1]; }
          else if (a) { P[0][c] = cmul(P[0][c], st[0]); P[1][c] = cmul(P[1][c], st[1]); }
          put((4 * a + c) * KT_ROW, P[0][c], P[1][c]);
        }
    }
    {
      f32x2 V[2][9];
      Cx mu[2];
#pragma unroll
      for (int m = 0; m < 2; m++) {
        const Cx c1 = mk(conj(l1[m]));
        V[m][0] = conj(W[m]) * scale;
#pragma unroll
        for (int s = 1; s < 9; s++) V[m][s] = cmul(V[m][s - 1], c1);
        mu[m] = mk(conj(l256[m]));
      }
#pragma unroll
      for (int s = 0; s < 8; s++) put(KT_HI + s * KT_ROW, V[0][s], V[1][s]);
      auto adv = [&](int s, int row) { V[0][s] = cmul(V[0][s], mu[0]); V[1][s] = cmul(V[1][s], mu[1]); put(KT_HI + row * KT_ROW, V[0][s], V[1][s]); };
      adv(0, 8); adv(2, 9); adv(4, 10); put(KT_HI + 12 * KT_ROW, V[0][4], V[1][4]); adv(6, 13); adv(8, 14);
      adv(0, 16); adv(4, 17); adv(8, 18);
      adv(0, 20); adv(4, 21); adv(8, 22);
#pragma unroll
      for (int z = 0; z < 4; z++) { adv(0, 24 + 2 * z); adv(8, 25 + 2 * z); }
    }
    STAMP(2);
    u32x4 st[4];
    const u32 st_off = WS_ST + (512 * w + 2 * lane) * 8;
#pragma unroll
    for (int j = 0; j < 4; j++) st[j] = __builtin_amdgcn_raw_buffer_load_b128(rsW, st_off + j * 1024, 0, AUX_SC1);
    __builtin_amdgcn_sched_barrier(0);
    f32x16 kd = {0.f, 0.f, 0.f, 0.f, 0.f, 0.f, 0.f, 0.f, 0.f, 0.f, 0.f, 0.f, 0.f, 0.f, 0.f, 0.f};
    const int r0 = lane & 31, hh = lane >> 5;
    {
      LAS char* const fr = Q + r0 * KT_ROW + hh * 16;
#pragma unroll
      for (int s = 0; s < 16; s++)
        kd = __builtin_amdgcn_mfma_f32_32x32x16_f16(*(LAS f16x8*)(fr + KT_HI + s * 32), *(LAS f16x8*)(fr + s * 32), kd, 0, 0, 0);
    }
    {
      if (lane == 0) kd[0] += dsk * scale;
      auto put2 = [&](int el, u32 d0, u32 d1) {
        LAS char* p = Q + K_OFF + el * 2;
        *(LAS u32x2*)p = (u32x2){d0, d1};
        *(LAS u32*)(p + K_COPY + 4) = d0; *(LAS u32*)(p + K_COPY + 8) = d1;
      };
      put2(2044 - 8 * r0 - 4 * hh, pk_f16(kd[3], kd[2]), pk_f16(kd[1], kd[0]));
      put2(2044 - 256 - 8 * r0 - 4 * hh, pk_f16(kd[6], kd[5]), pk_f16(kd[4], kd[4]));
      const int e2 = 2040 - 512 - 256 * hh - 8 * r0;
      put2(e2, pk_f16(kd[10], kd[10]), pk_f16(kd[9], kd[9]));
      put2(e2 + 4, pk_f16(kd[9], kd[8]), pk_f16(kd[8], kd[8]));
#pragma unroll
      for (int z = 0; z < 2; z++) {
        const float a = kd[12 + 2 * z], c = kd[13 + 2 * z], t = (r0 & 1) ? c : a;
        const int e3 = 2040 - 1024 - 512 * hh - 256 * z - 8 * r0;
        const u32 aa = pk_f16(a, a);
        put2(e3, pk_f16(c, c), pk_f16(c, t));
        put2(e3 + 4, aa, aa);
      }
      if (lane < 4) put2(2048 + 4 * lane, 0u, 0u);
      if (lane < 30) *(LAS u32x4*)(Q + U_OFF + (lane & 1) * U_HALF + (lane >> 1) * 16) = (u32x4){0u, 0u, 0u, 0u};
    }
    STAMP(3);
    tag = __builtin_amdgcn_readfirstlane(Ev) + 1u;
    for (u32 spin = 0;; spin++) {
      bool ok = true;
#pragma unroll
      for (int j = 0; j < 4; j++) ok &= (st[j].y == tag) & (st[j].w == tag);
      if (__all(ok) || spin > SPIN_CAP) break;
      __builtin_amdgcn_s_sleep(1);
      asm volatile("" ::: "memory");
#pragma unroll
      for (int j = 0; j < 4; j++) st[j] = __builtin_amdgcn_raw_buffer_load_b128(rsW, st_off + j * 1024, 0, AUX_SC1);
    }
    {
      LAS char* const up = lds + U_OFF + ((lane >> 2) & 1) * U_HALF + (32 * w + 15 + (lane >> 3)) * 16 + (lane & 3) * 4;
#pragma unroll
      for (int j = 0; j < 4; j++) {
        const float m0 = lo16(st[j].x), i0 = __builtin_amdgcn_rcpf(hi16(st[j].x));
        const float m1 = lo16(st[j].z), i1 = __builtin_amdgcn_rcpf(hi16(st[j].z));
        *(LAS u32*)(up + j * 128) = pk_f16((lo16(xc[j][0].x) - m0) * i0, (lo16(xc[j][1].x) - m1) * i1);
        *(LAS u32*)(up + j * 128 + QSZ) = pk_f16((hi16(xc[j][0].x) - m0) * i0, (hi16(xc[j][1].x) - m1) * i1);
        *(LAS u32*)(up + j * 128 + 2 * QSZ) = pk_f16((lo16(xc[j][0].y) - m0) * i0, (lo16(xc[j][1].y) - m1) * i1);
        *(LAS u32*)(up + j * 128 + 3 * QSZ) = pk_f16((hi16(xc[j][0].y) - m0) * i0, (hi16(xc[j][1].y) - m1) * i1);
      }
    }
    w1_dma(0, 3);
    __syncthreads();
    STAMP(4);
    f32x4 acc[8];
#pragma unroll
    for (int m = 0; m < 8; m++) acc[m] = (f32x4){0.f, 0.f, 0.f, 0.f};
    {
      const int e0 = 15 - r16 + 64 * q;
      const int d0 = (e0 >> 1) + 1;
      const u32 sh = (e0 & 1) * 16;
      LAS char* kp = Q + K_OFF + (d0 & 1) * (K_COPY + 4) + d0 * 4;
      LAS char* const up = Q + U_OFF + (r16 + 4 * q) * 16;
      u32 carry[8];
#pragma unroll
      for (int s1 = 0; s1 < 8; s1++) carry[s1] = *(LAS u32*)(kp - 4 + (7 - s1) * 512);
#pragma unroll 2
      for (int s0 = 0; s0 < 8; s0++) {
        u32x4 U[8], Kf[8];
#pragma unroll
        for (int s1 = 0; s1 < 8; s1++) {
          const u32x2 a = *(LAS u32x2*)(kp + s0 * 16 + (7 - s1) * 512), c = *(LAS u32x2*)(kp + s0 * 16 + (7 - s1) * 512 + 8);
          U[s1] = *(LAS u32x4*)(up + (s0 & 1) * U_HALF + (s0 >> 1) * 16 + s1 * 256);
          Kf[s1] = (u32x4){__builtin_amdgcn_alignbit(a.x, carry[s1], sh), __builtin_amdgcn_alignbit(a.y, a.x, sh),
                           __builtin_amdgcn_alignbit(c.x, a.y, sh), __builtin_amdgcn_alignbit(c.y, c.x, sh)};
          carry[s1] = c.y;
        }
#pragma unroll
        for (int s1 = 0; s1 < 8; s1++)
#pragma unroll
          for (int m = s1; m < 8; m++) acc[m] = mfma_h(Kf[s1], U[m - s1], acc[m]);
      }
    }
    STAMP(5);
#pragma unroll
    for (int m = 0; m < 8; m++) *(LAS f32x4*)(Q + K_OFF + m * 1024 + q * 256 + ((r16 ^ (4 * q)) * 16)) = acc[m] * inv_scale;
    __syncthreads();
    {
      const int yq = (lane >> 1) & 3;
#pragma unroll
      for (int j = 0; j < 4; j++) {
        LAS char* const yp = lds + K_OFF + (2 * w + (j >> 1)) * 1024 + yq * 256 + ((((lane >> 3) + 8 * (j & 1)) ^ (4 * yq)) * 16) + (lane & 1) * 8;
        const f32x2 y0 = *(LAS f32x2*)yp, y1 = *(LAS f32x2*)(yp + QSZ), y2 = *(LAS f32x2*)(yp + 2 * QSZ), y3 = *(LAS f32x2*)(yp + 3 * QSZ);
        const u32x4 hv = {pk_bf16(lo16(xc[j][0].x) + y0.x, hi16(xc[j][0].x) + y1.x), pk_bf16(lo16(xc[j][0].y) + y2.x, hi16(xc[j][0].y) + y3.x),
                          pk_bf16(lo16(xc[j][1].x) + y0.y, hi16(xc[j][1].x) + y1.y), pk_bf16(lo16(xc[j][1].y) + y2.y, hi16(xc[j][1].y) + y3.y)};
        __builtin_amdgcn_raw_buffer_store_b128(hv, rsW, WS_H + (4 * w + j) * H_PANEL + g * 1024 + lane * 16, 0, AUX_SC1);
      }
    }
    STAMP(6);
    WAIT_VM0();
    __syncthreads();
    STAMP(7);
    if (tid < 8) __builtin_amdgcn_raw_buffer_store_b32(tag, rsW, WS_F2 + tid * 4096 + b * 16, 0, AUX_SC1);
    w1_dma(3, 5);
  } else {
    const int hid = xcd * 12 + idx - 20;
    if (hid < 64) {
      u32x4 v[2][5];
#pragma unroll
      for (int p = 0; p < 2; p++)
#pragma unroll
        for (int c = 0; c < 5; c++) v[p][c] = *(const u32x4*)(x + (32 * hid + 8 * w + 4 * p + q) * D + (r16 + 16 * c) * 8);
      __builtin_amdgcn_sched_barrier(0);
      tag = __builtin_amdgcn_readfirstlane(Ev) + 1u;
#pragma unroll
      for (int p = 0; p < 2; p++) {
        float s = 0.f;
#pragma unroll
        for (int c = 0; c < 5; c++)
#pragma unroll
          for (int k = 0; k < 4; k++) s += lo16(v[p][c][k]) + hi16(v[p][c][k]);
        const float mean = sum16(s) * (1.f / D);
        float ss = 0.f;
#pragma unroll
        for (int c = 0; c < 5; c++)
#pragma unroll
          for (int k = 0; k < 4; k++) {
            const float a = lo16(v[p][c][k]) - mean, d = hi16(v[p][c][k]) - mean;
            ss = __builtin_fmaf(a, a, __builtin_fmaf(d, d, ss));
          }
        const float var = sum16(ss) * (1.f / D);
        const float den = __builtin_amdgcn_sqrtf(rbf(rbf(var) + 1e-5f));
        if (r16 == 0) __builtin_amdgcn_raw_buffer_store_b64((u32x2){pk_bf16(mean, den), tag}, rsW, WS_ST + (32 * hid + 8 * w + 4 * p + q) * 8, 0, AUX_SC1);
      }
    } else {
      tag = __builtin_amdgcn_readfirstlane(Ev) + 1u;
    }
    STAMP(1);
    if (tid < 8) __builtin_amdgcn_raw_buffer_store_b32(tag, rsW, WS_F2 + tid * 4096 + b * 16, 0, AUX_SC1);
    w1_dma(0, 5);
  }

  WAIT_VM0();
#pragma unroll
  for (int i = 0; i < 3; i++)
    if (i * 64 + lane < 160) *(LAS u32x2*)(Q + (i * 64 + lane) * 224 + 208) = (u32x2){0x3f803f80u, 0x3f803f80u};
  __syncthreads();
  LAS char* const w1p = lds + (lane >> 2) * 224 + (lane & 3) * 8;
  f32x4 cs[7];
#pragma unroll
  for (int nt = 0; nt < 7; nt++) cs[nt] = (f32x4){0.f, 0.f, 0.f, 0.f};
  for (int qq = 0; qq < 4; qq++)
#pragma unroll
    for (int s = 0; s < 5; s++)
#pragma unroll
      for (int nt = 0; nt < 7; nt++)
        cs[nt] = mfma_bf(tr_frag(w1p + qq * QSZ + s * SLOT1 + nt * 32, 16 * 224), (u32x4){0x3f803f80u, 0x3f803f80u, 0x3f803f80u, 0x3f803f80u}, cs[nt]);
  STAMP(8);
  if (w == 0) {
    for (u32 spin = 0;; spin++) {
      bool ok = true;
#pragma unroll
      for (int j = 0; j < 4; j++) ok &= __builtin_amdgcn_raw_buffer_load_b32(rsW, WS_F2 + xcd * 4096 + (lane + 64 * j) * 16, 0, AUX_SC1) == tag;
      if (__all(ok) || spin > SPIN_CAP) break;
      __builtin_amdgcn_s_sleep(1);
      asm volatile("" ::: "memory");
    }
    if (b == 0 && lane == 0) __builtin_amdgcn_raw_buffer_store_b32(tag, rsW, WS_E, 0, AUX_SC1);
  }
  __syncthreads();
  STAMP(9);
  {
    u32x4 X[20][2];
    const u32 hoff = WS_H + pm * H_PANEL + q * 1024 + w * 256 + r16 * 16;
#pragma unroll
    for (int s = 0; s < 20; s++) {
      X[s][0] = __builtin_amdgcn_raw_buffer_load_b128(rsW, hoff, s * 8192, AUX_SC1);
      X[s][1] = __builtin_amdgcn_raw_buffer_load_b128(rsW, hoff, s * 8192 + 4096, AUX_SC1);
      __builtin_amdgcn_sched_barrier(0);
    }
    f32x4 a1[7][2];
#pragma unroll
    for (int nt = 0; nt < 7; nt++) a1[nt][0] = a1[nt][1] = (f32x4){0.f, 0.f, 0.f, 0.f};
    float ss0 = 0.f, ss1 = 0.f;
#pragma unroll
    for (int s = 0; s < 20; s++) {
      const u32x4 f0 = {X[s][0].x, X[s][0].y, X[s][1].x, X[s][1].y}, f1 = {X[s][0].z, X[s][0].w, X[s][1].z, X[s][1].w};
#pragma unroll
      for (int k = 0; k < 4; k++) {
        ss0 = dot2(f0[k], f0[k], ss0);
        ss1 = dot2(f1[k], f1[k], ss1);
      }
#pragma unroll
      for (int nt = 0; nt < 7; nt++) {
        const u32x4 wf = tr_frag(w1p + (s / 5) * QSZ + (s % 5) * SLOT1 + nt * 32, 16 * 224);
        a1[nt][0] = mfma_bf(wf, f0, a1[nt][0]);
        a1[nt][1] = mfma_bf(wf, f1, a1[nt][1]);
      }
    }
    STAMP(10);
    ss0 += __shfl_xor(ss0, 16); ss1 += __shfl_xor(ss1, 16);
    ss0 += __shfl_xor(ss0, 32); ss1 += __shfl_xor(ss1, 32);
    const float mean0 = __shfl(a1[6][0].x, 32 + r16) * (1.f / D), mean1 = __shfl(a1[6][1].x, 32 + r16) * (1.f / D);
    const float rs0 = __builtin_amdgcn_rsqf(__builtin_fmaxf(ss0 * (1.f / D) - mean0 * mean0, 0.f) + 1e-5f);
    const float rs1v = __builtin_amdgcn_rsqf(__builtin_fmaxf(ss1 * (1.f / D) - mean1 * mean1, 0.f) + 1e-5f);
    const u32 ooff = WS_HID + pm * HID_PANEL + (27 * jn + q) * 1024 + w * 256 + r16 * 16;
#pragma unroll
    for (int nt = 0; nt < 7; nt++) {
      f32x4 v0 = (a1[nt][0] - mean0 * cs[nt]) * rs0, v1 = (a1[nt][1] - mean1 * cs[nt]) * rs1v;
#pragma unroll
      for (int k = 0; k < 4; k++) { v0[k] = __builtin_fmaxf(v0[k], 0.f); v1[k] = __builtin_fmaxf(v1[k], 0.f); }
      const u32x4 o = {pk_bf16(v0.x, v0.y), pk_bf16(v0.z, v0.w), pk_bf16(v1.x, v1.y), pk_bf16(v1.z, v1.w)};
      if (nt < 6) __builtin_amdgcn_raw_buffer_store_b128(o, rsW, ooff, nt * 4096, AUX_SC1);
      else if (q != 2) __builtin_amdgcn_raw_buffer_store_b128(o, rsW, ooff - (q == 3 ? 1024 : 0), 6 * 4096, AUX_SC1);
    }
  }
  WAIT_VM0();
  __syncthreads();
  STAMP(11);
  if (tid == 0) __builtin_amdgcn_raw_buffer_store_b32(tag, rsW, WS_F3 + pm * 256 + jn * 4, 0, AUX_SC1);

  const int half = (idx >> 3) & 1, ct = idx & 7;
  const int ks0 = 14 * w - (w == 3 ? 1 : 0);
  auto w2_dma = [&](int s0, int s1, int slot0) {
#pragma unroll
    for (int i = 0; i < 5; i++) {
      const int n = i * 64 + lane, row = n / 10, ck = n - row * 10;
      const u32 voff = row * (D * 2) + ct * 160 + ck * 16;
      for (int s = s0; s < s1; s++) dma16(lds0 + w * QSZ + (slot0 + s - s0) * SLOT2 + i * 1024, voff, rs2, (ks0 + s) * 32 * (D * 2));
    }
  };
  w2_dma(0, 8, 0);
  WAIT_VM0();
  LAS char* const w2p = Q + (lane >> 2) * 160 + (lane & 3) * 8;
  u32x4 wr[6][5];
#pragma unroll
  for (int s = 0; s < 6; s++)
#pragma unroll
    for (int nt = 0; nt < 5; nt++) wr[s][nt] = tr_frag(w2p + s * SLOT2 + nt * 32, 16 * 160);
#pragma unroll
  for (int s = 0; s < 6; s++)
#pragma unroll
    for (int nt = 0; nt < 5; nt++) asm volatile("" : "+v"(wr[s][nt]));
  w2_dma(8, 14, 0);
  STAMP(12);
  if (w == 0) {
    for (u32 spin = 0;; spin++) {
      const bool ok = __builtin_amdgcn_raw_buffer_load_b32(rsW, WS_F3 + pm * 256 + r16 * 4, 0, AUX_SC1) == tag;
      if (__all(ok) || spin > SPIN_CAP) break;
      __builtin_amdgcn_s_sleep(1);
      asm volatile("" ::: "memory");
    }
  }
  __syncthreads();
  STAMP(13);
  {
    u32x4 X[14][4];
    const u32 aoff = WS_HID + pm * HID_PANEL + (8 * ks0 + q) * 1024 + half * 512 + r16 * 16;
#pragma unroll
    for (int s = 0; s < 14; s++) {
      if (s < 13 || w < 2) {
        X[s][0] = __builtin_amdgcn_raw_buffer_load_b128(rsW, aoff, s * 8192, AUX_SC1);
        X[s][1] = __builtin_amdgcn_raw_buffer_load_b128(rsW, aoff, s * 8192 + 4096, AUX_SC1);
        X[s][2] = __builtin_amdgcn_raw_buffer_load_b128(rsW, aoff + 256, s * 8192, AUX_SC1);
        X[s][3] = __builtin_amdgcn_raw_buffer_load_b128(rsW, aoff + 256, s * 8192 + 4096, AUX_SC1);
      } else {
        X[s][0] = X[s][1] = X[s][2] = X[s][3] = (u32x4){0u, 0u, 0u, 0u};
      }
      __builtin_amdgcn_sched_barrier(0);
    }
    const int tok = half * 64 + 32 * (w >> 1) + 2 * r16 + (w & 1);
    u32x2 res[5];
#pragma unroll
    for (int nt = 0; nt < 5; nt++) res[nt] = __builtin_amdgcn_raw_buffer_load_b64(rsW, WS_H + pm * H_PANEL + (ct * 20 + q) * 1024 + tok * 8, nt * 4096, AUX_SC1);
    __builtin_amdgcn_sched_barrier(0);
    f32x4 a2[4][5];
#pragma unroll
    for (int mt = 0; mt < 4; mt++)
#pragma unroll
      for (int nt = 0; nt < 5; nt++) a2[mt][nt] = (f32x4){0.f, 0.f, 0.f, 0.f};
#pragma unroll
    for (int s = 0; s < 14; s++) {
      u32x4 f[4];
#pragma unroll
      for (int hh = 0; hh < 2; hh++) {
        f[2 * hh] = (u32x4){X[s][2 * hh].x, X[s][2 * hh].y, X[s][2 * hh + 1].x, X[s][2 * hh + 1].y};
        f[2 * hh + 1] = (u32x4){X[s][2 * hh].z, X[s][2 * hh].w, X[s][2 * hh + 1].z, X[s][2 * hh + 1].w};
      }
      if (s == 6) {
        asm volatile("" : "+v"(a2[0][0]) :: "memory");
      }
#pragma unroll
      for (int nt = 0; nt < 5; nt++) {
        const u32x4 wf = s < 6 ? wr[s][nt] : tr_frag(w2p + (s < 8 ? s : s - 8) * SLOT2 + nt * 32, 16 * 160);
#pragma unroll
        for (int mt = 0; mt < 4; mt++) a2[mt][nt] = mfma_bf(wf, f[mt], a2[mt][nt]);
      }
    }
    STAMP(14);
#pragma unroll
    for (int mt = 0; mt < 4; mt++)
#pragma unroll
      for (int nt = 0; nt < 5; nt++) *(LAS f32x4*)(Q + (mt * 5 + nt) * 1024 + lane * 16) = a2[mt][nt];
    __syncthreads();
#pragma unroll
    for (int nt = 0; nt < 5; nt++) {
      LAS char* const p = lds + (w * 5 + nt) * 1024 + lane * 16;
      const f32x4 v = (*(LAS f32x4*)p + *(LAS f32x4*)(p + QSZ)) + (*(LAS f32x4*)(p + 2 * QSZ) + *(LAS f32x4*)(p + 3 * QSZ));
      const u32x2 o = {pk_bf16(lo16(res[nt].x) + v.x, hi16(res[nt].x) + v.y), pk_bf16(lo16(res[nt].y) + v.z, hi16(res[nt].y) + v.w)};
      *(u32x2*)(out + (pm * 128 + tok) * D + ct * 80 + nt * 16 + q * 4) = o;
    }
  }
  asm volatile(".Lmega_end:" :: "v"(pfv));

}

extern "C" void kernel_launch(void* const* d_in, const int* in_sizes, int n_in, void* d_out, int out_size, void* d_ws, size_t ws_size,
                              hipStream_t stream) {
  mega<<<256, 256, 0, stream>>>((const u16*)d_in[0], (const u16*)d_in[1], (const u16*)d_in[2], (const u16*)d_in[3], (const u16*)d_in[4],
                                (const u16*)d_in[5], (const u16*)d_in[6], (const u16*)d_in[7], (const u16*)d_in[8], (const u16*)d_in[10],
                                (const u16*)d_in[11], (u16*)d_out, (char*)d_ws);
}
```

```cpp
#include <hip/hip_runtime.h>
typedef unsigned short u16;

__global__ __launch_bounds__(256, 1) void mega(const u16* x, const u16* Are, const u16* Aim, const u16* Bre, const u16* Bim, const u16* Cre, const u16* Cim,
                                               const u16* logdt, const u16* Dskip, const u16* w1, const u16* w2, u16* out, char* ws) {
  __shared__ __attribute__((aligned(16))) char smem[163840];
  asm volatile(
    "s_load_dwordx16 s[4:19], s[0:1], 0\n"
    "s_load_dwordx8 s[20:27], s[0:1], 64\n"
    "s_load_dwordx2 s[28:29], s[0:1], 96\n"
    "s_getpc_b64 s[44:45]\n"
    ".Lk_pf:\n"
    "s_mov_b32 s46, .Lk_end-.Lk_pf\n"
    "s_mov_b32 s47, 0x20000\n"
    "s_lshr_b32 s50, s2, 3\n"
    "s_lshl_b32 s58, s50, 7\n"
    "v_lshlrev_b32 v2, 12, v0\n"
    "v_lshrrev_b32 v1, 6, v0\n"
    "buffer_load_dword v255, v2, s[44:47], s58 offen\n"
    "v_readfirstlane_b32 s48, v1\n"
    "v_and_b32 v0, 63, v0\n"
    "s_and_b32 s49, s2, 7\n"
    "v_mov_b32 v3, 0\n"
    "s_mul_i32 s54, s49, 20\n"
    "s_add_u32 s54, s54, s50\n"
    "s_lshl2_add_u32 s58, s54, s48\n"
    "s_lshl_b32 s59, s58, 8\n"
    "v_lshl_add_u32 v26, v0, 2, s59\n"
    "s_lshl_b32 s60, s58, 1\n"
    "s_and_b32 s60, s60, -4\n"
    "s_mul_i32 s96, s48, 96\n"
    "s_add_u32 s96, s96, s2\n"
    "s_sub_u32 s96, s96, 160\n"
    "s_mul_i32 s58, s96, 10240\n"
    "v_lshrrev_b32 v4, 3, v0\n"
    "v_and_b32_e64 v5, 7, v0\n"
    "v_mul_u32_u24 v6, 1280, v4\n"
    "v_lshl_add_u32 v6, v5, 4, v6\n"
    "v_add_u32_e64 v6, s58, v6\n"
    "s_lshr_b32 s57, s49, 1\n"
    "s_lshr_b32 s56, s50, 3\n"
    "s_lshl2_add_u32 s52, s57, s56\n"
    "s_and_b32 s57, s49, 1\n"
    "s_and_b32 s56, s50, 7\n"
    "s_lshl3_add_u32 s53, s57, s56\n"
    "s_and_b32 s56, s50, 3\n"
    "s_lshl2_add_u32 s63, s57, s56\n"
    "s_mul_i32 s55, s48, 40960\n"
    "s_mul_i32 s74, s57, 10\n"
    "s_mul_i32 s57, s52, 163840\n"
    "s_add_u32 s57, s57, s55\n"
    "s_add_u32 s57, s57, 131072\n"
    "s_add_u32 s56, s57, 40960\n"
    "s_lshl_b32 s64, s74, 11\n"
    "s_add_u32 s64, s64, s57\n"
    "s_add_u32 s65, s64, 4096\n"
    "s_add_u32 s66, s65, 4096\n"
    "s_add_u32 s67, s66, 4096\n"
    "s_add_u32 s68, s67, 4096\n"
    "s_add_u32 s69, s68, 4096\n"
    "s_cmp_eq_u32 s69, s56\n"
    "s_cselect_b32 s69, s57, s69\n"
    "s_add_u32 s70, s69, 4096\n"
    "s_add_u32 s71, s70, 4096\n"
    "s_add_u32 s72, s71, 4096\n"
    "s_add_u32 s73, s72, 4096\n"
    "s_waitcnt lgkmcnt(0)\n"
    "s_cmp_ge_u32 s50, 20\n"
    "s_cbranch_scc1 .Lk_nopar_1\n"
    "s_nop 0\n"
    "global_load_dword v20, v26, s[6:7]\n"
    "global_load_dword v21, v26, s[8:9]\n"
    "global_load_dword v22, v26, s[10:11]\n"
    "global_load_dword v23, v26, s[12:13]\n"
    "global_load_dword v24, v26, s[14:15]\n"
    "global_load_dword v25, v26, s[16:17]\n"
    "s_load_dword s30, s[18:19], s60\n"
    "s_load_dword s31, s[20:21], s60\n"
    "global_load_dword v18, v3, s[28:29] sc1\n"
    "s_branch .Lk_loaded_2\n"
    ".Lk_nopar_1:\n"
    "global_load_dword v18, v3, s[28:29] sc1\n"
    "s_cmp_ge_u32 s96, 256\n"
    "s_cbranch_scc1 .Lk_loaded_2\n"
    "global_load_dwordx4 v[20:23], v6, s[4:5] offset:0\n"
    "global_load_dwordx4 v[24:27], v6, s[4:5] offset:128\n"
    "global_load_dwordx4 v[28:31], v6, s[4:5] offset:256\n"
    "global_load_dwordx4 v[32:35], v6, s[4:5] offset:384\n"
    "global_load_dwordx4 v[36:39], v6, s[4:5] offset:512\n"
    "global_load_dwordx4 v[40:43], v6, s[4:5] offset:640\n"
    "global_load_dwordx4 v[44:47], v6, s[4:5] offset:768\n"
    "global_load_dwordx4 v[48:51], v6, s[4:5] offset:896\n"
    "global_load_dwordx4 v[52:55], v6, s[4:5] offset:1024\n"
    "global_load_dwordx4 v[56:59], v6, s[4:5] offset:1152\n"
    ".Lk_loaded_2:\n"
    "v_and_b32 v1, 15, v0\n"
    "v_lshrrev_b32 v2, 4, v0\n"
    "v_lshlrev_b32 v251, 4, v0\n"
    "s_mov_b64 s[32:33], s[28:29]\n"
    "s_mov_b32 s34, 12189696\n"
    "s_mov_b32 s35, 0x20000\n"
    "s_mov_b64 s[36:37], s[22:23]\n"
    "s_mov_b32 s38, 2211840\n"
    "s_mov_b32 s39, 0x20000\n"
    "s_mov_b64 s[44:45], s[4:5]\n"
    "s_mov_b32 s46, 2621440\n"
    "s_bfe_u32 s7, s50, 0x10002\n"
    "s_cmp_ge_u32 s50, 20\n"
    "s_cbranch_scc1 .Lk_helper\n"
    "s_add_u32 s58, s50, 0\n"
    "s_and_b32 s58, s58, 3\n"
    "s_lshl2_add_u32 s96, s48, s58\n"
    "s_add_u32 s58, s50, 1\n"
    "s_and_b32 s58, s58, 3\n"
    "s_lshl2_add_u32 s97, s48, s58\n"
    "s_add_u32 s58, s50, 2\n"
    "s_and_b32 s58, s58, 3\n"
    "s_lshl2_add_u32 s98, s48, s58\n"
    "s_add_u32 s58, s50, 3\n"
    "s_and_b32 s58, s58, 3\n"
    "s_lshl2_add_u32 s99, s48, s58\n"
    "v_mul_u32_u24 v14, 2560, v0\n"
    "s_lshl_b32 s58, s54, 3\n"
    "v_add_u32 v14, s58, v14\n"
    "s_mul_i32 s10, s96, 163840\n"
    "buffer_load_dwordx2 v[212:213], v14, s[44:47], s10 offen offset:0\n"
    "buffer_load_dwordx2 v[214:215], v14, s[44:47], s10 offen offset:1280\n"
    "s_mul_i32 s58, s63, 54\n"
    "s_mul_i32 s62, s48, 13\n"
    "s_min_u32 s3, s48, 1\n"
    "s_add_u32 s58, s58, s3\n"
    "s_add_u32 s58, s58, s62\n"
    "s_mul_i32 s62, s58, 5120\n"
    "s_add_u32 s62, s62, 9830400\n"
    "v_min_u32_e64 v15, 47, v0\n"
    "s_lshl_b32 s58, s49, 11\n"
    "s_add_u32 s58, s58, 4096\n"
    "v_lshl_add_u32 v15, v15, 4, s58\n"
    "v_mul_u32_u24 v42, 4682, v0\n"
    "s_movk_i32 s60, 3240\n"
    "v_add_u32 v43, 4096, v251\n"
    "v_add_u32 v28, 0, v42\n"
    "v_add_u32 v30, 299648, v42\n"
    "v_add_u32 v32, 599296, v42\n"
    "v_add_u32 v34, 898944, v42\n"
    "v_add_u32 v36, 1198592, v42\n"
    "v_add_u32 v38, 1498240, v42\n"
    "v_add_u32 v40, 1797888, v42\n"
    "v_add_u32 v29, 4682, v42\n"
    "v_add_u32 v31, 304330, v42\n"
    "v_add_u32 v33, 603978, v42\n"
    "v_add_u32 v35, 903626, v42\n"
    "v_add_u32 v37, 1203274, v42\n"
    "v_add_u32 v39, 1502922, v42\n"
    "v_add_u32 v41, 1802570, v42\n"
    "v_lshrrev_b32 v28, 16, v28\n"
    "v_lshrrev_b32 v30, 16, v30\n"
    "v_lshrrev_b32 v32, 16, v32\n"
    "v_lshrrev_b32 v34, 16, v34\n"
    "v_lshrrev_b32 v36, 16, v36\n"
    "v_lshrrev_b32 v38, 16, v38\n"
    "v_lshrrev_b32 v40, 16, v40\n"
    "v_lshrrev_b32 v29, 16, v29\n"
    "v_lshrrev_b32 v31, 16, v31\n"
    "v_lshrrev_b32 v33, 16, v33\n"
    "v_lshrrev_b32 v35, 16, v35\n"
    "v_lshrrev_b32 v37, 16, v37\n"
    "v_lshrrev_b32 v39, 16, v39\n"
    "v_lshrrev_b32 v41, 16, v41\n"
    "v_mad_u32_u24 v28, v28, s60, v251\n"
    "v_mad_u32_u24 v30, v30, s60, v251\n"
    "v_mad_u32_u24 v32, v32, s60, v251\n"
    "v_mad_u32_u24 v34, v34, s60, v251\n"
    "v_mad_u32_u24 v36, v36, s60, v43\n"
    "v_mad_u32_u24 v38, v38, s60, v43\n"
    "v_mad_u32_u24 v40, v40, s60, v43\n"
    "v_mad_i32_i24 v244, v29, -8, v28\n"
    "v_mad_i32_i24 v245, v31, -8, v30\n"
    "v_mad_i32_i24 v246, v33, -8, v32\n"
    "v_mad_i32_i24 v247, v35, -8, v34\n"
    "v_mad_i32_i24 v248, v37, -8, v36\n"
    "v_mad_i32_i24 v249, v39, -8, v38\n"
    "v_mad_i32_i24 v250, v41, -8, v40\n"
    "s_mul_i32 s60, s53, 216\n"
    "s_lshl2_add_u32 s12, s48, s74\n"
    "s_add_u32 s14, s12, 2\n"
    "s_add_u32 s16, s48, s74\n"
    "s_add_u32 s16, s16, 16\n"
    "s_sub_u32 s59, s12, 20\n"
    "s_min_u32 s12, s12, s59\n"
    "s_mul_i32 s12, s12, 110592\n"
    "s_add_u32 s12, s12, s60\n"
    "s_sub_u32 s59, s14, 20\n"
    "s_min_u32 s14, s14, s59\n"
    "s_mul_i32 s14, s14, 110592\n"
    "s_add_u32 s14, s14, s60\n"
    "s_sub_u32 s59, s16, 20\n"
    "s_min_u32 s16, s16, s59\n"
    "s_mul_i32 s16, s16, 110592\n"
    "s_add_u32 s16, s16, s60\n"
    "s_add_u32 s13, s12, 110592\n"
    "s_add_u32 s15, s14, 110592\n"
    "v_and_b32 v46, 31, v0\n"
    "v_lshrrev_b32_e64 v47, 5, v0\n"
    "v_lshl_add_u32 v199, v0, 3, s55\n"
    "v_lshlrev_b32_e64 v48, 4, v46\n"
    "v_bfe_u32 v28, v0, 2, 1\n"
    "v_lshlrev_b32_e64 v31, 6, v2\n"
    "v_lshl_add_u32 v37, v47, 3, v48\n"
    "v_mul_u32_u24 v38, 528, v46\n"
    "v_and_b32 v39, 1, v0\n"
    "v_lshrrev_b32 v29, 3, v0\n"
    "v_sub_u32_e64 v31, v31, v1\n"
    "s_add_u32 s59, s55, 30268\n"
    "v_lshl_add_u32 v38, v47, 4, v38\n"
    "v_lshrrev_b32_e64 v40, 1, v0\n"
    "v_mul_u32_u24 v28, 2368, v28\n"
    "v_add_u32 v31, 47, v31\n"
    "v_sub_u32 v196, s59, v37\n"
    "v_add_u32_e64 v200, s55, v38\n"
    "v_mul_u32_u24 v39, 2368, v39\n"
    "v_and_b32 v30, 3, v0\n"
    "v_and_b32 v36, 1, v31\n"
    "v_lshl_add_u32 v37, v47, 9, v48\n"
    "v_lshl_add_u32 v38, v2, 2, v1\n"
    "v_min_u32_e64 v40, 14, v40\n"
    "v_lshl_add_u32 v28, v29, 4, v28\n"
    "v_lshlrev_b32_e64 v6, 4, v36\n"
    "s_add_u32 s59, s55, 29748\n"
    "s_add_u32 s60, s55, 35840\n"
    "v_lshl_add_u32 v39, v40, 4, v39\n"
    "v_lshl_add_u32 v28, v30, 2, v28\n"
    "v_lshrrev_b32 v31, 1, v31\n"
    "v_sub_u32 v197, s59, v37\n"
    "v_lshl_add_u32 v5, v38, 4, s60\n"
    "v_add_u32_e64 v39, s55, v39\n"
    "v_add_u32 v28, 36080, v28\n"
    "v_and_b32 v36, 1, v31\n"
    "v_lshlrev_b32 v37, 7, v47\n"
    "v_mov_b64_e64 v[32:33], 0\n"
    "v_lshl_add_u32 v7, s96, 7, v28\n"
    "v_mul_u32_u24 v36, 2364, v36\n"
    "v_lshl_add_u32 v37, v46, 1, v37\n"
    "v_mov_b64_e64 v[34:35], 0\n"
    "v_lshl_add_u32 v9, s97, 7, v28\n"
    "v_lshl_add_u32 v31, v31, 2, v36\n"
    "s_add_u32 s59, s55, 33472\n"
    "v_mov_b32 v42, 0x3c00\n"
    "v_lshl_add_u32 v11, s98, 7, v28\n"
    "s_add_u32 s58, s55, 28676\n"
    "v_sub_u32_e64 v198, s59, v37\n"
    "v_mov_b32 v43, 0x3c00\n"
    "v_lshl_add_u32 v19, s99, 7, v28\n"
    "v_add_u32_e64 v4, s58, v31\n"
    "v_add_u32 v8, 81920, v7\n"
    "v_add_u32 v10, 81920, v9\n"
    "v_add_u32 v12, 81920, v11\n"
    "v_add_u32 v252, 81920, v19\n"
    "v_bfe_u32 v28, v0, 1, 2\n"
    "v_lshlrev_b32 v31, 2, v2\n"
    "s_and_b32 s58, s96, 1\n"
    "s_lshl_b32 s60, s54, 8\n"
    "v_lshlrev_b32 v30, 2, v28\n"
    "v_xor_b32 v31, v31, v1\n"
    "s_lshr_b32 s59, s96, 1\n"
    "s_add_u32 s60, s60, 131072\n"
    "v_xor_b32 v30, v30, v29\n"
    "v_lshlrev_b32_e64 v31, 4, v31\n"
    "s_lshl3_add_u32 s58, s59, s58\n"
    "s_mul_i32 s22, s96, 163840\n"
    "v_lshlrev_b32_e64 v30, 4, v30\n"
    "v_lshl_or_b32 v31, v2, 8, v31\n"
    "s_lshl_b32 s40, s58, 7\n"
    "s_add_u32 s22, s22, s60\n"
    "v_lshl_or_b32 v30, v28, 8, v30\n"
    "v_add_u32 v13, s55, v31\n"
    "s_and_b32 s58, s97, 1\n"
    "s_mul_i32 s23, s97, 163840\n"
    "v_and_b32_e64 v28, 1, v0\n"
    "v_mul_u32_u24 v31, 40960, v2\n"
    "s_lshr_b32 s59, s97, 1\n"
    "s_add_u32 s23, s23, s60\n"
    "v_lshl_or_b32 v253, v28, 3, v30\n"
    "v_lshl_add_u32 v210, v1, 4, v31\n"
    "s_lshl3_add_u32 s58, s59, s58\n"
    "s_mul_i32 s24, s98, 163840\n"
    "s_lshl_b32 s41, s58, 7\n"
    "s_add_u32 s24, s24, s60\n"
    "s_and_b32 s58, s98, 1\n"
    "s_mul_i32 s25, s99, 163840\n"
    "s_lshr_b32 s59, s98, 1\n"
    "s_add_u32 s25, s25, s60\n"
    "s_lshl3_add_u32 s58, s59, s58\n"
    "s_lshl_b32 s42, s58, 7\n"
    "s_and_b32 s58, s99, 1\n"
    "s_lshr_b32 s59, s99, 1\n"
    "s_lshl3_add_u32 s58, s59, s58\n"
    "s_lshl_b32 s43, s58, 7\n"
    "v_add_u32 v166, 2112, v199\n"
    "v_add_u32 v167, 4224, v199\n"
    "v_add_u32 v168, 6336, v199\n"
    "v_add_u32 v169, 8448, v199\n"
    "v_add_u32 v170, 10560, v199\n"
    "v_add_u32 v171, 12672, v199\n"
    "v_add_u32 v172, 14784, v199\n"
    "v_add_u32 v173, 16896, v199\n"
    "v_add_u32 v174, 19008, v199\n"
    "v_add_u32 v175, 21120, v199\n"
    "v_add_u32 v176, 22176, v199\n"
    "v_add_u32 v177, 23760, v199\n"
    "v_add_u32 v178, 25344, v199\n"
    "v_add_u32 v179, 26400, v199\n"
    "v_add_u32 v180, 27984, v199\n"
    "v_add_u32 v181, 29568, v199\n"
    "v_add_u32 v182, 31680, v199\n"
    "v_lshlrev_b32_e64 v28, 3, v2\n"
    "v_mov_b32 v29, 0x1ff0\n"
    "v_mov_b32 v30, 0xfe0\n"
    "v_lshlrev_b32 v29, v1, v29\n"
    "v_lshlrev_b32 v30, v1, v30\n"
    "v_lshrrev_b32 v29, v28, v29\n"
    "v_lshrrev_b32 v30, v28, v30\n"
    "v_bfe_u32 v202, v29, 0, 2\n"
    "v_bfe_u32 v203, v29, 2, 2\n"
    "v_bfe_u32 v204, v29, 4, 2\n"
    "v_bfe_u32 v205, v29, 6, 2\n"
    "v_bfe_u32 v206, v30, 0, 2\n"
    "v_bfe_u32 v207, v30, 2, 2\n"
    "v_bfe_u32 v208, v30, 4, 2\n"
    "v_bfe_u32 v209, v30, 6, 2\n"
    "v_lshl_or_b32 v202, v202, 15, v202\n"
    "v_lshl_or_b32 v203, v203, 15, v203\n"
    "v_lshl_or_b32 v204, v204, 15, v204\n"
    "v_lshl_or_b32 v205, v205, 15, v205\n"
    "v_lshl_or_b32 v206, v206, 15, v206\n"
    "v_lshl_or_b32 v207, v207, 15, v207\n"
    "v_lshl_or_b32 v208, v208, 15, v208\n"
    "v_lshl_or_b32 v209, v209, 15, v209\n"
    "v_and_b32 v202, 0x10001, v202\n"
    "v_and_b32 v203, 0x10001, v203\n"
    "v_and_b32 v204, 0x10001, v204\n"
    "v_and_b32 v205, 0x10001, v205\n"
    "v_and_b32 v206, 0x10001, v206\n"
    "v_and_b32 v207, 0x10001, v207\n"
    "v_and_b32 v208, 0x10001, v208\n"
    "v_and_b32 v209, 0x10001, v209\n"
    "v_mul_u32_u24 v202, 0x3c00, v202\n"
    "v_mul_u32_u24 v203, 0x3c00, v203\n"
    "v_mul_u32_u24 v204, 0x3c00, v204\n"
    "v_mul_u32_u24 v205, 0x3c00, v205\n"
    "v_mul_u32_u24 v206, 0x3c00, v206\n"
    "v_mul_u32_u24 v207, 0x3c00, v207\n"
    "v_mul_u32_u24 v208, 0x3c00, v208\n"
    "v_mul_u32_u24 v209, 0x3c00, v209\n"
    "ds_write_b128 v39, v[32:35] offset:35840\n"
    "ds_write_b64 v199, v[42:43]\n"
    "s_mul_i32 s10, s97, 163840\n"
    "buffer_load_dwordx2 v[216:217], v14, s[44:47], s10 offen offset:0\n"
    "s_bitcmp1_b32 s48, 0\n"
    "s_cselect_b32 s58, 0, 16\n"
    "s_waitcnt lgkmcnt(0)\n"
    "s_lshl_b32 s30, s30, s58\n"
    "s_and_b32 s30, s30, 0xffff0000\n"
    "s_lshl_b32 s31, s31, s58\n"
    "s_and_b32 s59, s31, 0xffff0000\n"
    "v_mov_b32 v29, 0x3fb8aa3b\n"
    "v_mul_f32 v28, s30, v29\n"
    "v_exp_f32 v28, v28\n"
    "s_nop 0\n"
    "v_cvt_pk_bf16_f32 v28, 0, v28\n"
    "s_waitcnt vmcnt(9)\n"
    "v_lshlrev_b32 v50, 16, v20\n"
    "v_and_b32 v56, 0xffff0000, v20\n"
    "s_waitcnt vmcnt(8)\n"
    "v_lshlrev_b32 v51, 16, v21\n"
    "v_and_b32 v57, 0xffff0000, v21\n"
    "s_waitcnt vmcnt(7)\n"
    "v_lshlrev_b32 v52, 16, v22\n"
    "v_and_b32 v58, 0xffff0000, v22\n"
    "s_waitcnt vmcnt(6)\n"
    "v_lshlrev_b32 v53, 16, v23\n"
    "v_and_b32 v59, 0xffff0000, v23\n"
    "s_waitcnt vmcnt(5)\n"
    "v_lshlrev_b32 v54, 16, v24\n"
    "v_and_b32 v60, 0xffff0000, v24\n"
    "s_waitcnt vmcnt(4)\n"
    "v_lshlrev_b32 v55, 16, v25\n"
    "v_and_b32 v61, 0xffff0000, v25\n"
    "v_pk_mul_f32 v[50:51], v[50:51], v[28:29] op_sel_hi:[1,0]\n"
    "v_pk_mul_f32 v[52:53], v[52:53], v[28:29] op_sel_hi:[1,0]\n"
    "v_pk_mul_f32 v[56:57], v[56:57], v[28:29] op_sel_hi:[1,0]\n"
    "v_pk_mul_f32 v[58:59], v[58:59], v[28:29] op_sel_hi:[1,0]\n"
    "v_mul_f32 v62, 0x3fb8aa3b, v50\n"
    "v_mul_f32_e64 v64, 0.15915494, v51\n"
    "v_pk_mul_f32 v[68:69], v[54:55], v[52:53] op_sel_hi:[0,1]\n"
    "v_mul_f32 v63, 0x3fb8aa3b, v56\n"
    "v_mul_f32_e64 v65, 0.15915494, v57\n"
    "v_pk_mul_f32 v[70:71], v[60:61], v[58:59] op_sel_hi:[0,1]\n"
    "v_fma_f32 v66, v51, 0.15915494, -v64\n"
    "v_pk_fma_f32 v[30:31], v[54:55], v[52:53], v[68:69] op_sel:[1,1,0] op_sel_hi:[1,0,1] neg_lo:[0,1,0]\n"
    "v_fma_f32 v67, v57, 0.15915494, -v65\n"
    "v_pk_fma_f32 v[32:33], v[60:61], v[58:59], v[70:71] op_sel:[1,1,0] op_sel_hi:[1,0,1] neg_lo:[0,1,0]\n"
    "v_fmac_f32 v66, 0x31dc9c88, v51\n"
    "v_add_f32 v72, abs(v30), abs(v31)\n"
    "v_fmac_f32 v67, 0x31dc9c88, v57\n"
    "v_add_f32 v73, abs(v32), abs(v33)\n"
    "v_add_f32_e64 v72, v72, v73\n"
    "v_mul_f32 v74, 0x41000000, v62\n"
    "v_mul_f32 v76, 0x41000000, v64\n"
    "v_mul_f32 v78, 0x41000000, v63\n"
    "v_mul_f32 v80, 0x41000000, v65\n"
    "v_exp_f32 v74, v74\n"
    "v_fract_f32 v76, v76\n"
    "v_exp_f32 v78, v78\n"
    "v_fract_f32 v80, v80\n"
    "v_add_f32_dpp v72, v72, v72 row_ror:1 row_mask:0xf bank_mask:0xf\n"
    "v_fmac_f32 v76, 0x41000000, v66\n"
    "v_fmac_f32 v80, 0x41000000, v67\n"
    "v_cos_f32 v38, v76\n"
    "v_sin_f32 v39, v76\n"
    "v_cos_f32 v40, v80\n"
    "v_sin_f32 v41, v80\n"
    "v_add_f32_dpp v72, v72, v72 row_ror:2 row_mask:0xf bank_mask:0xf\n"
    "v_pk_mul_f32 v[38:39], v[38:39], v[74:75] op_sel_hi:[1,0]\n"
    "v_pk_mul_f32 v[40:41], v[40:41], v[78:79] op_sel_hi:[1,0]\n"
    "v_pk_mul_f32 v[68:69], v[38:39], v[38:39] op_sel_hi:[0,1]\n"
    "v_pk_mul_f32 v[70:71], v[40:41], v[40:41] op_sel_hi:[0,1]\n"
    "v_pk_fma_f32 v[98:99], v[38:39], v[38:39], v[68:69] op_sel:[1,1,0] op_sel_hi:[1,0,1] neg_lo:[0,1,0]\n"
    "v_pk_fma_f32 v[100:101], v[40:41], v[40:41], v[70:71] op_sel:[1,1,0] op_sel_hi:[1,0,1] neg_lo:[0,1,0]\n"
    "v_mul_f32 v82, 0x42000000, v62\n"
    "v_mul_f32 v84, 0x42000000, v64\n"
    "v_mul_f32 v86, 0x42000000, v63\n"
    "v_mul_f32 v88, 0x42000000, v65\n"
    "v_exp_f32 v82, v82\n"
    "v_fract_f32 v84, v84\n"
    "v_exp_f32 v86, v86\n"
    "v_fract_f32 v88, v88\n"
    "v_add_f32_dpp v72, v72, v72 row_ror:4 row_mask:0xf bank_mask:0xf\n"
    "v_fmac_f32 v84, 0x42000000, v66\n"
    "v_fmac_f32 v88, 0x42000000, v67\n"
    "v_cos_f32 v42, v84\n"
    "v_sin_f32 v43, v84\n"
    "v_cos_f32 v44, v88\n"
    "v_sin_f32 v45, v88\n"
    "v_add_f32_dpp v72, v72, v72 row_ror:8 row_mask:0xf bank_mask:0xf\n"
    "v_pk_mul_f32 v[42:43], v[42:43], v[82:83] op_sel_hi:[1,0]\n"
    "v_pk_mul_f32 v[44:45], v[44:45], v[86:87] op_sel_hi:[1,0]\n"
    "v_exp_f32 v74, v62\n"
    "v_fract_f32 v76, v64\n"
    "v_exp_f32 v78, v63\n"
    "v_fract_f32 v80, v65\n"
    "v_add_f32_dpp v72, v72, v72 row_bcast:15 row_mask:0xa bank_mask:0xf\n"
    "v_add_f32 v76, v76, v66\n"
    "v_add_f32 v80, v80, v67\n"
    "v_cos_f32 v34, v76\n"
    "v_sin_f32 v35, v76\n"
    "v_cos_f32 v36, v80\n"
    "v_sin_f32 v37, v80\n"
    "v_add_f32_dpp v72, v72, v72 row_bcast:31 row_mask:0xc bank_mask:0xf\n"
    "v_pk_mul_f32 v[34:35], v[34:35], v[74:75] op_sel_hi:[1,0]\n"
    "v_pk_mul_f32 v[36:37], v[36:37], v[78:79] op_sel_hi:[1,0]\n"
    "v_mul_f32 v82, 0x43800000, v62\n"
    "v_mul_f32 v84, 0x43800000, v64\n"
    "v_mul_f32 v86, 0x43800000, v63\n"
    "v_mul_f32 v88, 0x43800000, v65\n"
    "v_exp_f32 v82, v82\n"
    "v_fract_f32 v84, v84\n"
    "v_exp_f32 v86, v86\n"
    "v_fract_f32 v88, v88\n"
    "s_and_b32 s60, s59, 0x7fffffff\n"
    "v_add_f32_e64 v72, s60, v72\n"
    "v_fmac_f32 v84, 0x43800000, v66\n"
    "v_fmac_f32 v88, 0x43800000, v67\n"
    "v_readlane_b32 s58, v72, 63\n"
    "v_cos_f32 v46, v84\n"
    "v_sin_f32 v47, v84\n"
    "v_cos_f32 v48, v88\n"
    "v_sin_f32 v49, v88\n"
    "s_lshr_b32 s58, s58, 23\n"
    "s_max_u32 s58, s58, 14\n"
    "s_sub_u32 s30, 267, s58\n"
    "s_lshl_b32 s30, s30, 23\n"
    "v_pk_mul_f32 v[46:47], v[46:47], v[82:83] op_sel_hi:[1,0]\n"
    "v_pk_mul_f32 v[48:49], v[48:49], v[86:87] op_sel_hi:[1,0]\n"
    "v_pk_mul_f32 v[106:107], v[30:31], s[30:31] op_sel_hi:[1,0] neg_hi:[1,0]\n"
    "v_pk_mul_f32 v[108:109], v[32:33], s[30:31] op_sel_hi:[1,0] neg_hi:[1,0]\n"
    "s_sub_u32 s58, s58, 13\n"
    "s_lshl_b32 s58, s58, 23\n"
    "v_mov_b32 v28, s58\n"
    "s_lshl_b32 s58, s48, 2\n"
    "v_mov_b32 v29, s58\n"
    "ds_write_b32 v29, v28 offset:40512\n"
    "v_pk_mul_f32 v[146:147], v[98:99], v[38:39] op_sel_hi:[0,1]\n"
    "v_pk_mul_f32 v[148:149], v[100:101], v[40:41] op_sel_hi:[0,1]\n"
    "v_pk_mul_f32 v[154:155], v[106:107], v[34:35] op_sel_hi:[0,1] neg_hi:[0,1]\n"
    "v_pk_mul_f32 v[156:157], v[108:109], v[36:37] op_sel_hi:[0,1] neg_hi:[0,1]\n"
    "v_pk_fma_f32 v[102:103], v[98:99], v[38:39], v[146:147] op_sel:[1,1,0] op_sel_hi:[1,0,1] neg_lo:[0,1,0]\n"
    "v_pk_fma_f32 v[104:105], v[100:101], v[40:41], v[148:149] op_sel:[1,1,0] op_sel_hi:[1,0,1] neg_lo:[0,1,0]\n"
    "v_pk_fma_f32 v[110:111], v[106:107], v[34:35], v[154:155] op_sel:[1,1,0] op_sel_hi:[1,0,1]\n"
    "v_pk_fma_f32 v[112:113], v[108:109], v[36:37], v[156:157] op_sel:[1,1,0] op_sel_hi:[1,0,1]\n"
    "v_cvt_pk_f16_f32 v58, v38, v39\n"
    "v_cvt_pk_f16_f32 v59, v40, v41\n"
    "v_cvt_pk_f16_f32 v60, v98, v99\n"
    "v_cvt_pk_f16_f32 v61, v100, v101\n"
    "v_cvt_pk_f16_f32 v62, v102, v103\n"
    "v_cvt_pk_f16_f32 v63, v104, v105\n"
    "v_cvt_pk_f16_f32 v64, v106, v107\n"
    "v_cvt_pk_f16_f32 v65, v108, v109\n"
    "v_cvt_pk_f16_f32 v66, v110, v111\n"
    "v_cvt_pk_f16_f32 v67, v112, v113\n"
    "ds_write_b64 v199, v[58:59] offset:528\n"
    "ds_write2_b64 v199, v[60:61], v[62:63] offset0:132 offset1:198\n"
    "ds_write2_b64 v173, v[64:65], v[66:67] offset0:0 offset1:66\n"
    "v_pk_mul_f32 v[138:139], v[106:107], v[38:39] op_sel_hi:[0,1] neg_hi:[0,1]\n"
    "v_pk_mul_f32 v[140:141], v[108:109], v[40:41] op_sel_hi:[0,1] neg_hi:[0,1]\n"
    "v_pk_mul_f32 v[142:143], v[106:107], v[46:47] op_sel_hi:[0,1] neg_hi:[0,1]\n"
    "v_pk_mul_f32 v[144:145], v[108:109], v[48:49] op_sel_hi:[0,1] neg_hi:[0,1]\n"
    "v_pk_mul_f32 v[150:151], v[38:39], v[42:43] op_sel_hi:[0,1]\n"
    "v_pk_mul_f32 v[152:153], v[40:41], v[44:45] op_sel_hi:[0,1]\n"
    "v_pk_mul_f32 v[154:155], v[110:111], v[34:35] op_sel_hi:[0,1] neg_hi:[0,1]\n"
    "v_pk_mul_f32 v[156:157], v[112:113], v[36:37] op_sel_hi:[0,1] neg_hi:[0,1]\n"
    "v_pk_fma_f32 v[50:51], v[106:107], v[38:39], v[138:139] op_sel:[1,1,0] op_sel_hi:[1,0,1]\n"
    "v_pk_fma_f32 v[52:53], v[108:109], v[40:41], v[140:141] op_sel:[1,1,0] op_sel_hi:[1,0,1]\n"
    "v_pk_fma_f32 v[54:55], v[106:107], v[46:47], v[142:143] op_sel:[1,1,0] op_sel_hi:[1,0,1]\n"
    "v_pk_fma_f32 v[56:57], v[108:109], v[48:49], v[144:145] op_sel:[1,1,0] op_sel_hi:[1,0,1]\n"
    "v_pk_fma_f32 v[94:95], v[38:39], v[42:43], v[150:151] op_sel:[1,1,0] op_sel_hi:[1,0,1] neg_lo:[0,1,0]\n"
    "v_pk_fma_f32 v[96:97], v[40:41], v[44:45], v[152:153] op_sel:[1,1,0] op_sel_hi:[1,0,1] neg_lo:[0,1,0]\n"
    "v_pk_fma_f32 v[114:115], v[110:111], v[34:35], v[154:155] op_sel:[1,1,0] op_sel_hi:[1,0,1]\n"
    "v_pk_fma_f32 v[116:117], v[112:113], v[36:37], v[156:157] op_sel:[1,1,0] op_sel_hi:[1,0,1]\n"
    "v_cvt_pk_f16_f32 v68, v54, v55\n"
    "v_cvt_pk_f16_f32 v69, v56, v57\n"
    "v_cvt_pk_f16_f32 v70, v42, v43\n"
    "v_cvt_pk_f16_f32 v71, v44, v45\n"
    "v_cvt_pk_f16_f32 v72, v94, v95\n"
    "v_cvt_pk_f16_f32 v73, v96, v97\n"
    "v_cvt_pk_f16_f32 v74, v114, v115\n"
    "v_cvt_pk_f16_f32 v75, v116, v117\n"
    "ds_write2_b64 v166, v[70:71], v[72:73] offset0:0 offset1:66\n"
    "v_pk_mul_f32 v[138:139], v[98:99], v[42:43] op_sel_hi:[0,1]\n"
    "v_pk_mul_f32 v[140:141], v[100:101], v[44:45] op_sel_hi:[0,1]\n"
    "v_pk_mul_f32 v[142:143], v[102:103], v[42:43] op_sel_hi:[0,1]\n"
    "v_pk_mul_f32 v[144:145], v[104:105], v[44:45] op_sel_hi:[0,1]\n"
    "v_pk_mul_f32 v[146:147], v[114:115], v[34:35] op_sel_hi:[0,1] neg_hi:[0,1]\n"
    "v_pk_mul_f32 v[148:149], v[116:117], v[36:37] op_sel_hi:[0,1] neg_hi:[0,1]\n"
    "v_pk_mul_f32 v[150:151], v[54:55], v[46:47] op_sel_hi:[0,1] neg_hi:[0,1]\n"
    "v_pk_mul_f32 v[152:153], v[56:57], v[48:49] op_sel_hi:[0,1] neg_hi:[0,1]\n"
    "v_pk_fma_f32 v[98:99], v[98:99], v[42:43], v[138:139] op_sel:[1,1,0] op_sel_hi:[1,0,1] neg_lo:[0,1,0]\n"
    "v_pk_fma_f32 v[100:101], v[100:101], v[44:45], v[140:141] op_sel:[1,1,0] op_sel_hi:[1,0,1] neg_lo:[0,1,0]\n"
    "v_pk_fma_f32 v[102:103], v[102:103], v[42:43], v[142:143] op_sel:[1,1,0] op_sel_hi:[1,0,1] neg_lo:[0,1,0]\n"
    "v_pk_fma_f32 v[104:105], v[104:105], v[44:45], v[144:145] op_sel:[1,1,0] op_sel_hi:[1,0,1] neg_lo:[0,1,0]\n"
    "v_pk_fma_f32 v[118:119], v[114:115], v[34:35], v[146:147] op_sel:[1,1,0] op_sel_hi:[1,0,1]\n"
    "v_pk_fma_f32 v[120:121], v[116:117], v[36:37], v[148:149] op_sel:[1,1,0] op_sel_hi:[1,0,1]\n"
    "v_pk_fma_f32 v[54:55], v[54:55], v[46:47], v[150:151] op_sel:[1,1,0] op_sel_hi:[1,0,1]\n"
    "v_pk_fma_f32 v[56:57], v[56:57], v[48:49], v[152:153] op_sel:[1,1,0] op_sel_hi:[1,0,1]\n"
    "v_cvt_pk_f16_f32 v76, v98, v99\n"
    "v_cvt_pk_f16_f32 v77, v100, v101\n"
    "v_cvt_pk_f16_f32 v78, v102, v103\n"
    "v_cvt_pk_f16_f32 v79, v104, v105\n"
    "v_cvt_pk_f16_f32 v80, v118, v119\n"
    "v_cvt_pk_f16_f32 v81, v120, v121\n"
    "v_cvt_pk_f16_f32 v82, v54, v55\n"
    "v_cvt_pk_f16_f32 v83, v56, v57\n"
    "ds_write2_b64 v166, v[76:77], v[78:79] offset0:132 offset1:198\n"
    "ds_write2_b64 v173, v[74:75], v[80:81] offset0:132 offset1:198\n"
    "buffer_load_dwordx2 v[218:219], v14, s[44:47], s10 offen offset:1280\n"
    "v_pk_mul_f32 v[138:139], v[42:43], v[42:43] op_sel_hi:[0,1]\n"
    "v_pk_mul_f32 v[140:141], v[44:45], v[44:45] op_sel_hi:[0,1]\n"
    "v_pk_mul_f32 v[142:143], v[94:95], v[42:43] op_sel_hi:[0,1]\n"
    "v_pk_mul_f32 v[144:145], v[96:97], v[44:45] op_sel_hi:[0,1]\n"
    "v_pk_mul_f32 v[146:147], v[118:119], v[34:35] op_sel_hi:[0,1] neg_hi:[0,1]\n"
    "v_pk_mul_f32 v[148:149], v[120:121], v[36:37] op_sel_hi:[0,1] neg_hi:[0,1]\n"
    "v_pk_mul_f32 v[150:151], v[114:115], v[46:47] op_sel_hi:[0,1] neg_hi:[0,1]\n"
    "v_pk_mul_f32 v[152:153], v[116:117], v[48:49] op_sel_hi:[0,1] neg_hi:[0,1]\n"
    "v_pk_fma_f32 v[90:91], v[42:43], v[42:43], v[138:139] op_sel:[1,1,0] op_sel_hi:[1,0,1] neg_lo:[0,1,0]\n"
    "v_pk_fma_f32 v[92:93], v[44:45], v[44:45], v[140:141] op_sel:[1,1,0] op_sel_hi:[1,0,1] neg_lo:[0,1,0]\n"
    "v_pk_fma_f32 v[94:95], v[94:95], v[42:43], v[142:143] op_sel:[1,1,0] op_sel_hi:[1,0,1] neg_lo:[0,1,0]\n"
    "v_pk_fma_f32 v[96:97], v[96:97], v[44:45], v[144:145] op_sel:[1,1,0] op_sel_hi:[1,0,1] neg_lo:[0,1,0]\n"
    "v_pk_fma_f32 v[122:123], v[118:119], v[34:35], v[146:147] op_sel:[1,1,0] op_sel_hi:[1,0,1]\n"
    "v_pk_fma_f32 v[124:125], v[120:121], v[36:37], v[148:149] op_sel:[1,1,0] op_sel_hi:[1,0,1]\n"
    "v_pk_fma_f32 v[114:115], v[114:115], v[46:47], v[150:151] op_sel:[1,1,0] op_sel_hi:[1,0,1]\n"
    "v_pk_fma_f32 v[116:117], v[116:117], v[48:49], v[152:153] op_sel:[1,1,0] op_sel_hi:[1,0,1]\n"
    "v_cvt_pk_f16_f32 v84, v90, v91\n"
    "v_cvt_pk_f16_f32 v85, v92, v93\n"
    "v_cvt_pk_f16_f32 v86, v94, v95\n"
    "v_cvt_pk_f16_f32 v87, v96, v97\n"
    "v_cvt_pk_f16_f32 v88, v122, v123\n"
    "v_cvt_pk_f16_f32 v89, v124, v125\n"
    "v_cvt_pk_f16_f32 v58, v114, v115\n"
    "v_cvt_pk_f16_f32 v59, v116, v117\n"
    "ds_write2_b64 v167, v[84:85], v[86:87] offset0:0 offset1:66\n"
    "ds_write2_b64 v175, v[68:69], v[58:59] offset0:0 offset1:66\n"
    "v_pk_mul_f32 v[138:139], v[98:99], v[42:43] op_sel_hi:[0,1]\n"
    "v_pk_mul_f32 v[140:141], v[100:101], v[44:45] op_sel_hi:[0,1]\n"
    "v_pk_mul_f32 v[142:143], v[102:103], v[42:43] op_sel_hi:[0,1]\n"
    "v_pk_mul_f32 v[144:145], v[104:105], v[44:45] op_sel_hi:[0,1]\n"
    "v_pk_mul_f32 v[146:147], v[122:123], v[34:35] op_sel_hi:[0,1] neg_hi:[0,1]\n"
    "v_pk_mul_f32 v[148:149], v[124:125], v[36:37] op_sel_hi:[0,1] neg_hi:[0,1]\n"
    "v_pk_mul_f32 v[150:151], v[50:51], v[46:47] op_sel_hi:[0,1] neg_hi:[0,1]\n"
    "v_pk_mul_f32 v[152:153], v[52:53], v[48:49] op_sel_hi:[0,1] neg_hi:[0,1]\n"
    "v_pk_fma_f32 v[98:99], v[98:99], v[42:43], v[138:139] op_sel:[1,1,0] op_sel_hi:[1,0,1] neg_lo:[0,1,0]\n"
    "v_pk_fma_f32 v[100:101], v[100:101], v[44:45], v[140:141] op_sel:[1,1,0] op_sel_hi:[1,0,1] neg_lo:[0,1,0]\n"
    "v_pk_fma_f32 v[102:103], v[102:103], v[42:43], v[142:143] op_sel:[1,1,0] op_sel_hi:[1,0,1] neg_lo:[0,1,0]\n"
    "v_pk_fma_f32 v[104:105], v[104:105], v[44:45], v[144:145] op_sel:[1,1,0] op_sel_hi:[1,0,1] neg_lo:[0,1,0]\n"
    "v_pk_fma_f32 v[126:127], v[122:123], v[34:35], v[146:147] op_sel:[1,1,0] op_sel_hi:[1,0,1]\n"
    "v_pk_fma_f32 v[128:129], v[124:125], v[36:37], v[148:149] op_sel:[1,1,0] op_sel_hi:[1,0,1]\n"
    "v_pk_fma_f32 v[50:51], v[50:51], v[46:47], v[150:151] op_sel:[1,1,0] op_sel_hi:[1,0,1]\n"
    "v_pk_fma_f32 v[52:53], v[52:53], v[48:49], v[152:153] op_sel:[1,1,0] op_sel_hi:[1,0,1]\n"
    "v_cvt_pk_f16_f32 v60, v98, v99\n"
    "v_cvt_pk_f16_f32 v61, v100, v101\n"
    "v_cvt_pk_f16_f32 v62, v102, v103\n"
    "v_cvt_pk_f16_f32 v63, v104, v105\n"
    "v_cvt_pk_f16_f32 v64, v126, v127\n"
    "v_cvt_pk_f16_f32 v65, v128, v129\n"
    "v_cvt_pk_f16_f32 v66, v50, v51\n"
    "v_cvt_pk_f16_f32 v67, v52, v53\n"
    "ds_write2_b64 v167, v[60:61], v[62:63] offset0:132 offset1:198\n"
    "ds_write2_b64 v174, v[88:89], v[64:65] offset0:0 offset1:66\n"
    "v_pk_mul_f32 v[138:139], v[90:91], v[42:43] op_sel_hi:[0,1]\n"
    "v_pk_mul_f32 v[140:141], v[92:93], v[44:45] op_sel_hi:[0,1]\n"
    "v_pk_mul_f32 v[142:143], v[94:95], v[42:43] op_sel_hi:[0,1]\n"
    "v_pk_mul_f32 v[144:145], v[96:97], v[44:45] op_sel_hi:[0,1]\n"
    "v_pk_mul_f32 v[146:147], v[126:127], v[34:35] op_sel_hi:[0,1] neg_hi:[0,1]\n"
    "v_pk_mul_f32 v[148:149], v[128:129], v[36:37] op_sel_hi:[0,1] neg_hi:[0,1]\n"
    "v_pk_mul_f32 v[150:151], v[122:123], v[46:47] op_sel_hi:[0,1] neg_hi:[0,1]\n"
    "v_pk_mul_f32 v[152:153], v[124:125], v[48:49] op_sel_hi:[0,1] neg_hi:[0,1]\n"
    "v_pk_fma_f32 v[90:91], v[90:91], v[42:43], v[138:139] op_sel:[1,1,0] op_sel_hi:[1,0,1] neg_lo:[0,1,0]\n"
    "v_pk_fma_f32 v[92:93], v[92:93], v[44:45], v[140:141] op_sel:[1,1,0] op_sel_hi:[1,0,1] neg_lo:[0,1,0]\n"
    "v_pk_fma_f32 v[94:95], v[94:95], v[42:43], v[142:143] op_sel:[1,1,0] op_sel_hi:[1,0,1] neg_lo:[0,1,0]\n"
    "v_pk_fma_f32 v[96:97], v[96:97], v[44:45], v[144:145] op_sel:[1,1,0] op_sel_hi:[1,0,1] neg_lo:[0,1,0]\n"
    "v_pk_fma_f32 v[130:131], v[126:127], v[34:35], v[146:147] op_sel:[1,1,0] op_sel_hi:[1,0,1]\n"
    "v_pk_fma_f32 v[132:133], v[128:129], v[36:37], v[148:149] op_sel:[1,1,0] op_sel_hi:[1,0,1]\n"
    "v_pk_fma_f32 v[122:123], v[122:123], v[46:47], v[150:151] op_sel:[1,1,0] op_sel_hi:[1,0,1]\n"
    "v_pk_fma_f32 v[124:125], v[124:125], v[48:49], v[152:153] op_sel:[1,1,0] op_sel_hi:[1,0,1]\n"
    "v_cvt_pk_f16_f32 v68, v90, v91\n"
    "v_cvt_pk_f16_f32 v69, v92, v93\n"
    "v_cvt_pk_f16_f32 v70, v94, v95\n"
    "v_cvt_pk_f16_f32 v71, v96, v97\n"
    "v_cvt_pk_f16_f32 v72, v130, v131\n"
    "v_cvt_pk_f16_f32 v73, v132, v133\n"
    "v_cvt_pk_f16_f32 v74, v122, v123\n"
    "v_cvt_pk_f16_f32 v75, v124, v125\n"
    "ds_write2_b64 v168, v[68:69], v[70:71] offset0:0 offset1:66\n"
    "ds_write2_b64 v176, v[74:75], v[74:75] offset0:0 offset1:132\n"
    "s_mul_i32 s10, s98, 163840\n"
    "buffer_load_dwordx2 v[220:221], v14, s[44:47], s10 offen offset:0\n"
    "v_pk_mul_f32 v[138:139], v[98:99], v[42:43] op_sel_hi:[0,1]\n"
    "v_pk_mul_f32 v[140:141], v[100:101], v[44:45] op_sel_hi:[0,1]\n"
    "v_pk_mul_f32 v[142:143], v[102:103], v[42:43] op_sel_hi:[0,1]\n"
    "v_pk_mul_f32 v[144:145], v[104:105], v[44:45] op_sel_hi:[0,1]\n"
    "v_pk_mul_f32 v[146:147], v[130:131], v[34:35] op_sel_hi:[0,1] neg_hi:[0,1]\n"
    "v_pk_mul_f32 v[148:149], v[132:133], v[36:37] op_sel_hi:[0,1] neg_hi:[0,1]\n"
    "v_pk_mul_f32 v[150:151], v[54:55], v[46:47] op_sel_hi:[0,1] neg_hi:[0,1]\n"
    "v_pk_mul_f32 v[152:153], v[56:57], v[48:49] op_sel_hi:[0,1] neg_hi:[0,1]\n"
    "v_pk_fma_f32 v[98:99], v[98:99], v[42:43], v[138:139] op_sel:[1,1,0] op_sel_hi:[1,0,1] neg_lo:[0,1,0]\n"
    "v_pk_fma_f32 v[100:101], v[100:101], v[44:45], v[140:141] op_sel:[1,1,0] op_sel_hi:[1,0,1] neg_lo:[0,1,0]\n"
    "v_pk_fma_f32 v[102:103], v[102:103], v[42:43], v[142:143] op_sel:[1,1,0] op_sel_hi:[1,0,1] neg_lo:[0,1,0]\n"
    "v_pk_fma_f32 v[104:105], v[104:105], v[44:45], v[144:145] op_sel:[1,1,0] op_sel_hi:[1,0,1] neg_lo:[0,1,0]\n"
    "v_pk_fma_f32 v[134:135], v[130:131], v[34:35], v[146:147] op_sel:[1,1,0] op_sel_hi:[1,0,1]\n"
    "v_pk_fma_f32 v[136:137], v[132:133], v[36:37], v[148:149] op_sel:[1,1,0] op_sel_hi:[1,0,1]\n"
    "v_pk_fma_f32 v[54:55], v[54:55], v[46:47], v[150:151] op_sel:[1,1,0] op_sel_hi:[1,0,1]\n"
    "v_pk_fma_f32 v[56:57], v[56:57], v[48:49], v[152:153] op_sel:[1,1,0] op_sel_hi:[1,0,1]\n"
    "v_cvt_pk_f16_f32 v76, v98, v99\n"
    "v_cvt_pk_f16_f32 v77, v100, v101\n"
    "v_cvt_pk_f16_f32 v78, v102, v103\n"
    "v_cvt_pk_f16_f32 v79, v104, v105\n"
    "v_cvt_pk_f16_f32 v80, v134, v135\n"
    "v_cvt_pk_f16_f32 v81, v136, v137\n"
    "v_cvt_pk_f16_f32 v84, v54, v55\n"
    "v_cvt_pk_f16_f32 v85, v56, v57\n"
    "ds_write2_b64 v168, v[76:77], v[78:79] offset0:132 offset1:198\n"
    "ds_write2_b64 v174, v[72:73], v[80:81] offset0:132 offset1:198\n"
    "v_pk_mul_f32 v[138:139], v[90:91], v[42:43] op_sel_hi:[0,1]\n"
    "v_pk_mul_f32 v[140:141], v[92:93], v[44:45] op_sel_hi:[0,1]\n"
    "v_pk_mul_f32 v[142:143], v[94:95], v[42:43] op_sel_hi:[0,1]\n"
    "v_pk_mul_f32 v[144:145], v[96:97], v[44:45] op_sel_hi:[0,1]\n"
    "v_pk_mul_f32 v[146:147], v[130:131], v[46:47] op_sel_hi:[0,1] neg_hi:[0,1]\n"
    "v_pk_mul_f32 v[148:149], v[132:133], v[48:49] op_sel_hi:[0,1] neg_hi:[0,1]\n"
    "v_pk_mul_f32 v[150:151], v[54:55], v[46:47] op_sel_hi:[0,1] neg_hi:[0,1]\n"
    "v_pk_mul_f32 v[152:153], v[56:57], v[48:49] op_sel_hi:[0,1] neg_hi:[0,1]\n"
    "v_pk_fma_f32 v[90:91], v[90:91], v[42:43], v[138:139] op_sel:[1,1,0] op_sel_hi:[1,0,1] neg_lo:[0,1,0]\n"
    "v_pk_fma_f32 v[92:93], v[92:93], v[44:45], v[140:141] op_sel:[1,1,0] op_sel_hi:[1,0,1] neg_lo:[0,1,0]\n"
    "v_pk_fma_f32 v[94:95], v[94:95], v[42:43], v[142:143] op_sel:[1,1,0] op_sel_hi:[1,0,1] neg_lo:[0,1,0]\n"
    "v_pk_fma_f32 v[96:97], v[96:97], v[44:45], v[144:145] op_sel:[1,1,0] op_sel_hi:[1,0,1] neg_lo:[0,1,0]\n"
    "v_pk_fma_f32 v[130:131], v[130:131], v[46:47], v[146:147] op_sel:[1,1,0] op_sel_hi:[1,0,1]\n"
    "v_pk_fma_f32 v[132:133], v[132:133], v[48:49], v[148:149] op_sel:[1,1,0] op_sel_hi:[1,0,1]\n"
    "v_pk_fma_f32 v[54:55], v[54:55], v[46:47], v[150:151] op_sel:[1,1,0] op_sel_hi:[1,0,1]\n"
    "v_pk_fma_f32 v[56:57], v[56:57], v[48:49], v[152:153] op_sel:[1,1,0] op_sel_hi:[1,0,1]\n"
    "v_cvt_pk_f16_f32 v86, v90, v91\n"
    "v_cvt_pk_f16_f32 v87, v92, v93\n"
    "v_cvt_pk_f16_f32 v88, v94, v95\n"
    "v_cvt_pk_f16_f32 v89, v96, v97\n"
    "v_cvt_pk_f16_f32 v58, v130, v131\n"
    "v_cvt_pk_f16_f32 v59, v132, v133\n"
    "v_cvt_pk_f16_f32 v60, v54, v55\n"
    "v_cvt_pk_f16_f32 v61, v56, v57\n"
    "ds_write2_b64 v169, v[86:87], v[88:89] offset0:0 offset1:66\n"
    "ds_write2_b64 v177, v[58:59], v[66:67] offset0:0 offset1:66\n"
    "v_pk_mul_f32 v[138:139], v[98:99], v[42:43] op_sel_hi:[0,1]\n"
    "v_pk_mul_f32 v[140:141], v[100:101], v[44:45] op_sel_hi:[0,1]\n"
    "v_pk_mul_f32 v[142:143], v[102:103], v[42:43] op_sel_hi:[0,1]\n"
    "v_pk_mul_f32 v[144:145], v[104:105], v[44:45] op_sel_hi:[0,1]\n"
    "v_pk_mul_f32 v[146:147], v[122:123], v[46:47] op_sel_hi:[0,1] neg_hi:[0,1]\n"
    "v_pk_mul_f32 v[148:149], v[124:125], v[48:49] op_sel_hi:[0,1] neg_hi:[0,1]\n"
    "v_pk_mul_f32 v[150:151], v[54:55], v[46:47] op_sel_hi:[0,1] neg_hi:[0,1]\n"
    "v_pk_mul_f32 v[152:153], v[56:57], v[48:49] op_sel_hi:[0,1] neg_hi:[0,1]\n"
    "v_pk_fma_f32 v[98:99], v[98:99], v[42:43], v[138:139] op_sel:[1,1,0] op_sel_hi:[1,0,1] neg_lo:[0,1,0]\n"
    "v_pk_fma_f32 v[100:101], v[100:101], v[44:45], v[140:141] op_sel:[1,1,0] op_sel_hi:[1,0,1] neg_lo:[0,1,0]\n"
    "v_pk_fma_f32 v[102:103], v[102:103], v[42:43], v[142:143] op_sel:[1,1,0] op_sel_hi:[1,0,1] neg_lo:[0,1,0]\n"
    "v_pk_fma_f32 v[104:105], v[104:105], v[44:45], v[144:145] op_sel:[1,1,0] op_sel_hi:[1,0,1] neg_lo:[0,1,0]\n"
    "v_pk_fma_f32 v[122:123], v[122:123], v[46:47], v[146:147] op_sel:[1,1,0] op_sel_hi:[1,0,1]\n"
    "v_pk_fma_f32 v[124:125], v[124:125], v[48:49], v[148:149] op_sel:[1,1,0] op_sel_hi:[1,0,1]\n"
    "v_pk_fma_f32 v[54:55], v[54:55], v[46:47], v[150:151] op_sel:[1,1,0] op_sel_hi:[1,0,1]\n"
    "v_pk_fma_f32 v[56:57], v[56:57], v[48:49], v[152:153] op_sel:[1,1,0] op_sel_hi:[1,0,1]\n"
    "v_cvt_pk_f16_f32 v62, v98, v99\n"
    "v_cvt_pk_f16_f32 v63, v100, v101\n"
    "v_cvt_pk_f16_f32 v64, v102, v103\n"
    "v_cvt_pk_f16_f32 v65, v104, v105\n"
    "v_cvt_pk_f16_f32 v66, v122, v123\n"
    "v_cvt_pk_f16_f32 v67, v124, v125\n"
    "v_cvt_pk_f16_f32 v68, v54, v55\n"
    "v_cvt_pk_f16_f32 v69, v56, v57\n"
    "ds_write2_b64 v169, v[62:63], v[64:65] offset0:132 offset1:198\n"
    "ds_write2_b64 v178, v[82:83], v[66:67] offset0:0 offset1:66\n"
    "ds_write2_b64 v181, v[60:61], v[68:69] offset0:0 offset1:66\n"
    "buffer_load_dwordx2 v[222:223], v14, s[44:47], s10 offen offset:1280\n"
    "v_pk_mul_f32 v[138:139], v[90:91], v[42:43] op_sel_hi:[0,1]\n"
    "v_pk_mul_f32 v[140:141], v[92:93], v[44:45] op_sel_hi:[0,1]\n"
    "v_pk_mul_f32 v[142:143], v[94:95], v[42:43] op_sel_hi:[0,1]\n"
    "v_pk_mul_f32 v[144:145], v[96:97], v[44:45] op_sel_hi:[0,1]\n"
    "v_pk_mul_f32 v[146:147], v[50:51], v[46:47] op_sel_hi:[0,1] neg_hi:[0,1]\n"
    "v_pk_mul_f32 v[148:149], v[52:53], v[48:49] op_sel_hi:[0,1] neg_hi:[0,1]\n"
    "v_pk_mul_f32 v[150:151], v[54:55], v[46:47] op_sel_hi:[0,1] neg_hi:[0,1]\n"
    "v_pk_mul_f32 v[152:153], v[56:57], v[48:49] op_sel_hi:[0,1] neg_hi:[0,1]\n"
    "v_pk_fma_f32 v[90:91], v[90:91], v[42:43], v[138:139] op_sel:[1,1,0] op_sel_hi:[1,0,1] neg_lo:[0,1,0]\n"
    "v_pk_fma_f32 v[92:93], v[92:93], v[44:45], v[140:141] op_sel:[1,1,0] op_sel_hi:[1,0,1] neg_lo:[0,1,0]\n"
    "v_pk_fma_f32 v[94:95], v[94:95], v[42:43], v[142:143] op_sel:[1,1,0] op_sel_hi:[1,0,1] neg_lo:[0,1,0]\n"
    "v_pk_fma_f32 v[96:97], v[96:97], v[44:45], v[144:145] op_sel:[1,1,0] op_sel_hi:[1,0,1] neg_lo:[0,1,0]\n"
    "v_pk_fma_f32 v[50:51], v[50:51], v[46:47], v[146:147] op_sel:[1,1,0] op_sel_hi:[1,0,1]\n"
    "v_pk_fma_f32 v[52:53], v[52:53], v[48:49], v[148:149] op_sel:[1,1,0] op_sel_hi:[1,0,1]\n"
    "v_pk_fma_f32 v[54:55], v[54:55], v[46:47], v[150:151] op_sel:[1,1,0] op_sel_hi:[1,0,1]\n"
    "v_pk_fma_f32 v[56:57], v[56:57], v[48:49], v[152:153] op_sel:[1,1,0] op_sel_hi:[1,0,1]\n"
    "v_cvt_pk_f16_f32 v70, v90, v91\n"
    "v_cvt_pk_f16_f32 v71, v92, v93\n"
    "v_cvt_pk_f16_f32 v72, v94, v95\n"
    "v_cvt_pk_f16_f32 v73, v96, v97\n"
    "v_cvt_pk_f16_f32 v74, v50, v51\n"
    "v_cvt_pk_f16_f32 v75, v52, v53\n"
    "v_cvt_pk_f16_f32 v76, v54, v55\n"
    "v_cvt_pk_f16_f32 v77, v56, v57\n"
    "ds_write2_b64 v170, v[70:71], v[72:73] offset0:0 offset1:66\n"
    "ds_write2_b64 v179, v[74:75], v[84:85] offset0:0 offset1:132\n"
    "v_pk_mul_f32 v[138:139], v[98:99], v[42:43] op_sel_hi:[0,1]\n"
    "v_pk_mul_f32 v[140:141], v[100:101], v[44:45] op_sel_hi:[0,1]\n"
    "v_pk_mul_f32 v[142:143], v[102:103], v[42:43] op_sel_hi:[0,1]\n"
    "v_pk_mul_f32 v[144:145], v[104:105], v[44:45] op_sel_hi:[0,1]\n"
    "v_pk_mul_f32 v[146:147], v[122:123], v[46:47] op_sel_hi:[0,1] neg_hi:[0,1]\n"
    "v_pk_mul_f32 v[148:149], v[124:125], v[48:49] op_sel_hi:[0,1] neg_hi:[0,1]\n"
    "v_pk_mul_f32 v[150:151], v[50:51], v[46:47] op_sel_hi:[0,1] neg_hi:[0,1]\n"
    "v_pk_mul_f32 v[152:153], v[52:53], v[48:49] op_sel_hi:[0,1] neg_hi:[0,1]\n"
    "v_pk_fma_f32 v[98:99], v[98:99], v[42:43], v[138:139] op_sel:[1,1,0] op_sel_hi:[1,0,1] neg_lo:[0,1,0]\n"
    "v_pk_fma_f32 v[100:101], v[100:101], v[44:45], v[140:141] op_sel:[1,1,0] op_sel_hi:[1,0,1] neg_lo:[0,1,0]\n"
    "v_pk_fma_f32 v[102:103], v[102:103], v[42:43], v[142:143] op_sel:[1,1,0] op_sel_hi:[1,0,1] neg_lo:[0,1,0]\n"
    "v_pk_fma_f32 v[104:105], v[104:105], v[44:45], v[144:145] op_sel:[1,1,0] op_sel_hi:[1,0,1] neg_lo:[0,1,0]\n"
    "v_pk_fma_f32 v[122:123], v[122:123], v[46:47], v[146:147] op_sel:[1,1,0] op_sel_hi:[1,0,1]\n"
    "v_pk_fma_f32 v[124:125], v[124:125], v[48:49], v[148:149] op_sel:[1,1,0] op_sel_hi:[1,0,1]\n"
    "v_pk_fma_f32 v[50:51], v[50:51], v[46:47], v[150:151] op_sel:[1,1,0] op_sel_hi:[1,0,1]\n"
    "v_pk_fma_f32 v[52:53], v[52:53], v[48:49], v[152:153] op_sel:[1,1,0] op_sel_hi:[1,0,1]\n"
    "v_cvt_pk_f16_f32 v78, v98, v99\n"
    "v_cvt_pk_f16_f32 v79, v100, v101\n"
    "v_cvt_pk_f16_f32 v80, v102, v103\n"
    "v_cvt_pk_f16_f32 v81, v104, v105\n"
    "v_cvt_pk_f16_f32 v82, v122, v123\n"
    "v_cvt_pk_f16_f32 v83, v124, v125\n"
    "v_cvt_pk_f16_f32 v84, v50, v51\n"
    "v_cvt_pk_f16_f32 v85, v52, v53\n"
    "ds_write2_b64 v170, v[78:79], v[80:81] offset0:132 offset1:198\n"
    "ds_write2_b64 v180, v[82:83], v[84:85] offset0:0 offset1:66\n"
    "s_mul_i32 s10, s99, 163840\n"
    "buffer_load_dwordx2 v[224:225], v14, s[44:47], s10 offen offset:0\n"
    "v_pk_mul_f32 v[138:139], v[90:91], v[42:43] op_sel_hi:[0,1]\n"
    "v_pk_mul_f32 v[140:141], v[92:93], v[44:45] op_sel_hi:[0,1]\n"
    "v_pk_mul_f32 v[142:143], v[94:95], v[42:43] op_sel_hi:[0,1]\n"
    "v_pk_mul_f32 v[144:145], v[96:97], v[44:45] op_sel_hi:[0,1]\n"
    "v_pk_mul_f32 v[146:147], v[54:55], v[46:47] op_sel_hi:[0,1] neg_hi:[0,1]\n"
    "v_pk_mul_f32 v[148:149], v[56:57], v[48:49] op_sel_hi:[0,1] neg_hi:[0,1]\n"
    "v_pk_mul_f32 v[150:151], v[98:99], v[42:43] op_sel_hi:[0,1]\n"
    "v_pk_mul_f32 v[152:153], v[100:101], v[44:45] op_sel_hi:[0,1]\n"
    "v_pk_fma_f32 v[90:91], v[90:91], v[42:43], v[138:139] op_sel:[1,1,0] op_sel_hi:[1,0,1] neg_lo:[0,1,0]\n"
    "v_pk_fma_f32 v[92:93], v[92:93], v[44:45], v[140:141] op_sel:[1,1,0] op_sel_hi:[1,0,1] neg_lo:[0,1,0]\n"
    "v_pk_fma_f32 v[94:95], v[94:95], v[42:43], v[142:143] op_sel:[1,1,0] op_sel_hi:[1,0,1] neg_lo:[0,1,0]\n"
    "v_pk_fma_f32 v[96:97], v[96:97], v[44:45], v[144:145] op_sel:[1,1,0] op_sel_hi:[1,0,1] neg_lo:[0,1,0]\n"
    "v_pk_fma_f32 v[54:55], v[54:55], v[46:47], v[146:147] op_sel:[1,1,0] op_sel_hi:[1,0,1]\n"
    "v_pk_fma_f32 v[56:57], v[56:57], v[48:49], v[148:149] op_sel:[1,1,0] op_sel_hi:[1,0,1]\n"
    "v_pk_fma_f32 v[98:99], v[98:99], v[42:43], v[150:151] op_sel:[1,1,0] op_sel_hi:[1,0,1] neg_lo:[0,1,0]\n"
    "v_pk_fma_f32 v[100:101], v[100:101], v[44:45], v[152:153] op_sel:[1,1,0] op_sel_hi:[1,0,1] neg_lo:[0,1,0]\n"
    "v_cvt_pk_f16_f32 v86, v90, v91\n"
    "v_cvt_pk_f16_f32 v87, v92, v93\n"
    "v_cvt_pk_f16_f32 v88, v94, v95\n"
    "v_cvt_pk_f16_f32 v89, v96, v97\n"
    "v_cvt_pk_f16_f32 v58, v54, v55\n"
    "v_cvt_pk_f16_f32 v59, v56, v57\n"
    "v_cvt_pk_f16_f32 v60, v98, v99\n"
    "v_cvt_pk_f16_f32 v61, v100, v101\n"
    "ds_write2_b64 v171, v[86:87], v[88:89] offset0:0 offset1:66\n"
    "ds_write2_b64 v182, v[76:77], v[58:59] offset0:0 offset1:66\n"
    "v_pk_mul_f32 v[138:139], v[102:103], v[42:43] op_sel_hi:[0,1]\n"
    "v_pk_mul_f32 v[140:141], v[104:105], v[44:45] op_sel_hi:[0,1]\n"
    "v_pk_mul_f32 v[142:143], v[90:91], v[42:43] op_sel_hi:[0,1]\n"
    "v_pk_mul_f32 v[144:145], v[92:93], v[44:45] op_sel_hi:[0,1]\n"
    "v_pk_mul_f32 v[146:147], v[94:95], v[42:43] op_sel_hi:[0,1]\n"
    "v_pk_mul_f32 v[148:149], v[96:97], v[44:45] op_sel_hi:[0,1]\n"
    "v_pk_mul_f32 v[150:151], v[54:55], v[46:47] op_sel_hi:[0,1] neg_hi:[0,1]\n"
    "v_pk_mul_f32 v[152:153], v[56:57], v[48:49] op_sel_hi:[0,1] neg_hi:[0,1]\n"
    "v_pk_fma_f32 v[102:103], v[102:103], v[42:43], v[138:139] op_sel:[1,1,0] op_sel_hi:[1,0,1] neg_lo:[0,1,0]\n"
    "v_pk_fma_f32 v[104:105], v[104:105], v[44:45], v[140:141] op_sel:[1,1,0] op_sel_hi:[1,0,1] neg_lo:[0,1,0]\n"
    "v_pk_fma_f32 v[90:91], v[90:91], v[42:43], v[142:143] op_sel:[1,1,0] op_sel_hi:[1,0,1] neg_lo:[0,1,0]\n"
    "v_pk_fma_f32 v[92:93], v[92:93], v[44:45], v[144:145] op_sel:[1,1,0] op_sel_hi:[1,0,1] neg_lo:[0,1,0]\n"
    "v_pk_fma_f32 v[94:95], v[94:95], v[42:43], v[146:147] op_sel:[1,1,0] op_sel_hi:[1,0,1] neg_lo:[0,1,0]\n"
    "v_pk_fma_f32 v[96:97], v[96:97], v[44:45], v[148:149] op_sel:[1,1,0] op_sel_hi:[1,0,1] neg_lo:[0,1,0]\n"
    "v_pk_fma_f32 v[54:55], v[54:55], v[46:47], v[150:151] op_sel:[1,1,0] op_sel_hi:[1,0,1]\n"
    "v_pk_fma_f32 v[56:57], v[56:57], v[48:49], v[152:153] op_sel:[1,1,0] op_sel_hi:[1,0,1]\n"
    "v_cvt_pk_f16_f32 v62, v102, v103\n"
    "v_cvt_pk_f16_f32 v63, v104, v105\n"
    "v_cvt_pk_f16_f32 v64, v90, v91\n"
    "v_cvt_pk_f16_f32 v65, v92, v93\n"
    "v_cvt_pk_f16_f32 v66, v94, v95\n"
    "v_cvt_pk_f16_f32 v67, v96, v97\n"
    "v_cvt_pk_f16_f32 v68, v54, v55\n"
    "v_cvt_pk_f16_f32 v69, v56, v57\n"
    "ds_write2_b64 v171, v[60:61], v[62:63] offset0:132 offset1:198\n"
    "ds_write2_b64 v172, v[64:65], v[66:67] offset0:0 offset1:66\n"
    "ds_write_b64 v199, v[68:69] offset:30624\n"
    "buffer_load_dwordx2 v[226:227], v14, s[44:47], s10 offen offset:1280\n"
    "v_pk_mul_f32 v[138:139], v[98:99], v[42:43] op_sel_hi:[0,1]\n"
    "v_pk_mul_f32 v[140:141], v[100:101], v[44:45] op_sel_hi:[0,1]\n"
    "v_pk_mul_f32 v[142:143], v[102:103], v[42:43] op_sel_hi:[0,1]\n"
    "v_pk_mul_f32 v[144:145], v[104:105], v[44:45] op_sel_hi:[0,1]\n"
    "v_pk_fma_f32 v[98:99], v[98:99], v[42:43], v[138:139] op_sel:[1,1,0] op_sel_hi:[1,0,1] neg_lo:[0,1,0]\n"
    "v_pk_fma_f32 v[100:101], v[100:101], v[44:45], v[140:141] op_sel:[1,1,0] op_sel_hi:[1,0,1] neg_lo:[0,1,0]\n"
    "v_pk_fma_f32 v[102:103], v[102:103], v[42:43], v[142:143] op_sel:[1,1,0] op_sel_hi:[1,0,1] neg_lo:[0,1,0]\n"
    "v_pk_fma_f32 v[104:105], v[104:105], v[44:45], v[144:145] op_sel:[1,1,0] op_sel_hi:[1,0,1] neg_lo:[0,1,0]\n"
    "v_cvt_pk_f16_f32 v70, v98, v99\n"
    "v_cvt_pk_f16_f32 v71, v100, v101\n"
    "v_cvt_pk_f16_f32 v72, v102, v103\n"
    "v_cvt_pk_f16_f32 v73, v104, v105\n"
    "ds_write2_b64 v172, v[70:71], v[72:73] offset0:132 offset1:198\n"
    "ds_read_b128 v[92:95], v200 offset:16896\n"
    "ds_read_b128 v[96:99], v200 offset:0\n"
    "ds_read_b128 v[100:103], v200 offset:16928\n"
    "ds_read_b128 v[104:107], v200 offset:32\n"
    "ds_read_b128 v[108:111], v200 offset:16960\n"
    "ds_read_b128 v[112:115], v200 offset:64\n"
    "ds_read_b128 v[116:119], v200 offset:16992\n"
    "ds_read_b128 v[120:123], v200 offset:96\n"
    "ds_read_b128 v[124:127], v200 offset:17024\n"
    "ds_read_b128 v[128:131], v200 offset:128\n"
    "s_waitcnt lgkmcnt(8)\n"
    "v_mfma_f32_32x32x16_f16 v[20:35], v[92:95], v[96:99], 0\n"
    "s_waitcnt lgkmcnt(6)\n"
    "ds_read_b128 v[132:135], v200 offset:17056\n"
    "ds_read_b128 v[136:139], v200 offset:160\n"
    "v_mfma_f32_32x32x16_f16 v[20:35], v[100:103], v[104:107], v[20:35]\n"
    "s_waitcnt lgkmcnt(6)\n"
    "ds_read_b128 v[92:95], v200 offset:17088\n"
    "ds_read_b128 v[96:99], v200 offset:192\n"
    "v_mfma_f32_32x32x16_f16 v[20:35], v[108:111], v[112:115], v[20:35]\n"
    "s_waitcnt lgkmcnt(6)\n"
    "ds_read_b128 v[100:103], v200 offset:17120\n"
    "ds_read_b128 v[104:107], v200 offset:224\n"
    "v_mfma_f32_32x32x16_f16 v[20:35], v[116:119], v[120:123], v[20:35]\n"
    "s_waitcnt lgkmcnt(6)\n"
    "ds_read_b128 v[108:111], v200 offset:17152\n"
    "ds_read_b128 v[112:115], v200 offset:256\n"
    "v_mfma_f32_32x32x16_f16 v[20:35], v[124:127], v[128:131], v[20:35]\n"
    "s_waitcnt lgkmcnt(6)\n"
    "ds_read_b128 v[116:119], v200 offset:17184\n"
    "ds_read_b128 v[120:123], v200 offset:288\n"
    "v_mfma_f32_32x32x16_f16 v[20:35], v[132:135], v[136:139], v[20:35]\n"
    "s_waitcnt lgkmcnt(6)\n"
    "ds_read_b128 v[124:127], v200 offset:17216\n"
    "ds_read_b128 v[128:131], v200 offset:320\n"
    "v_mfma_f32_32x32x16_f16 v[20:35], v[92:95], v[96:99], v[20:35]\n"
    "s_waitcnt lgkmcnt(6)\n"
    "ds_read_b128 v[132:135], v200 offset:17248\n"
    "ds_read_b128 v[136:139], v200 offset:352\n"
    "v_mfma_f32_32x32x16_f16 v[20:35], v[100:103], v[104:107], v[20:35]\n"
    "s_waitcnt lgkmcnt(6)\n"
    "ds_read_b128 v[92:95], v200 offset:17280\n"
    "ds_read_b128 v[96:99], v200 offset:384\n"
    "v_mfma_f32_32x32x16_f16 v[20:35], v[108:111], v[112:115], v[20:35]\n"
    "s_waitcnt lgkmcnt(6)\n"
    "ds_read_b128 v[100:103], v200 offset:17312\n"
    "ds_read_b128 v[104:107], v200 offset:416\n"
    "s_lshl_b32 s58, s96, 10\n"
    "s_add_u32 s58, s58, 65536\n"
    "buffer_load_dwordx4 v[228:231], v251, s[32:35], s58 offen sc1\n"
    "s_lshl_b32 s58, s97, 10\n"
    "s_add_u32 s58, s58, 65536\n"
    "buffer_load_dwordx4 v[232:235], v251, s[32:35], s58 offen sc1\n"
    "s_lshl_b32 s58, s98, 10\n"
    "s_add_u32 s58, s58, 65536\n"
    "buffer_load_dwordx4 v[236:239], v251, s[32:35], s58 offen sc1\n"
    "s_lshl_b32 s58, s99, 10\n"
    "s_add_u32 s58, s58, 65536\n"
    "buffer_load_dwordx4 v[240:243], v251, s[32:35], s58 offen sc1\n"
    "v_mfma_f32_32x32x16_f16 v[20:35], v[116:119], v[120:123], v[20:35]\n"
    "s_waitcnt lgkmcnt(6)\n"
    "ds_read_b128 v[108:111], v200 offset:17344\n"
    "ds_read_b128 v[112:115], v200 offset:448\n"
    "v_mfma_f32_32x32x16_f16 v[20:35], v[124:127], v[128:131], v[20:35]\n"
    "s_waitcnt lgkmcnt(6)\n"
    "ds_read_b128 v[116:119], v200 offset:17376\n"
    "ds_read_b128 v[120:123], v200 offset:480\n"
    "v_mfma_f32_32x32x16_f16 v[20:35], v[132:135], v[136:139], v[20:35]\n"
    "s_waitcnt vmcnt(12) lgkmcnt(6)\n"
    "v_readfirstlane_b32 s51, v18\n"
    "s_nop 0\n"
    "s_add_u32 s51, s51, 1\n"
    "v_mfma_f32_32x32x16_f16 v[20:35], v[92:95], v[96:99], v[20:35]\n"
    "s_waitcnt lgkmcnt(4)\n"
    "v_mov_b32 v36, s59\n"
    "v_mul_f32 v36, s30, v36\n"
    "v_cmp_eq_u32 vcc, 0, v0\n"
    "v_mov_b32 v38, 0\n"
    "v_mov_b32 v39, 0\n"
    "v_cndmask_b32_e64 v36, 0, v36, vcc\n"
    "v_mfma_f32_32x32x16_f16 v[20:35], v[100:103], v[104:107], v[20:35]\n"
    "s_waitcnt lgkmcnt(2)\n"
    "v_min_u32 v37, 10, v0\n"
    "v_cmp_lt_u32_e64 vcc, 6, v0\n"
    "v_mov_b32 v40, 2056\n"
    "s_add_u32 s58, s55, 28676\n"
    "v_cndmask_b32_e64 v40, 0, v40, vcc\n"
    "v_lshl_add_u32 v37, v37, 3, s58\n"
    "v_mfma_f32_32x32x16_f16 v[20:35], v[108:111], v[112:115], v[20:35]\n"
    "s_waitcnt lgkmcnt(0)\n"
    "v_add_u32 v37, v37, v40\n"
    "v_and_b32_e64 v41, 7, v0\n"
    "v_lshl_add_u32 v41, v41, 3, s55\n"
    "v_mov_b32_e64 v42, s55\n"
    "v_mfma_f32_32x32x16_f16 v[20:35], v[116:119], v[120:123], v[20:35]\n"
    "ds_write2_b32 v37, v38, v39 offset0:0 offset1:1\n"
    "ds_write_b64 v37, v[38:39] offset:2364\n"
    "s_nop 9\n"
    "v_add_f32 v20, v20, v36\n"
    "v_cvt_pk_f16_f32 v44, v23, v22\n"
    "v_cvt_pk_f16_f32 v46, v26, v25\n"
    "v_cvt_pk_f16_f32 v47, v24, v24\n"
    "v_cvt_pk_f16_f32 v45, v21, v20\n"
    "v_cvt_pk_f16_f32 v48, v30, v30\n"
    "v_cvt_pk_f16_f32 v49, v29, v29\n"
    "v_cvt_pk_f16_f32 v50, v29, v28\n"
    "v_cvt_pk_f16_f32 v51, v28, v28\n"
    "v_cvt_pk_f16_f32 v52, v32, v33\n"
    "v_cvt_pk_f16_f32 v54, v32, v32\n"
    "v_cvt_pk_f16_f32 v55, v32, v32\n"
    "v_cvt_pk_f16_f32 v53, 0, v32\n"
    "v_cvt_pk_f16_f32 v56, v34, v34\n"
    "ds_write_b64 v196, v[44:45] offset:2876\n"
    "ds_write2_b32 v196, v44, v45 offset0:128 offset1:129\n"
    "ds_write_b64 v196, v[46:47] offset:2364\n"
    "ds_write2_b32 v196, v46, v47 offset0:0 offset1:1\n"
    "ds_write_b128 v197, v[48:51] offset:2364\n"
    "ds_write2_b32 v197, v48, v49 offset0:0 offset1:1\n"
    "ds_write2_b32 v197, v50, v51 offset0:2 offset1:3\n"
    "ds_write_b16 v198, v52 offset:64\n"
    "ds_write_b16_d16_hi v198, v52\n"
    "ds_write_b64 v41, v[38:39] offset:33536\n"
    "s_mov_b64 exec, 1\n"
    "s_nop 0\n"
    "ds_write_b32 v42, v53 offset:28728\n"
    "ds_write_b32 v42, v53 offset:31092\n"
    "ds_write_b64 v42, v[54:55] offset:31096\n"
    "ds_write_b32 v42, v54 offset:28732\n"
    "ds_write_b32 v42, v54 offset:28736\n"
    "ds_write_b16 v42, v56 offset:33280\n"
    "s_mov_b64 exec, -1\n"
    "s_add_u32 m0, s55, 0\n"
    "s_nop 0\n"
    "s_nop 0\n"
    "buffer_load_dwordx4 v244, s[36:39], s12 offen offset:0 lds\n"
    "buffer_load_dwordx4 v245, s[36:39], s12 offen offset:1024 lds\n"
    "ds_read_b32 v135, v4 offset:1536\n"
    "ds_read_b64 v[116:117], v4 offset:1540\n"
    "ds_read_b64 v[118:119], v4 offset:1548\n"
    "ds_read_b32 v139, v4 offset:1024\n"
    "ds_read_b64 v[120:121], v4 offset:1028\n"
    "ds_read_b64 v[122:123], v4 offset:1036\n"
    "ds_read_b32 v143, v4 offset:512\n"
    "ds_read_b64 v[124:125], v4 offset:516\n"
    "ds_read_b64 v[126:127], v4 offset:524\n"
    "ds_read_b32 v147, v4 offset:0\n"
    "ds_read_b64 v[128:129], v4 offset:4\n"
    "ds_read_b64 v[130:131], v4 offset:12\n"
    "s_waitcnt vmcnt(5)\n"
    "v_xor_b32 v60, s51, v229\n"
    "v_xor_b32 v61, s51, v231\n"
    "s_waitcnt vmcnt(4)\n"
    "v_bitop3_b32 v60, v233, s51, v60 bitop3:0xbe\n"
    "v_bitop3_b32 v61, v235, s51, v61 bitop3:0xbe\n"
    "s_waitcnt vmcnt(3)\n"
    "v_bitop3_b32 v60, v237, s51, v60 bitop3:0xbe\n"
    "v_bitop3_b32 v61, v239, s51, v61 bitop3:0xbe\n"
    "s_waitcnt vmcnt(2)\n"
    "v_bitop3_b32 v60, v241, s51, v60 bitop3:0xbe\n"
    "v_bitop3_b32 v61, v243, s51, v61 bitop3:0xbe\n"
    "v_or_b32 v60, v60, v61\n"
    "v_cmp_ne_u32 vcc, 0, v60\n"
    "s_nop 0\n"
    "s_cbranch_vccnz .Lk_ool_3\n"
    ".Lk_ret_4:\n"
    "buffer_load_dwordx4 v246, s[36:39], s12 offen offset:2048 lds\n"
    "buffer_load_dwordx4 v247, s[36:39], s12 offen offset:3072 lds\n"
    "v_and_b32 v60, 0xffff0000, v228\n"
    "v_lshlrev_b32_e64 v62, 16, v228\n"
    "v_and_b32 v61, 0xffff0000, v230\n"
    "v_lshlrev_b32 v63, 16, v230\n"
    "v_rcp_f32 v60, v60\n"
    "v_rcp_f32 v61, v61\n"
    "v_lshlrev_b32 v64, 16, v212\n"
    "v_lshlrev_b32_e64 v65, 16, v214\n"
    "v_and_b32 v66, 0xffff0000, v212\n"
    "v_and_b32 v67, 0xffff0000, v214\n"
    "v_lshlrev_b32 v68, 16, v213\n"
    "v_lshlrev_b32 v69, 16, v215\n"
    "v_and_b32 v70, 0xffff0000, v213\n"
    "v_and_b32 v71, 0xffff0000, v215\n"
    "v_pk_mul_f32 v[62:63], v[62:63], v[60:61]\n"
    "v_pk_fma_f32 v[64:65], v[64:65], v[60:61], v[62:63] neg_lo:[0,0,1] neg_hi:[0,0,1]\n"
    "v_pk_fma_f32 v[66:67], v[66:67], v[60:61], v[62:63] neg_lo:[0,0,1] neg_hi:[0,0,1]\n"
    "v_pk_fma_f32 v[68:69], v[68:69], v[60:61], v[62:63] neg_lo:[0,0,1] neg_hi:[0,0,1]\n"
    "v_pk_fma_f32 v[70:71], v[70:71], v[60:61], v[62:63] neg_lo:[0,0,1] neg_hi:[0,0,1]\n"
    "v_cvt_pk_f16_f32 v20, v64, v65\n"
    "v_cvt_pk_f16_f32 v21, v66, v67\n"
    "v_cvt_pk_f16_f32 v22, v68, v69\n"
    "v_cvt_pk_f16_f32 v23, v70, v71\n"
    "s_add_u32 m0, s55, 4096\n"
    "s_nop 0\n"
    "buffer_load_dwordx4 v248, s[36:39], s12 offen offset:0 lds\n"
    "buffer_load_dwordx4 v249, s[36:39], s12 offen offset:1024 lds\n"
    "v_and_b32 v76, 0xffff0000, v232\n"
    "v_lshlrev_b32 v78, 16, v232\n"
    "v_and_b32 v77, 0xffff0000, v234\n"
    "v_lshlrev_b32 v79, 16, v234\n"
    "v_rcp_f32 v76, v76\n"
    "v_rcp_f32 v77, v77\n"
    "v_lshlrev_b32 v80, 16, v216\n"
    "v_lshlrev_b32_e64 v81, 16, v218\n"
    "v_and_b32 v82, 0xffff0000, v216\n"
    "v_and_b32 v83, 0xffff0000, v218\n"
    "v_lshlrev_b32 v84, 16, v217\n"
    "v_lshlrev_b32 v85, 16, v219\n"
    "v_and_b32 v86, 0xffff0000, v217\n"
    "v_and_b32 v87, 0xffff0000, v219\n"
    "v_pk_mul_f32 v[78:79], v[78:79], v[76:77]\n"
    "v_pk_fma_f32 v[80:81], v[80:81], v[76:77], v[78:79] neg_lo:[0,0,1] neg_hi:[0,0,1]\n"
    "v_pk_fma_f32 v[82:83], v[82:83], v[76:77], v[78:79] neg_lo:[0,0,1] neg_hi:[0,0,1]\n"
    "v_pk_fma_f32 v[84:85], v[84:85], v[76:77], v[78:79] neg_lo:[0,0,1] neg_hi:[0,0,1]\n"
    "v_pk_fma_f32 v[86:87], v[86:87], v[76:77], v[78:79] neg_lo:[0,0,1] neg_hi:[0,0,1]\n"
    "v_cvt_pk_f16_f32 v24, v80, v81\n"
    "v_cvt_pk_f16_f32 v25, v82, v83\n"
    "v_cvt_pk_f16_f32 v26, v84, v85\n"
    "v_cvt_pk_f16_f32 v27, v86, v87\n"
    "ds_write_b32 v7, v20 offset:0\n"
    "ds_write_b32 v7, v21 offset:40960\n"
    "ds_write_b32 v8, v22 offset:0\n"
    "ds_write_b32 v8, v23 offset:40960\n"
    "ds_write_b32 v9, v24 offset:0\n"
    "ds_write_b32 v9, v25 offset:40960\n"
    "ds_write_b32 v10, v26 offset:0\n"
    "ds_write_b32 v10, v27 offset:40960\n"
    "buffer_load_dwordx4 v250, s[36:39], s12 offen offset:2048 lds\n"
    "s_add_u32 m0, s55, 7168\n"
    "s_nop 0\n"
    "buffer_load_dwordx4 v244, s[36:39], s13 offen offset:0 lds\n"
    "v_and_b32 v60, 0xffff0000, v236\n"
    "v_lshlrev_b32 v62, 16, v236\n"
    "v_and_b32 v61, 0xffff0000, v238\n"
    "v_lshlrev_b32 v63, 16, v238\n"
    "v_rcp_f32 v60, v60\n"
    "v_rcp_f32 v61, v61\n"
    "v_lshlrev_b32 v64, 16, v220\n"
    "v_lshlrev_b32_e64 v65, 16, v222\n"
    "v_and_b32 v66, 0xffff0000, v220\n"
    "v_and_b32 v67, 0xffff0000, v222\n"
    "v_lshlrev_b32 v68, 16, v221\n"
    "v_lshlrev_b32 v69, 16, v223\n"
    "v_and_b32 v70, 0xffff0000, v221\n"
    "v_and_b32 v71, 0xffff0000, v223\n"
    "v_pk_mul_f32 v[62:63], v[62:63], v[60:61]\n"
    "v_pk_fma_f32 v[64:65], v[64:65], v[60:61], v[62:63] neg_lo:[0,0,1] neg_hi:[0,0,1]\n"
    "v_pk_fma_f32 v[66:67], v[66:67], v[60:61], v[62:63] neg_lo:[0,0,1] neg_hi:[0,0,1]\n"
    "v_pk_fma_f32 v[68:69], v[68:69], v[60:61], v[62:63] neg_lo:[0,0,1] neg_hi:[0,0,1]\n"
    "v_pk_fma_f32 v[70:71], v[70:71], v[60:61], v[62:63] neg_lo:[0,0,1] neg_hi:[0,0,1]\n"
    "v_cvt_pk_f16_f32 v28, v64, v65\n"
    "v_cvt_pk_f16_f32 v29, v66, v67\n"
    "v_cvt_pk_f16_f32 v30, v68, v69\n"
    "v_cvt_pk_f16_f32 v31, v70, v71\n"
    "buffer_load_dwordx4 v245, s[36:39], s13 offen offset:1024 lds\n"
    "buffer_load_dwordx4 v246, s[36:39], s13 offen offset:2048 lds\n"
    "v_and_b32 v76, 0xffff0000, v240\n"
    "v_lshlrev_b32_e64 v78, 16, v240\n"
    "v_and_b32 v77, 0xffff0000, v242\n"
    "v_lshlrev_b32 v79, 16, v242\n"
    "v_rcp_f32 v76, v76\n"
    "v_rcp_f32 v77, v77\n"
    "v_lshlrev_b32 v80, 16, v224\n"
    "v_lshlrev_b32_e64 v81, 16, v226\n"
    "v_and_b32 v82, 0xffff0000, v224\n"
    "v_and_b32 v83, 0xffff0000, v226\n"
    "v_lshlrev_b32 v84, 16, v225\n"
    "v_lshlrev_b32 v85, 16, v227\n"
    "v_and_b32 v86, 0xffff0000, v225\n"
    "v_and_b32 v87, 0xffff0000, v227\n"
    "v_pk_mul_f32 v[78:79], v[78:79], v[76:77]\n"
    "v_pk_fma_f32 v[80:81], v[80:81], v[76:77], v[78:79] neg_lo:[0,0,1] neg_hi:[0,0,1]\n"
    "v_pk_fma_f32 v[82:83], v[82:83], v[76:77], v[78:79] neg_lo:[0,0,1] neg_hi:[0,0,1]\n"
    "v_pk_fma_f32 v[84:85], v[84:85], v[76:77], v[78:79] neg_lo:[0,0,1] neg_hi:[0,0,1]\n"
    "v_pk_fma_f32 v[86:87], v[86:87], v[76:77], v[78:79] neg_lo:[0,0,1] neg_hi:[0,0,1]\n"
    "v_cvt_pk_f16_f32 v32, v80, v81\n"
    "v_cvt_pk_f16_f32 v33, v82, v83\n"
    "v_cvt_pk_f16_f32 v34, v84, v85\n"
    "v_cvt_pk_f16_f32 v35, v86, v87\n"
    "ds_write_b32 v11, v28 offset:0\n"
    "ds_write_b32 v11, v29 offset:40960\n"
    "ds_write_b32 v12, v30 offset:0\n"
    "ds_write_b32 v12, v31 offset:40960\n"
    "ds_write_b32 v19, v32 offset:0\n"
    "ds_write_b32 v19, v33 offset:40960\n"
    "ds_write_b32 v252, v34 offset:0\n"
    "ds_write_b32 v252, v35 offset:40960\n"
    "s_waitcnt lgkmcnt(14)\n"
    "s_nop 0\n"
    "v_alignbit_b32 v84, v116, v135, v6\n"
    "v_alignbit_b32 v85, v117, v116, v6\n"
    "v_alignbit_b32 v86, v118, v117, v6\n"
    "v_alignbit_b32 v87, v119, v118, v6\n"
    "v_alignbit_b32 v88, v120, v139, v6\n"
    "v_alignbit_b32 v89, v121, v120, v6\n"
    "v_alignbit_b32 v90, v122, v121, v6\n"
    "v_alignbit_b32 v91, v123, v122, v6\n"
    "s_memrealtime s[0:1]\n"
    "s_memtime s[76:77]\n"
    "s_waitcnt lgkmcnt(0)\n"
    "s_barrier\n"
    "ds_read_b128 v[52:55], v5 offset:0\n"
    "ds_read_b128 v[56:59], v5 offset:256\n"
    "ds_read_b128 v[60:63], v5 offset:512\n"
    "ds_read_b128 v[64:67], v5 offset:768\n"
    "ds_read_b128 v[68:71], v5 offset:1024\n"
    "ds_read_b128 v[72:75], v5 offset:1280\n"
    "ds_read_b128 v[76:79], v5 offset:1536\n"
    "ds_read_b128 v[80:83], v5 offset:1792\n"
    "s_lshl_b32 s58, s54, 3\n"
    "s_lshl_b32 s59, s54, 4\n"
    "s_sub_u32 s59, s59, 1024\n"
    "s_max_i32 s58, s58, s59\n"
    "s_add_u32 s58, s58, 32768\n"
    "s_cmp_eq_u32 s48, 0\n"
    "s_cselect_b32 s58, s58, 0x70000000\n"
    "v_lshlrev_b32 v196, 11, v0\n"
    "v_mov_b32 v198, s0\n"
    "v_mov_b32_e64 v199, s51\n"
    "s_mov_b64 exec, 0xff\n"
    "buffer_store_dwordx2 v[198:199], v196, s[32:35], s58 offen sc1\n"
    "s_mov_b64 exec, -1\n"
    "s_waitcnt lgkmcnt(7)\n"
    "v_mfma_f32_16x16x32_f16 v[20:23], v[84:87], v[52:55], 0\n"
    "s_waitcnt lgkmcnt(6)\n"
    "v_alignbit_b32 v92, v124, v143, v6\n"
    "v_alignbit_b32 v93, v125, v124, v6\n"
    "v_mfma_f32_16x16x32_f16 v[24:27], v[84:87], v[56:59], 0\n"
    "v_alignbit_b32 v94, v126, v125, v6\n"
    "v_alignbit_b32 v95, v127, v126, v6\n"
    "v_mfma_f32_16x16x32_f16 v[24:27], v[88:91], v[52:55], v[24:27]\n"
    "s_waitcnt lgkmcnt(5)\n"
    "ds_read_b64 v[132:133], v4 offset:1556\n"
    "s_add_u32 m0, s55, 7168\n"
    "v_mfma_f32_16x16x32_f16 v[28:31], v[84:87], v[60:63], 0\n"
    "buffer_load_dwordx4 v247, s[36:39], s13 offen offset:3072 lds\n"
    "v_mfma_f32_16x16x32_f16 v[28:31], v[88:91], v[56:59], v[28:31]\n"
    "v_alignbit_b32 v96, v128, v147, v6\n"
    "v_alignbit_b32 v97, v129, v128, v6\n"
    "v_mfma_f32_16x16x32_f16 v[28:31], v[92:95], v[52:55], v[28:31]\n"
    "s_waitcnt lgkmcnt(5)\n"
    "v_and_b32 v10, 1, v2\n"
    "v_alignbit_b32 v98, v130, v129, v6\n"
    "v_mfma_f32_16x16x32_f16 v[32:35], v[84:87], v[64:67], 0\n"
    "ds_read_b64 v[134:135], v4 offset:1564\n"
    "v_alignbit_b32 v99, v131, v130, v6\n"
    "v_mfma_f32_16x16x32_f16 v[32:35], v[88:91], v[60:63], v[32:35]\n"
    "v_mul_u32_u24 v19, 112, v2\n"
    "s_add_u32 m0, s55, 11264\n"
    "v_mfma_f32_16x16x32_f16 v[32:35], v[92:95], v[56:59], v[32:35]\n"
    "buffer_load_dwordx4 v248, s[36:39], s13 offen offset:0 lds\n"
    "v_mfma_f32_16x16x32_f16 v[32:35], v[96:99], v[52:55], v[32:35]\n"
    "s_waitcnt lgkmcnt(5)\n"
    "ds_read_b64 v[136:137], v4 offset:1044\n"
    "v_lshrrev_b32 v12, 1, v2\n"
    "v_mfma_f32_16x16x32_f16 v[36:39], v[84:87], v[68:71], 0\n"
    "ds_read_b64 v[138:139], v4 offset:1052\n"
    "v_sub_u32_e64 v19, v4, v19\n"
    "v_mfma_f32_16x16x32_f16 v[36:39], v[88:91], v[64:67], v[36:39]\n"
    "ds_read_b128 v[52:55], v5 offset:2368\n"
    "v_mul_u32_u24 v11, 2368, v10\n"
    "v_mfma_f32_16x16x32_f16 v[36:39], v[92:95], v[60:63], v[36:39]\n"
    "ds_read_b64 v[140:141], v4 offset:532\n"
    "v_add_u32 v9, -64, v19\n"
    "v_mfma_f32_16x16x32_f16 v[36:39], v[96:99], v[56:59], v[36:39]\n"
    "s_waitcnt lgkmcnt(5)\n"
    "ds_read_b64 v[142:143], v4 offset:540\n"
    "v_alignbit_b32 v148, v132, v119, v6\n"
    "v_mfma_f32_16x16x32_f16 v[40:43], v[84:87], v[72:75], 0\n"
    "buffer_load_dwordx4 v249, s[36:39], s13 offen offset:1024 lds\n"
    "v_mfma_f32_16x16x32_f16 v[40:43], v[88:91], v[68:71], v[40:43]\n"
    "ds_read_b128 v[56:59], v5 offset:2624\n"
    "v_alignbit_b32 v149, v133, v132, v6\n"
    "v_mfma_f32_16x16x32_f16 v[40:43], v[92:95], v[64:67], v[40:43]\n"
    "s_waitcnt lgkmcnt(6)\n"
    "ds_read_b64 v[144:145], v4 offset:20\n"
    "v_alignbit_b32 v150, v134, v133, v6\n"
    "v_mfma_f32_16x16x32_f16 v[40:43], v[96:99], v[60:63], v[40:43]\n"
    "ds_read_b128 v[60:63], v5 offset:2880\n"
    "v_alignbit_b32 v151, v135, v134, v6\n"
    "v_mfma_f32_16x16x32_f16 v[44:47], v[84:87], v[76:79], 0\n"
    "ds_read_b64 v[146:147], v4 offset:28\n"
    "v_add_u32_e64 v12, v12, v1\n"
    "v_mfma_f32_16x16x32_f16 v[44:47], v[88:91], v[72:75], v[44:47]\n"
    "s_waitcnt lgkmcnt(8)\n"
    "ds_read_b64 v[116:117], v4 offset:1572\n"
    "v_alignbit_b32 v152, v136, v123, v6\n"
    "v_mfma_f32_16x16x32_f16 v[44:47], v[92:95], v[68:71], v[44:47]\n"
    "buffer_load_dwordx4 v250, s[36:39], s13 offen offset:2048 lds\n"
    "v_mfma_f32_16x16x32_f16 v[44:47], v[96:99], v[64:67], v[44:47]\n"
    "ds_read_b128 v[64:67], v5 offset:3136\n"
    "v_alignbit_b32 v153, v137, v136, v6\n"
    "v_mfma_f32_16x16x32_f16 v[48:51], v[84:87], v[80:83], 0\n"
    "s_waitcnt lgkmcnt(9)\n"
    "ds_read_b64 v[118:119], v4 offset:1580\n"
    "v_alignbit_b32 v154, v138, v137, v6\n"
    "v_mfma_f32_16x16x32_f16 v[48:51], v[88:91], v[76:79], v[48:51]\n"
    "ds_read_b128 v[76:79], v5 offset:3904\n"
    "v_alignbit_b32 v155, v139, v138, v6\n"
    "v_mfma_f32_16x16x32_f16 v[48:51], v[92:95], v[72:75], v[48:51]\n"
    "ds_read_b64 v[120:121], v4 offset:1060\n"
    "v_lshlrev_b32_e64 v19, 2, v2\n"
    "v_mfma_f32_16x16x32_f16 v[48:51], v[96:99], v[68:71], v[48:51]\n"
    "s_waitcnt lgkmcnt(11)\n"
    "ds_read_b128 v[68:71], v5 offset:3392\n"
    "s_add_u32 m0, s55, 14336\n"
    "v_mfma_f32_16x16x32_f16 v[20:23], v[148:151], v[52:55], v[20:23]\n"
    "s_waitcnt lgkmcnt(9)\n"
    "buffer_load_dwordx4 v244, s[36:39], s14 offen offset:0 lds\n"
    "v_mfma_f32_16x16x32_f16 v[24:27], v[148:151], v[56:59], v[24:27]\n"
    "ds_read_b128 v[72:75], v5 offset:3648\n"
    "v_alignbit_b32 v156, v140, v127, v6\n"
    "v_mfma_f32_16x16x32_f16 v[24:27], v[152:155], v[52:55], v[24:27]\n"
    "s_waitcnt lgkmcnt(8)\n"
    "v_alignbit_b32 v157, v141, v140, v6\n"
    "v_alignbit_b32 v158, v142, v141, v6\n"
    "v_mfma_f32_16x16x32_f16 v[28:31], v[148:151], v[60:63], v[28:31]\n"
    "v_alignbit_b32 v159, v143, v142, v6\n"
    "s_add_u32 s58, s55, 36048\n"
    "v_mfma_f32_16x16x32_f16 v[28:31], v[152:155], v[56:59], v[28:31]\n"
    "v_alignbit_b32 v160, v144, v131, v6\n"
    "v_alignbit_b32 v161, v145, v144, v6\n"
    "v_mfma_f32_16x16x32_f16 v[28:31], v[156:159], v[52:55], v[28:31]\n"
    "s_waitcnt lgkmcnt(5)\n"
    "v_alignbit_b32 v162, v146, v145, v6\n"
    "v_alignbit_b32 v163, v147, v146, v6\n"
    "v_mfma_f32_16x16x32_f16 v[32:35], v[148:151], v[64:67], v[32:35]\n"
    "ds_read_b128 v[80:83], v5 offset:4160\n"
    "v_sub_u32_e64 v19, v19, v1\n"
    "v_mfma_f32_16x16x32_f16 v[32:35], v[152:155], v[60:63], v[32:35]\n"
    "buffer_load_dwordx4 v245, s[36:39], s14 offen offset:1024 lds\n"
    "v_mfma_f32_16x16x32_f16 v[32:35], v[156:159], v[56:59], v[32:35]\n"
    "ds_read_b64 v[122:123], v4 offset:1068\n"
    "v_lshl_add_u32 v12, v12, 4, s58\n"
    "v_mfma_f32_16x16x32_f16 v[32:35], v[160:163], v[52:55], v[32:35]\n"
    "s_waitcnt lgkmcnt(3)\n"
    "ds_read_b128 v[52:55], v5 offset:16\n"
    "s_add_u32 s59, s55, 33340\n"
    "v_mfma_f32_16x16x32_f16 v[36:39], v[148:151], v[68:71], v[36:39]\n"
    "ds_read_b64 v[124:125], v4 offset:548\n"
    "v_mul_u32_u24 v10, 2352, v10\n"
    "v_mfma_f32_16x16x32_f16 v[36:39], v[152:155], v[64:67], v[36:39]\n"
    "ds_read_b64 v[126:127], v4 offset:556\n"
    "v_lshl_add_u32 v254, v19, 2, s59\n"
    "v_mfma_f32_16x16x32_f16 v[36:39], v[156:159], v[60:63], v[36:39]\n"
    "buffer_load_dwordx4 v246, s[36:39], s14 offen offset:2048 lds\n"
    "v_mfma_f32_16x16x32_f16 v[36:39], v[160:163], v[56:59], v[36:39]\n"
    "s_waitcnt lgkmcnt(5)\n"
    "ds_read_b128 v[56:59], v5 offset:272\n"
    "v_add3_u32 v7, v12, v11, 16\n"
    "v_mfma_f32_16x16x32_f16 v[40:43], v[148:151], v[72:75], v[40:43]\n"
    "ds_read_b64 v[128:129], v4 offset:36\n"
    "v_alignbit_b32 v84, v116, v135, v6\n"
    "v_mfma_f32_16x16x32_f16 v[40:43], v[152:155], v[68:71], v[40:43]\n"
    "ds_read_b64 v[130:131], v4 offset:44\n"
    "v_alignbit_b32 v85, v117, v116, v6\n"
    "v_mfma_f32_16x16x32_f16 v[40:43], v[156:159], v[64:67], v[40:43]\n"
    "v_alignbit_b32 v86, v118, v117, v6\n"
    "v_alignbit_b32 v87, v119, v118, v6\n"
    "v_mfma_f32_16x16x32_f16 v[40:43], v[160:163], v[60:63], v[40:43]\n"
    "ds_read_b128 v[60:63], v5 offset:528\n"
    "v_alignbit_b32 v88, v120, v139, v6\n"
    "v_mfma_f32_16x16x32_f16 v[44:47], v[148:151], v[76:79], v[44:47]\n"
    "buffer_load_dwordx4 v247, s[36:39], s14 offen offset:3072 lds\n"
    "v_mfma_f32_16x16x32_f16 v[44:47], v[152:155], v[72:75], v[44:47]\n"
    "ds_read_b64 v[132:133], v4 offset:1588\n"
    "v_alignbit_b32 v89, v121, v120, v6\n"
    "v_mfma_f32_16x16x32_f16 v[44:47], v[156:159], v[68:71], v[44:47]\n"
    "ds_read_b64 v[134:135], v4 offset:1596\n"
    "v_sub_u32_e64 v12, v12, v10\n"
    "v_mfma_f32_16x16x32_f16 v[44:47], v[160:163], v[64:67], v[44:47]\n"
    "s_waitcnt lgkmcnt(10)\n"
    "ds_read_b128 v[64:67], v5 offset:784\n"
    "s_add_u32 m0, s55, 18432\n"
    "v_mfma_f32_16x16x32_f16 v[48:51], v[148:151], v[80:83], v[48:51]\n"
    "s_waitcnt lgkmcnt(10)\n"
    "v_alignbit_b32 v90, v122, v121, v6\n"
    "v_alignbit_b32 v91, v123, v122, v6\n"
    "v_mfma_f32_16x16x32_f16 v[48:51], v[152:155], v[76:79], v[48:51]\n"
    "s_waitcnt lgkmcnt(8)\n"
    "ds_read_b128 v[76:79], v5 offset:1552\n"
    "v_alignbit_b32 v92, v124, v143, v6\n"
    "v_mfma_f32_16x16x32_f16 v[48:51], v[156:159], v[72:75], v[48:51]\n"
    "buffer_load_dwordx4 v248, s[36:39], s14 offen offset:0 lds\n"
    "v_mfma_f32_16x16x32_f16 v[48:51], v[160:163], v[68:71], v[48:51]\n"
    "ds_read_b128 v[68:71], v5 offset:1040\n"
    "v_alignbit_b32 v93, v125, v124, v6\n"
    "v_mfma_f32_16x16x32_f16 v[20:23], v[84:87], v[52:55], v[20:23]\n"
    "s_waitcnt lgkmcnt(8)\n"
    "ds_read_b64 v[136:137], v4 offset:1076\n"
    "v_add_u32 v8, 2368, v12\n"
    "v_mfma_f32_16x16x32_f16 v[24:27], v[84:87], v[56:59], v[24:27]\n"
    "ds_read_b128 v[72:75], v5 offset:1296\n"
    "v_alignbit_b32 v94, v126, v125, v6\n"
    "v_mfma_f32_16x16x32_f16 v[24:27], v[88:91], v[52:55], v[24:27]\n"
    "s_waitcnt lgkmcnt(7)\n"
    "ds_read_b32 v181, v9\n"
    "v_alignbit_b32 v95, v127, v126, v6\n"
    "v_mfma_f32_16x16x32_f16 v[28:31], v[84:87], v[60:63], v[28:31]\n"
    "ds_read_b64 v[182:183], v9 offset:4\n"
    "v_alignbit_b32 v96, v128, v147, v6\n"
    "v_mfma_f32_16x16x32_f16 v[28:31], v[88:91], v[56:59], v[28:31]\n"
    "buffer_load_dwordx4 v249, s[36:39], s14 offen offset:1024 lds\n"
    "v_mfma_f32_16x16x32_f16 v[28:31], v[92:95], v[52:55], v[28:31]\n"
    "s_waitcnt lgkmcnt(6)\n"
    "ds_read_b64 v[184:185], v9 offset:12\n"
    "v_alignbit_b32 v97, v129, v128, v6\n"
    "v_mfma_f32_16x16x32_f16 v[32:35], v[84:87], v[64:67], v[32:35]\n"
    "ds_read_b128 v[80:83], v5 offset:1808\n"
    "v_alignbit_b32 v98, v130, v129, v6\n"
    "v_mfma_f32_16x16x32_f16 v[32:35], v[88:91], v[60:63], v[32:35]\n"
    "ds_read_b64 v[138:139], v4 offset:1084\n"
    "v_alignbit_b32 v99, v131, v130, v6\n"
    "v_mfma_f32_16x16x32_f16 v[32:35], v[92:95], v[56:59], v[32:35]\n"
    "ds_read2_b32 v[164:165], v254 offset0:48 offset1:49\n"
    "v_alignbit_b32 v148, v132, v119, v6\n"
    "v_mfma_f32_16x16x32_f16 v[32:35], v[96:99], v[52:55], v[32:35]\n"
    "s_waitcnt lgkmcnt(8)\n"
    "ds_read_b64 v[140:141], v4 offset:564\n"
    "v_alignbit_b32 v149, v133, v132, v6\n"
    "v_mfma_f32_16x16x32_f16 v[36:39], v[84:87], v[68:71], v[36:39]\n"
    "buffer_load_dwordx4 v250, s[36:39], s14 offen offset:2048 lds\n"
    "v_mfma_f32_16x16x32_f16 v[36:39], v[88:91], v[64:67], v[36:39]\n"
    "ds_read_b128 v[236:239], v7 offset:0\n"
    "s_add_u32 m0, s55, 21504\n"
    "v_mfma_f32_16x16x32_f16 v[36:39], v[92:95], v[60:63], v[36:39]\n"
    "s_waitcnt lgkmcnt(6)\n"
    "ds_read_b64 v[142:143], v4 offset:572\n"
    "v_alignbit_b32 v192, v182, v181, v6\n"
    "v_mfma_f32_16x16x32_f16 v[36:39], v[96:99], v[56:59], v[36:39]\n"
    "ds_read_b128 v[240:243], v8 offset:0\n"
    "v_alignbit_b32 v193, v183, v182, v6\n"
    "v_mfma_f32_16x16x32_f16 v[40:43], v[84:87], v[72:75], v[40:43]\n"
    "s_waitcnt lgkmcnt(7)\n"
    "ds_read_b64 v[144:145], v4 offset:52\n"
    "v_alignbit_b32 v194, v184, v183, v6\n"
    "v_mfma_f32_16x16x32_f16 v[40:43], v[88:91], v[68:71], v[40:43]\n"
    "ds_read2_b32 v[166:167], v254 offset0:50 offset1:51\n"
    "v_alignbit_b32 v195, v185, v184, v6\n"
    "v_mfma_f32_16x16x32_f16 v[40:43], v[92:95], v[64:67], v[40:43]\n"
    "buffer_load_dwordx4 v244, s[36:39], s15 offen offset:0 lds\n"
    "v_mfma_f32_16x16x32_f16 v[40:43], v[96:99], v[60:63], v[40:43]\n"
    "ds_read_b128 v[52:55], v5 offset:2384\n"
    "v_alignbit_b32 v150, v134, v133, v6\n"
    "v_mfma_f32_16x16x32_f16 v[44:47], v[84:87], v[76:79], v[44:47]\n"
    "ds_read_b64 v[146:147], v4 offset:60\n"
    "v_alignbit_b32 v151, v135, v134, v6\n"
    "v_mfma_f32_16x16x32_f16 v[44:47], v[88:91], v[72:75], v[44:47]\n"
    "ds_read_b128 v[56:59], v5 offset:2640\n"
    "v_alignbit_b32 v152, v136, v123, v6\n"
    "v_mfma_f32_16x16x32_f16 v[44:47], v[92:95], v[68:71], v[44:47]\n"
    "ds_read_b64 v[116:117], v4 offset:1604\n"
    "v_alignbit_b32 v153, v137, v136, v6\n"
    "v_mfma_f32_16x16x32_f16 v[44:47], v[96:99], v[64:67], v[44:47]\n"
    "s_waitcnt lgkmcnt(11)\n"
    "ds_read2_b32 v[168:169], v254 offset0:32 offset1:33\n"
    "v_alignbit_b32 v154, v138, v137, v6\n"
    "v_mfma_f32_16x16x32_f16 v[48:51], v[84:87], v[80:83], v[48:51]\n"
    "buffer_load_dwordx4 v245, s[36:39], s15 offen offset:1024 lds\n"
    "v_mfma_f32_16x16x32_f16 v[48:51], v[88:91], v[76:79], v[48:51]\n"
    "ds_read_b128 v[60:63], v5 offset:2896\n"
    "v_alignbit_b32 v155, v139, v138, v6\n"
    "v_mfma_f32_16x16x32_f16 v[48:51], v[92:95], v[72:75], v[48:51]\n"
    "s_waitcnt lgkmcnt(11)\n"
    "ds_read2_b32 v[170:171], v254 offset0:34 offset1:35\n"
    "v_alignbit_b32 v156, v140, v127, v6\n"
    "v_mfma_f32_16x16x32_f16 v[48:51], v[96:99], v[68:71], v[48:51]\n"
    "s_waitcnt lgkmcnt(9)\n"
    "ds_read_b128 v[64:67], v5 offset:3152\n"
    "v_alignbit_b32 v157, v141, v140, v6\n"
    "v_mfma_f32_16x16x32_f16 v[180:183], v[240:243], v[202:205], 0\n"
    "ds_read_b64 v[118:119], v4 offset:1612\n"
    "v_alignbit_b32 v158, v142, v141, v6\n"
    "v_mfma_f32_16x16x32_f16 v[184:187], v[236:239], v[206:209], 0\n"
    "ds_read2_b32 v[172:173], v254 offset0:16 offset1:17\n"
    "v_alignbit_b32 v159, v143, v142, v6\n"
    "v_mfma_f32_16x16x32_f16 v[36:39], v[192:195], v[236:239], v[36:39]\n"
    "s_waitcnt lgkmcnt(9)\n"
    "buffer_load_dwordx3 v[16:18], v15, s[32:35], 0 offen offset:1024 sc1\n"
    "v_mfma_f32_16x16x32_f16 v[20:23], v[148:151], v[52:55], v[20:23]\n"
    "s_waitcnt lgkmcnt(7)\n"
    "buffer_load_dwordx4 v246, s[36:39], s15 offen offset:2048 lds\n"
    "v_mfma_f32_16x16x32_f16 v[24:27], v[148:151], v[56:59], v[24:27]\n"
    "ds_read_b128 v[68:71], v5 offset:3408\n"
    "v_alignbit_b32 v160, v144, v131, v6\n"
    "v_mfma_f32_16x16x32_f16 v[24:27], v[152:155], v[52:55], v[24:27]\n"
    "s_waitcnt lgkmcnt(5)\n"
    "ds_read2_b32 v[174:175], v254 offset0:18 offset1:19\n"
    "v_alignbit_b32 v161, v145, v144, v6\n"
    "v_mfma_f32_16x16x32_f16 v[28:31], v[148:151], v[60:63], v[28:31]\n"
    "ds_read_b64 v[120:121], v4 offset:1092\n"
    "v_alignbit_b32 v162, v146, v145, v6\n"
    "v_mfma_f32_16x16x32_f16 v[28:31], v[152:155], v[56:59], v[28:31]\n"
    "ds_read_b128 v[72:75], v5 offset:3664\n"
    "v_alignbit_b32 v163, v147, v146, v6\n"
    "v_mfma_f32_16x16x32_f16 v[28:31], v[156:159], v[52:55], v[28:31]\n"
    "s_waitcnt lgkmcnt(6)\n"
    "buffer_load_dwordx4 v247, s[36:39], s15 offen offset:3072 lds\n"
    "v_mfma_f32_16x16x32_f16 v[32:35], v[148:151], v[64:67], v[32:35]\n"
    "ds_read2_b32 v[176:177], v254 offset0:0 offset1:1\n"
    "s_add_u32 m0, s55, 25600\n"
    "v_mfma_f32_16x16x32_f16 v[32:35], v[152:155], v[60:63], v[32:35]\n"
    "ds_read2_b32 v[178:179], v254 offset0:2 offset1:3\n"
    "v_cvt_pk_f16_f32 v188, v180, v184\n"
    "v_mfma_f32_16x16x32_f16 v[32:35], v[156:159], v[56:59], v[32:35]\n"
    "ds_read_b128 v[76:79], v5 offset:3920\n"
    "v_cvt_pk_f16_f32 v189, v181, v185\n"
    "v_mfma_f32_16x16x32_f16 v[32:35], v[160:163], v[52:55], v[32:35]\n"
    "s_waitcnt lgkmcnt(6)\n"
    "ds_read_b64 v[122:123], v4 offset:1100\n"
    "v_cvt_pk_f16_f32 v190, v182, v186\n"
    "v_mfma_f32_16x16x32_f16 v[36:39], v[148:151], v[68:71], v[36:39]\n"
    "ds_read_b128 v[80:83], v5 offset:4176\n"
    "v_cvt_pk_f16_f32 v191, v183, v187\n"
    "v_mfma_f32_16x16x32_f16 v[36:39], v[152:155], v[64:67], v[36:39]\n"
    "buffer_load_dwordx4 v248, s[36:39], s15 offen offset:0 lds\n"
    "v_mfma_f32_16x16x32_f16 v[36:39], v[156:159], v[60:63], v[36:39]\n"
    "ds_read_b128 v[236:239], v7 offset:256\n"
    "v_alignbit_b32 v84, v116, v135, v6\n"
    "v_mfma_f32_16x16x32_f16 v[36:39], v[160:163], v[56:59], v[36:39]\n"
    "s_waitcnt lgkmcnt(6)\n"
    "ds_read_b64 v[124:125], v4 offset:580\n"
    "v_alignbit_b32 v85, v117, v116, v6\n"
    "v_mfma_f32_16x16x32_f16 v[40:43], v[148:151], v[72:75], v[40:43]\n"
    "ds_read_b128 v[240:243], v8 offset:256\n"
    "v_alignbit_b32 v86, v118, v117, v6\n"
    "v_mfma_f32_16x16x32_f16 v[40:43], v[152:155], v[68:71], v[40:43]\n"
    "ds_read_b64 v[126:127], v4 offset:588\n"
    "v_alignbit_b32 v87, v119, v118, v6\n"
    "v_mfma_f32_16x16x32_f16 v[40:43], v[156:159], v[64:67], v[40:43]\n"
    "ds_read_b128 v[52:55], v5 offset:32\n"
    "v_alignbit_b32 v88, v120, v139, v6\n"
    "v_mfma_f32_16x16x32_f16 v[40:43], v[160:163], v[60:63], v[40:43]\n"
    "s_waitcnt lgkmcnt(7)\n"
    "buffer_load_dwordx4 v249, s[36:39], s15 offen offset:1024 lds\n"
    "v_mfma_f32_16x16x32_f16 v[44:47], v[148:151], v[76:79], v[44:47]\n"
    "ds_read_b128 v[56:59], v5 offset:288\n"
    "v_alignbit_b32 v89, v121, v120, v6\n"
    "v_mfma_f32_16x16x32_f16 v[44:47], v[152:155], v[72:75], v[44:47]\n"
    "s_waitcnt lgkmcnt(7)\n"
    "ds_read_b64 v[128:129], v4 offset:68\n"
    "v_alignbit_b32 v90, v122, v121, v6\n"
    "v_mfma_f32_16x16x32_f16 v[44:47], v[156:159], v[68:71], v[44:47]\n"
    "ds_read_b128 v[60:63], v5 offset:544\n"
    "v_alignbit_b32 v91, v123, v122, v6\n"
    "v_mfma_f32_16x16x32_f16 v[44:47], v[160:163], v[64:67], v[44:47]\n"
    "s_waitcnt lgkmcnt(6)\n"
    "ds_read_b64 v[130:131], v4 offset:76\n"
    "v_alignbit_b32 v92, v124, v143, v6\n"
    "v_mfma_f32_16x16x32_f16 v[48:51], v[148:151], v[80:83], v[48:51]\n"
    "ds_read_b128 v[64:67], v5 offset:800\n"
    "v_alignbit_b32 v93, v125, v124, v6\n"
    "v_mfma_f32_16x16x32_f16 v[48:51], v[152:155], v[76:79], v[48:51]\n"
    "buffer_load_dwordx4 v250, s[36:39], s15 offen offset:2048 lds\n"
    "v_mfma_f32_16x16x32_f16 v[48:51], v[156:159], v[72:75], v[48:51]\n"
    "s_waitcnt lgkmcnt(6)\n"
    "ds_read_b128 v[72:75], v5 offset:1312\n"
    "v_alignbit_b32 v94, v126, v125, v6\n"
    "v_mfma_f32_16x16x32_f16 v[48:51], v[160:163], v[68:71], v[48:51]\n"
    "s_waitcnt vmcnt(5)\n"
    "ds_read_b64 v[132:133], v4 offset:1620\n"
    "v_xor_b32 v16, s51, v16\n"
    "v_mfma_f32_16x16x32_f16 v[36:39], v[188:191], v[164:167], v[36:39]\n"
    "ds_read_b128 v[68:71], v5 offset:1056\n"
    "v_bitop3_b32 v16, v18, s51, v16 bitop3:0xbe\n"
    "v_mfma_f32_16x16x32_f16 v[40:43], v[188:191], v[168:171], v[40:43]\n"
    "ds_read_b64 v[134:135], v4 offset:1628\n"
    "v_cmp_ne_u32_e64 vcc, 0, v16\n"
    "v_mfma_f32_16x16x32_f16 v[44:47], v[188:191], v[172:175], v[44:47]\n"
    "s_cbranch_vccnz .Lk_ool_5\n"
    ".Lk_ret_6:\n"
    "v_alignbit_b32 v95, v127, v126, v6\n"
    "v_mfma_f32_16x16x32_f16 v[48:51], v[188:191], v[176:179], v[48:51]\n"
    "ds_read_b128 v[76:79], v5 offset:1568\n"
    "s_add_u32 s3, s62, 0\n"
    "v_mfma_f32_16x16x32_f16 v[180:183], v[240:243], v[202:205], 0\n"
    "s_waitcnt lgkmcnt(8)\n"
    "ds_read_b64 v[136:137], v4 offset:1108\n"
    "v_alignbit_b32 v96, v128, v147, v6\n"
    "v_mfma_f32_16x16x32_f16 v[184:187], v[236:239], v[206:209], 0\n"
    "buffer_load_dwordx4 a[0:3], v251, s[32:35], s3 offen offset:0 sc1\n"
    "v_mfma_f32_16x16x32_f16 v[40:43], v[192:195], v[236:239], v[40:43]\n"
    "ds_read_b128 v[80:83], v5 offset:1824\n"
    "v_alignbit_b32 v97, v129, v128, v6\n"
    "v_mfma_f32_16x16x32_f16 v[20:23], v[84:87], v[52:55], v[20:23]\n"
    "s_waitcnt lgkmcnt(8)\n"
    "ds_read_b64 v[138:139], v4 offset:1116\n"
    "v_alignbit_b32 v98, v130, v129, v6\n"
    "v_mfma_f32_16x16x32_f16 v[24:27], v[84:87], v[56:59], v[24:27]\n"
    "ds_read_b128 v[236:239], v7 offset:512\n"
    "v_alignbit_b32 v99, v131, v130, v6\n"
    "v_mfma_f32_16x16x32_f16 v[24:27], v[88:91], v[52:55], v[24:27]\n"
    "buffer_load_dwordx4 a[4:7], v251, s[32:35], s3 offen offset:1024 sc1\n"
    "v_mfma_f32_16x16x32_f16 v[28:31], v[84:87], v[60:63], v[28:31]\n"
    "ds_read_b128 v[240:243], v8 offset:512\n"
    "v_cvt_pk_f16_f32 v188, v180, v184\n"
    "v_mfma_f32_16x16x32_f16 v[28:31], v[88:91], v[56:59], v[28:31]\n"
    "ds_read_b64 v[140:141], v4 offset:596\n"
    "v_cvt_pk_f16_f32 v189, v181, v185\n"
    "v_mfma_f32_16x16x32_f16 v[28:31], v[92:95], v[52:55], v[28:31]\n"
    "s_waitcnt lgkmcnt(11)\n"
    "ds_read_b64 v[142:143], v4 offset:604\n"
    "v_cvt_pk_f16_f32 v190, v182, v186\n"
    "v_mfma_f32_16x16x32_f16 v[32:35], v[84:87], v[64:67], v[32:35]\n"
    "buffer_load_dwordx4 a[8:11], v251, s[32:35], s3 offen offset:2048 sc1\n"
    "v_mfma_f32_16x16x32_f16 v[32:35], v[88:91], v[60:63], v[32:35]\n"
    "ds_read_b64 v[144:145], v4 offset:84\n"
    "v_cvt_pk_f16_f32 v191, v183, v187\n"
    "v_mfma_f32_16x16x32_f16 v[32:35], v[92:95], v[56:59], v[32:35]\n"
    "s_waitcnt lgkmcnt(11)\n"
    "ds_read_b64 v[146:147], v4 offset:92\n"
    "v_alignbit_b32 v148, v132, v119, v6\n"
    "v_mfma_f32_16x16x32_f16 v[32:35], v[96:99], v[52:55], v[32:35]\n"
    "s_waitcnt lgkmcnt(11)\n"
    "ds_read_b128 v[52:55], v5 offset:2400\n"
    "v_alignbit_b32 v149, v133, v132, v6\n"
    "v_mfma_f32_16x16x32_f16 v[36:39], v[84:87], v[68:71], v[36:39]\n"
    "buffer_load_dwordx4 a[12:15], v251, s[32:35], s3 offen offset:3072 sc1\n"
    "v_mfma_f32_16x16x32_f16 v[36:39], v[88:91], v[64:67], v[36:39]\n"
    "s_waitcnt lgkmcnt(11)\n"
    "s_add_u32 s3, s62, 4096\n"
    "v_alignbit_b32 v150, v134, v133, v6\n"
    "v_mfma_f32_16x16x32_f16 v[36:39], v[92:95], v[60:63], v[36:39]\n"
    "s_waitcnt lgkmcnt(9)\n"
    "v_alignbit_b32 v151, v135, v134, v6\n"
    "v_alignbit_b32 v152, v136, v123, v6\n"
    "v_mfma_f32_16x16x32_f16 v[36:39], v[96:99], v[56:59], v[36:39]\n"
    "ds_read_b128 v[56:59], v5 offset:2656\n"
    "v_alignbit_b32 v153, v137, v136, v6\n"
    "v_mfma_f32_16x16x32_f16 v[40:43], v[84:87], v[72:75], v[40:43]\n"
    "buffer_load_dwordx4 a[16:19], v251, s[32:35], s3 offen offset:0 sc1\n"
    "v_mfma_f32_16x16x32_f16 v[40:43], v[88:91], v[68:71], v[40:43]\n"
    "s_waitcnt lgkmcnt(8)\n"
    "ds_read_b64 v[116:117], v4 offset:1636\n"
    "v_alignbit_b32 v154, v138, v137, v6\n"
    "v_mfma_f32_16x16x32_f16 v[40:43], v[92:95], v[64:67], v[40:43]\n"
    "ds_read_b64 v[118:119], v4 offset:1644\n"
    "v_alignbit_b32 v155, v139, v138, v6\n"
    "v_mfma_f32_16x16x32_f16 v[40:43], v[96:99], v[60:63], v[40:43]\n"
    "s_waitcnt lgkmcnt(7)\n"
    "ds_read_b128 v[60:63], v5 offset:2912\n"
    "v_alignbit_b32 v156, v140, v127, v6\n"
    "v_mfma_f32_16x16x32_f16 v[44:47], v[84:87], v[76:79], v[44:47]\n"
    "buffer_load_dwordx4 a[20:23], v251, s[32:35], s3 offen offset:1024 sc1\n"
    "v_mfma_f32_16x16x32_f16 v[44:47], v[88:91], v[72:75], v[44:47]\n"
    "ds_read_b64 v[120:121], v4 offset:1124\n"
    "v_alignbit_b32 v157, v141, v140, v6\n"
    "v_mfma_f32_16x16x32_f16 v[44:47], v[92:95], v[68:71], v[44:47]\n"
    "s_waitcnt lgkmcnt(8)\n"
    "ds_read_b64 v[122:123], v4 offset:1132\n"
    "v_alignbit_b32 v158, v142, v141, v6\n"
    "v_mfma_f32_16x16x32_f16 v[44:47], v[96:99], v[64:67], v[44:47]\n"
    "ds_read_b128 v[64:67], v5 offset:3168\n"
    "v_alignbit_b32 v159, v143, v142, v6\n"
    "v_mfma_f32_16x16x32_f16 v[48:51], v[84:87], v[80:83], v[48:51]\n"
    "buffer_load_dwordx4 a[24:27], v251, s[32:35], s3 offen offset:2048 sc1\n"
    "v_mfma_f32_16x16x32_f16 v[48:51], v[88:91], v[76:79], v[48:51]\n"
    "s_waitcnt lgkmcnt(9)\n"
    "ds_read_b128 v[76:79], v5 offset:3936\n"
    "v_alignbit_b32 v160, v144, v131, v6\n"
    "v_mfma_f32_16x16x32_f16 v[48:51], v[92:95], v[72:75], v[48:51]\n"
    "ds_read_b64 v[124:125], v4 offset:612\n"
    "v_alignbit_b32 v161, v145, v144, v6\n"
    "v_mfma_f32_16x16x32_f16 v[48:51], v[96:99], v[68:71], v[48:51]\n"
    "s_waitcnt lgkmcnt(10)\n"
    "ds_read_b128 v[68:71], v5 offset:3424\n"
    "v_alignbit_b32 v162, v146, v145, v6\n"
    "v_mfma_f32_16x16x32_f16 v[40:43], v[188:191], v[164:167], v[40:43]\n"
    "buffer_load_dwordx4 a[28:31], v251, s[32:35], s3 offen offset:3072 sc1\n"
    "v_mfma_f32_16x16x32_f16 v[44:47], v[188:191], v[168:171], v[44:47]\n"
    "ds_read_b128 v[72:75], v5 offset:3680\n"
    "s_add_u32 s3, s62, 8192\n"
    "v_mfma_f32_16x16x32_f16 v[48:51], v[188:191], v[172:175], v[48:51]\n"
    "ds_read_b64 v[126:127], v4 offset:620\n"
    "v_alignbit_b32 v163, v147, v146, v6\n"
    "v_mfma_f32_16x16x32_f16 v[180:183], v[240:243], v[202:205], 0\n"
    "s_waitcnt lgkmcnt(10)\n"
    "ds_read_b128 v[80:83], v5 offset:4192\n"
    "v_alignbit_b32 v84, v116, v135, v6\n"
    "v_mfma_f32_16x16x32_f16 v[184:187], v[236:239], v[206:209], 0\n"
    "buffer_load_dwordx4 a[32:35], v251, s[32:35], s3 offen offset:0 sc1\n"
    "v_mfma_f32_16x16x32_f16 v[44:47], v[192:195], v[236:239], v[44:47]\n"
    "ds_read_b128 v[236:239], v7 offset:768\n"
    "v_alignbit_b32 v85, v117, v116, v6\n"
    "v_mfma_f32_16x16x32_f16 v[20:23], v[148:151], v[52:55], v[20:23]\n"
    "s_waitcnt lgkmcnt(11)\n"
    "ds_read_b64 v[128:129], v4 offset:100\n"
    "v_alignbit_b32 v86, v118, v117, v6\n"
    "v_mfma_f32_16x16x32_f16 v[24:27], v[148:151], v[56:59], v[24:27]\n"
    "ds_read_b128 v[240:243], v8 offset:768\n"
    "v_alignbit_b32 v87, v119, v118, v6\n"
    "v_mfma_f32_16x16x32_f16 v[24:27], v[152:155], v[52:55], v[24:27]\n"
    "s_waitcnt lgkmcnt(12)\n"
    "buffer_load_dwordx4 a[36:39], v251, s[32:35], s3 offen offset:1024 sc1\n"
    "v_mfma_f32_16x16x32_f16 v[28:31], v[148:151], v[60:63], v[28:31]\n"
    "ds_read_b64 v[130:131], v4 offset:108\n"
    "v_cvt_pk_f16_f32 v188, v180, v184\n"
    "v_mfma_f32_16x16x32_f16 v[28:31], v[152:155], v[56:59], v[28:31]\n"
    "ds_read_b64 v[132:133], v4 offset:1652\n"
    "v_cvt_pk_f16_f32 v189, v181, v185\n"
    "v_mfma_f32_16x16x32_f16 v[28:31], v[156:159], v[52:55], v[28:31]\n"
    "s_waitcnt lgkmcnt(11)\n"
    "ds_read_b64 v[134:135], v4 offset:1660\n"
    "v_cvt_pk_f16_f32 v190, v182, v186\n"
    "v_mfma_f32_16x16x32_f16 v[32:35], v[148:151], v[64:67], v[32:35]\n"
    "buffer_load_dwordx4 a[40:43], v251, s[32:35], s3 offen offset:2048 sc1\n"
    "v_mfma_f32_16x16x32_f16 v[32:35], v[152:155], v[60:63], v[32:35]\n"
    "v_cvt_pk_f16_f32 v191, v183, v187\n"
    "v_alignbit_b32 v88, v120, v139, v6\n"
    "v_mfma_f32_16x16x32_f16 v[32:35], v[156:159], v[56:59], v[32:35]\n"
    "v_alignbit_b32 v89, v121, v120, v6\n"
    "v_alignbit_b32 v90, v122, v121, v6\n"
    "v_mfma_f32_16x16x32_f16 v[32:35], v[160:163], v[52:55], v[32:35]\n"
    "s_waitcnt lgkmcnt(9)\n"
    "ds_read_b128 v[52:55], v5 offset:48\n"
    "v_alignbit_b32 v91, v123, v122, v6\n"
    "v_mfma_f32_16x16x32_f16 v[36:39], v[148:151], v[68:71], v[36:39]\n"
    "buffer_load_dwordx4 a[44:47], v251, s[32:35], s3 offen offset:3072 sc1\n"
    "v_mfma_f32_16x16x32_f16 v[36:39], v[152:155], v[64:67], v[36:39]\n"
    "ds_read_b64 v[136:137], v4 offset:1140\n"
    "s_add_u32 s3, s62, 12288\n"
    "v_mfma_f32_16x16x32_f16 v[36:39], v[156:159], v[60:63], v[36:39]\n"
    "ds_read_b64 v[138:139], v4 offset:1148\n"
    "s_lshl_b32 s17, s49, 11\n"
    "v_mfma_f32_16x16x32_f16 v[36:39], v[160:163], v[56:59], v[36:39]\n"
    "s_waitcnt lgkmcnt(11)\n"
    "ds_read_b128 v[56:59], v5 offset:304\n"
    "s_add_u32 s17, s17, 32768\n"
    "v_mfma_f32_16x16x32_f16 v[40:43], v[148:151], v[72:75], v[40:43]\n"
    "buffer_load_dwordx4 a[48:51], v251, s[32:35], s3 offen offset:0 sc1\n"
    "v_mfma_f32_16x16x32_f16 v[40:43], v[152:155], v[68:71], v[40:43]\n"
    "v_alignbit_b32 v92, v124, v143, v6\n"
    "v_alignbit_b32 v93, v125, v124, v6\n"
    "v_mfma_f32_16x16x32_f16 v[40:43], v[156:159], v[64:67], v[40:43]\n"
    "s_waitcnt lgkmcnt(11)\n"
    "v_alignbit_b32 v94, v126, v125, v6\n"
    "v_alignbit_b32 v95, v127, v126, v6\n"
    "v_mfma_f32_16x16x32_f16 v[40:43], v[160:163], v[60:63], v[40:43]\n"
    "s_waitcnt lgkmcnt(8)\n"
    "ds_read_b128 v[60:63], v5 offset:560\n"
    "v_alignbit_b32 v96, v128, v147, v6\n"
    "v_mfma_f32_16x16x32_f16 v[44:47], v[148:151], v[76:79], v[44:47]\n"
    "buffer_load_dwordx4 a[52:55], v251, s[32:35], s3 offen offset:1024 sc1\n"
    "v_mfma_f32_16x16x32_f16 v[44:47], v[152:155], v[72:75], v[44:47]\n"
    "ds_read_b64 v[140:141], v4 offset:628\n"
    "v_alignbit_b32 v97, v129, v128, v6\n"
    "v_mfma_f32_16x16x32_f16 v[44:47], v[156:159], v[68:71], v[44:47]\n"
    "s_waitcnt lgkmcnt(8)\n"
    "ds_read_b64 v[142:143], v4 offset:636\n"
    "v_alignbit_b32 v98, v130, v129, v6\n"
    "v_mfma_f32_16x16x32_f16 v[44:47], v[160:163], v[64:67], v[44:47]\n"
    "ds_read_b128 v[64:67], v5 offset:816\n"
    "v_alignbit_b32 v99, v131, v130, v6\n"
    "v_mfma_f32_16x16x32_f16 v[48:51], v[148:151], v[80:83], v[48:51]\n"
    "buffer_load_dwordx4 a[56:59], v251, s[32:35], s3 offen offset:2048 sc1\n"
    "v_mfma_f32_16x16x32_f16 v[48:51], v[152:155], v[76:79], v[48:51]\n"
    "ds_read_b128 v[76:79], v5 offset:1584\n"
    "v_xor_b32_e64 v228, s40, v253\n"
    "v_mfma_f32_16x16x32_f16 v[48:51], v[156:159], v[72:75], v[48:51]\n"
    "ds_read_b64 v[144:145], v4 offset:116\n"
    "v_xor_b32_e64 v230, s41, v253\n"
    "v_mfma_f32_16x16x32_f16 v[48:51], v[160:163], v[68:71], v[48:51]\n"
    "ds_read_b128 v[68:71], v5 offset:1072\n"
    "v_xor_b32_e64 v232, s42, v253\n"
    "v_mfma_f32_16x16x32_f16 v[44:47], v[188:191], v[164:167], v[44:47]\n"
    "buffer_load_dwordx4 a[60:63], v251, s[32:35], s3 offen offset:3072 sc1\n"
    "v_mfma_f32_16x16x32_f16 v[48:51], v[188:191], v[168:171], v[48:51]\n"
    "ds_read_b128 v[72:75], v5 offset:1328\n"
    "s_add_u32 s3, s62, 16384\n"
    "v_mfma_f32_16x16x32_f16 v[180:183], v[240:243], v[202:205], 0\n"
    "ds_read_b64 v[146:147], v4 offset:124\n"
    "v_xor_b32_e64 v234, s43, v253\n"
    "v_mfma_f32_16x16x32_f16 v[184:187], v[236:239], v[206:209], 0\n"
    "ds_read_b128 v[80:83], v5 offset:1840\n"
    "v_add_u32 v229, 110592, v228\n"
    "v_mfma_f32_16x16x32_f16 v[48:51], v[192:195], v[236:239], v[48:51]\n"
    "s_waitcnt lgkmcnt(13)\n"
    "buffer_load_dwordx4 a[64:67], v251, s[32:35], s3 offen offset:0 sc1\n"
    "v_mfma_f32_16x16x32_f16 v[20:23], v[84:87], v[52:55], v[20:23]\n"
    "s_waitcnt lgkmcnt(10)\n"
    "v_add_u32 v228, 28672, v228\n"
    "v_add_u32 v231, 110592, v230\n"
    "v_mfma_f32_16x16x32_f16 v[24:27], v[84:87], v[56:59], v[24:27]\n"
    "v_add_u32 v230, 28672, v230\n"
    "v_add_u32 v233, 110592, v232\n"
    "v_mfma_f32_16x16x32_f16 v[24:27], v[88:91], v[52:55], v[24:27]\n"
    "s_waitcnt lgkmcnt(9)\n"
    "v_add_u32 v232, 28672, v232\n"
    "v_add_u32 v235, 110592, v234\n"
    "v_mfma_f32_16x16x32_f16 v[28:31], v[84:87], v[60:63], v[28:31]\n"
    "buffer_load_dwordx4 v[196:199], v251, s[32:35], s17 offen sc1\n"
    "v_mfma_f32_16x16x32_f16 v[28:31], v[88:91], v[56:59], v[28:31]\n"
    "buffer_load_dwordx4 a[68:71], v251, s[32:35], s3 offen offset:1024 sc1\n"
    "v_mfma_f32_16x16x32_f16 v[28:31], v[92:95], v[52:55], v[28:31]\n"
    "s_waitcnt lgkmcnt(6)\n"
    "buffer_load_dwordx2 v[200:201], v251, s[32:35], s17 offen offset:1024 sc1\n"
    "v_mfma_f32_16x16x32_f16 v[32:35], v[84:87], v[64:67], v[32:35]\n"
    "v_add_u32 v234, 28672, v234\n"
    "v_cvt_pk_f16_f32 v188, v180, v184\n"
    "v_mfma_f32_16x16x32_f16 v[32:35], v[88:91], v[60:63], v[32:35]\n"
    "buffer_load_dwordx4 a[72:75], v251, s[32:35], s3 offen offset:2048 sc1\n"
    "v_mfma_f32_16x16x32_f16 v[32:35], v[92:95], v[56:59], v[32:35]\n"
    "v_cvt_pk_f16_f32 v189, v181, v185\n"
    "v_alignbit_b32 v148, v132, v119, v6\n"
    "v_mfma_f32_16x16x32_f16 v[32:35], v[96:99], v[52:55], v[32:35]\n"
    "s_waitcnt lgkmcnt(3)\n"
    "ds_read_b128 v[52:55], v5 offset:2416\n"
    "v_alignbit_b32 v149, v133, v132, v6\n"
    "v_mfma_f32_16x16x32_f16 v[36:39], v[84:87], v[68:71], v[36:39]\n"
    "v_cvt_pk_f16_f32 v190, v182, v186\n"
    "v_cvt_pk_f16_f32 v191, v183, v187\n"
    "v_mfma_f32_16x16x32_f16 v[36:39], v[88:91], v[64:67], v[36:39]\n"
    "buffer_load_dwordx4 a[76:79], v251, s[32:35], s3 offen offset:3072 sc1\n"
    "v_mfma_f32_16x16x32_f16 v[36:39], v[92:95], v[60:63], v[36:39]\n"
    "s_add_u32 s3, s62, 20480\n"
    "v_alignbit_b32 v150, v134, v133, v6\n"
    "v_mfma_f32_16x16x32_f16 v[36:39], v[96:99], v[56:59], v[36:39]\n"
    "s_waitcnt lgkmcnt(3)\n"
    "ds_read_b128 v[56:59], v5 offset:2672\n"
    "v_alignbit_b32 v151, v135, v134, v6\n"
    "v_mfma_f32_16x16x32_f16 v[40:43], v[84:87], v[72:75], v[40:43]\n"
    "v_alignbit_b32 v152, v136, v123, v6\n"
    "v_alignbit_b32 v153, v137, v136, v6\n"
    "v_mfma_f32_16x16x32_f16 v[40:43], v[88:91], v[68:71], v[40:43]\n"
    "buffer_load_dwordx4 a[80:83], v251, s[32:35], s3 offen offset:0 sc1\n"
    "v_mfma_f32_16x16x32_f16 v[40:43], v[92:95], v[64:67], v[40:43]\n"
    "v_alignbit_b32 v154, v138, v137, v6\n"
    "v_alignbit_b32 v155, v139, v138, v6\n"
    "v_mfma_f32_16x16x32_f16 v[40:43], v[96:99], v[60:63], v[40:43]\n"
    "ds_read_b128 v[60:63], v5 offset:2928\n"
    "v_alignbit_b32 v156, v140, v127, v6\n"
    "v_mfma_f32_16x16x32_f16 v[44:47], v[84:87], v[76:79], v[44:47]\n"
    "v_alignbit_b32 v157, v141, v140, v6\n"
    "v_alignbit_b32 v158, v142, v141, v6\n"
    "v_mfma_f32_16x16x32_f16 v[44:47], v[88:91], v[72:75], v[44:47]\n"
    "buffer_load_dwordx4 a[84:87], v251, s[32:35], s3 offen offset:1024 sc1\n"
    "v_mfma_f32_16x16x32_f16 v[44:47], v[92:95], v[68:71], v[44:47]\n"
    "v_alignbit_b32 v159, v143, v142, v6\n"
    "v_alignbit_b32 v160, v144, v131, v6\n"
    "v_mfma_f32_16x16x32_f16 v[44:47], v[96:99], v[64:67], v[44:47]\n"
    "s_waitcnt lgkmcnt(3)\n"
    "ds_read_b128 v[64:67], v5 offset:3184\n"
    "v_alignbit_b32 v161, v145, v144, v6\n"
    "v_mfma_f32_16x16x32_f16 v[48:51], v[84:87], v[80:83], v[48:51]\n"
    "v_alignbit_b32 v162, v146, v145, v6\n"
    "v_alignbit_b32 v163, v147, v146, v6\n"
    "v_mfma_f32_16x16x32_f16 v[48:51], v[88:91], v[76:79], v[48:51]\n"
    "ds_read_b128 v[76:79], v5 offset:3952\n"
    "v_mul_u32_u24 v10, 224, v0\n"
    "v_mfma_f32_16x16x32_f16 v[48:51], v[92:95], v[72:75], v[48:51]\n"
    "v_mov_b32 v16, 0x3f803f80\n"
    "v_add_u32 v10, s55, v10\n"
    "v_mfma_f32_16x16x32_f16 v[48:51], v[96:99], v[68:71], v[48:51]\n"
    "ds_read_b128 v[68:71], v5 offset:3440\n"
    "v_mov_b32 v17, 0x3f803f80\n"
    "v_mfma_f32_16x16x32_f16 v[48:51], v[188:191], v[164:167], v[48:51]\n"
    "s_waitcnt lgkmcnt(5)\n"
    "v_lshlrev_b32 v84, 16, v212\n"
    "v_lshlrev_b32_e64 v85, 16, v214\n"
    "v_mfma_f32_16x16x32_f16 v[20:23], v[148:151], v[52:55], v[20:23]\n"
    "s_waitcnt lgkmcnt(4)\n"
    "ds_read_b128 v[72:75], v5 offset:3696\n"
    "v_and_b32 v86, 0xffff0000, v212\n"
    "v_mfma_f32_16x16x32_f16 v[24:27], v[148:151], v[56:59], v[24:27]\n"
    "v_and_b32 v87, 0xffff0000, v214\n"
    "v_lshlrev_b32 v88, 16, v213\n"
    "v_mfma_f32_16x16x32_f16 v[24:27], v[152:155], v[52:55], v[24:27]\n"
    "s_waitcnt lgkmcnt(4)\n"
    "ds_read_b128 v[80:83], v5 offset:4208\n"
    "v_lshlrev_b32 v89, 16, v215\n"
    "v_mfma_f32_16x16x32_f16 v[28:31], v[148:151], v[60:63], v[28:31]\n"
    "v_and_b32 v90, 0xffff0000, v213\n"
    "v_and_b32 v91, 0xffff0000, v215\n"
    "v_mfma_f32_16x16x32_f16 v[28:31], v[152:155], v[56:59], v[28:31]\n"
    "ds_write_b128 v13, v[20:23] offset:28672\n"
    "v_mfma_f32_16x16x32_f16 v[28:31], v[156:159], v[52:55], v[28:31]\n"
    "s_waitcnt lgkmcnt(5)\n"
    "v_lshlrev_b32 v92, 16, v216\n"
    "v_lshlrev_b32_e64 v93, 16, v218\n"
    "v_mfma_f32_16x16x32_f16 v[32:35], v[148:151], v[64:67], v[32:35]\n"
    "ds_write_b128 v13, v[24:27] offset:29696\n"
    "v_mfma_f32_16x16x32_f16 v[32:35], v[152:155], v[60:63], v[32:35]\n"
    "v_and_b32 v94, 0xffff0000, v216\n"
    "v_and_b32 v95, 0xffff0000, v218\n"
    "v_mfma_f32_16x16x32_f16 v[32:35], v[156:159], v[56:59], v[32:35]\n"
    "v_lshlrev_b32 v96, 16, v217\n"
    "v_lshlrev_b32 v97, 16, v219\n"
    "v_mfma_f32_16x16x32_f16 v[32:35], v[160:163], v[52:55], v[32:35]\n"
    "s_waitcnt lgkmcnt(4)\n"
    "ds_write_b128 v13, v[28:31] offset:30720\n"
    "v_mfma_f32_16x16x32_f16 v[36:39], v[148:151], v[68:71], v[36:39]\n"
    "v_and_b32 v98, 0xffff0000, v217\n"
    "v_and_b32 v99, 0xffff0000, v219\n"
    "v_mfma_f32_16x16x32_f16 v[36:39], v[152:155], v[64:67], v[36:39]\n"
    "v_lshlrev_b32 v100, 16, v220\n"
    "v_lshlrev_b32_e64 v101, 16, v222\n"
    "v_mfma_f32_16x16x32_f16 v[36:39], v[156:159], v[60:63], v[36:39]\n"
    "v_and_b32 v102, 0xffff0000, v220\n"
    "v_and_b32 v103, 0xffff0000, v222\n"
    "v_mfma_f32_16x16x32_f16 v[36:39], v[160:163], v[56:59], v[36:39]\n"
    "s_waitcnt lgkmcnt(4)\n"
    "ds_write_b128 v13, v[32:35] offset:31744\n"
    "v_mfma_f32_16x16x32_f16 v[40:43], v[148:151], v[72:75], v[40:43]\n"
    "v_lshlrev_b32 v104, 16, v221\n"
    "v_lshlrev_b32_e64 v105, 16, v223\n"
    "v_mfma_f32_16x16x32_f16 v[40:43], v[152:155], v[68:71], v[40:43]\n"
    "v_and_b32 v106, 0xffff0000, v221\n"
    "v_and_b32 v107, 0xffff0000, v223\n"
    "v_mfma_f32_16x16x32_f16 v[40:43], v[156:159], v[64:67], v[40:43]\n"
    "v_lshlrev_b32 v108, 16, v224\n"
    "v_lshlrev_b32 v109, 16, v226\n"
    "v_mfma_f32_16x16x32_f16 v[40:43], v[160:163], v[60:63], v[40:43]\n"
    "ds_write_b128 v13, v[36:39] offset:32768\n"
    "v_mfma_f32_16x16x32_f16 v[44:47], v[148:151], v[76:79], v[44:47]\n"
    "v_and_b32 v110, 0xffff0000, v224\n"
    "v_and_b32 v111, 0xffff0000, v226\n"
    "v_mfma_f32_16x16x32_f16 v[44:47], v[152:155], v[72:75], v[44:47]\n"
    "v_lshlrev_b32 v112, 16, v225\n"
    "v_lshlrev_b32 v113, 16, v227\n"
    "v_mfma_f32_16x16x32_f16 v[44:47], v[156:159], v[68:71], v[44:47]\n"
    "v_and_b32 v114, 0xffff0000, v225\n"
    "v_and_b32 v115, 0xffff0000, v227\n"
    "v_mfma_f32_16x16x32_f16 v[44:47], v[160:163], v[64:67], v[44:47]\n"
    "s_waitcnt lgkmcnt(5)\n"
    "ds_write_b128 v13, v[40:43] offset:33792\n"
    "v_mfma_f32_16x16x32_f16 v[48:51], v[148:151], v[80:83], v[48:51]\n"
    "v_mfma_f32_16x16x32_f16 v[48:51], v[152:155], v[76:79], v[48:51]\n"
    "v_mfma_f32_16x16x32_f16 v[48:51], v[156:159], v[72:75], v[48:51]\n"
    "v_mfma_f32_16x16x32_f16 v[48:51], v[160:163], v[68:71], v[48:51]\n"
    "s_nop 1\n"
    "ds_write_b128 v13, v[44:47] offset:34816\n"
    "ds_read_b128 v[116:119], v3 offset:40512\n"
    "s_waitcnt vmcnt(4)\n"
    "ds_write_b64 v10, v[16:17] offset:208\n"
    "ds_write_b64 v10, v[16:17] offset:14544\n"
    "s_nop 0\n"
    "ds_write_b128 v13, v[48:51] offset:35840\n"
    "s_waitcnt lgkmcnt(0)\n"
    "s_barrier\n"
    "ds_read_b64 v[52:53], v228 offset:0\n"
    "ds_read_b64 v[54:55], v228 offset:40960\n"
    "ds_read_b64 v[56:57], v229 offset:0\n"
    "ds_read_b64 v[58:59], v229 offset:40960\n"
    "ds_read_b64 v[60:61], v230 offset:0\n"
    "ds_read_b64 v[62:63], v230 offset:40960\n"
    "ds_read_b64 v[64:65], v231 offset:0\n"
    "ds_read_b64 v[66:67], v231 offset:40960\n"
    "ds_read_b64 v[68:69], v232 offset:0\n"
    "ds_read_b64 v[70:71], v232 offset:40960\n"
    "ds_read_b64 v[72:73], v233 offset:0\n"
    "ds_read_b64 v[74:75], v233 offset:40960\n"
    "ds_read_b64 v[76:77], v234 offset:0\n"
    "ds_read_b64 v[78:79], v234 offset:40960\n"
    "ds_read_b64 v[80:81], v235 offset:0\n"
    "ds_read_b64 v[82:83], v235 offset:40960\n"
    "s_waitcnt lgkmcnt(14)\n"
    "v_pk_fma_f32 v[52:53], v[52:53], v[116:117], v[84:85] op_sel:[0,0,0] op_sel_hi:[1,0,1]\n"
    "v_pk_fma_f32 v[54:55], v[54:55], v[116:117], v[86:87] op_sel:[0,1,0] op_sel_hi:[1,1,1]\n"
    "s_waitcnt lgkmcnt(13)\n"
    "v_pk_fma_f32 v[56:57], v[56:57], v[118:119], v[88:89] op_sel:[0,0,0] op_sel_hi:[1,0,1]\n"
    "s_waitcnt lgkmcnt(12)\n"
    "s_nop 0\n"
    "v_pk_fma_f32 v[58:59], v[58:59], v[118:119], v[90:91] op_sel:[0,1,0] op_sel_hi:[1,1,1]\n"
    "v_cvt_pk_bf16_f32 v124, v52, v54\n"
    "v_cvt_pk_bf16_f32 v125, v56, v58\n"
    "v_cvt_pk_bf16_f32 v126, v53, v55\n"
    "v_cvt_pk_bf16_f32 v127, v57, v59\n"
    "buffer_store_dwordx4 v[124:127], v210, s[32:35], s22 offen sc1\n"
    "s_waitcnt lgkmcnt(11)\n"
    "v_pk_fma_f32 v[60:61], v[60:61], v[116:117], v[92:93] op_sel:[0,0,0] op_sel_hi:[1,0,1]\n"
    "s_waitcnt lgkmcnt(10)\n"
    "v_pk_fma_f32 v[62:63], v[62:63], v[116:117], v[94:95] op_sel:[0,1,0] op_sel_hi:[1,1,1]\n"
    "s_waitcnt lgkmcnt(9)\n"
    "v_pk_fma_f32 v[64:65], v[64:65], v[118:119], v[96:97] op_sel:[0,0,0] op_sel_hi:[1,0,1]\n"
    "s_waitcnt lgkmcnt(8)\n"
    "v_pk_fma_f32 v[66:67], v[66:67], v[118:119], v[98:99] op_sel:[0,1,0] op_sel_hi:[1,1,1]\n"
    "v_cvt_pk_bf16_f32 v128, v60, v62\n"
    "v_cvt_pk_bf16_f32 v129, v64, v66\n"
    "v_cvt_pk_bf16_f32 v130, v61, v63\n"
    "v_cvt_pk_bf16_f32 v131, v65, v67\n"
    "buffer_store_dwordx4 v[128:131], v210, s[32:35], s23 offen sc1\n"
    "s_waitcnt lgkmcnt(7)\n"
    "v_pk_fma_f32 v[68:69], v[68:69], v[116:117], v[100:101] op_sel:[0,0,0] op_sel_hi:[1,0,1]\n"
    "s_waitcnt lgkmcnt(6)\n"
    "v_pk_fma_f32 v[70:71], v[70:71], v[116:117], v[102:103] op_sel:[0,1,0] op_sel_hi:[1,1,1]\n"
    "s_waitcnt lgkmcnt(5)\n"
    "v_pk_fma_f32 v[72:73], v[72:73], v[118:119], v[104:105] op_sel:[0,0,0] op_sel_hi:[1,0,1]\n"
    "s_waitcnt lgkmcnt(4)\n"
    "v_pk_fma_f32 v[74:75], v[74:75], v[118:119], v[106:107] op_sel:[0,1,0] op_sel_hi:[1,1,1]\n"
    "v_cvt_pk_bf16_f32 v132, v68, v70\n"
    "v_cvt_pk_bf16_f32 v133, v72, v74\n"
    "v_cvt_pk_bf16_f32 v134, v69, v71\n"
    "v_cvt_pk_bf16_f32 v135, v73, v75\n"
    "buffer_store_dwordx4 v[132:135], v210, s[32:35], s24 offen sc1\n"
    "s_waitcnt lgkmcnt(3)\n"
    "v_pk_fma_f32 v[76:77], v[76:77], v[116:117], v[108:109] op_sel:[0,0,0] op_sel_hi:[1,0,1]\n"
    "s_waitcnt lgkmcnt(2)\n"
    "v_pk_fma_f32 v[78:79], v[78:79], v[116:117], v[110:111] op_sel:[0,1,0] op_sel_hi:[1,1,1]\n"
    "s_waitcnt lgkmcnt(1)\n"
    "v_pk_fma_f32 v[80:81], v[80:81], v[118:119], v[112:113] op_sel:[0,0,0] op_sel_hi:[1,0,1]\n"
    "s_waitcnt lgkmcnt(0)\n"
    "v_pk_fma_f32 v[82:83], v[82:83], v[118:119], v[114:115] op_sel:[0,1,0] op_sel_hi:[1,1,1]\n"
    "v_cvt_pk_bf16_f32 v136, v76, v78\n"
    "v_cvt_pk_bf16_f32 v137, v80, v82\n"
    "v_cvt_pk_bf16_f32 v138, v77, v79\n"
    "v_cvt_pk_bf16_f32 v139, v81, v83\n"
    "buffer_store_dwordx4 v[136:139], v210, s[32:35], s25 offen sc1\n"
    "s_barrier\n"
    "s_add_u32 m0, s55, 28672\n"
    "s_nop 0\n"
    "buffer_load_dwordx4 v244, s[36:39], s16 offen offset:0 lds\n"
    "buffer_load_dwordx4 v245, s[36:39], s16 offen offset:1024 lds\n"
    "buffer_load_dwordx4 v246, s[36:39], s16 offen offset:2048 lds\n"
    "buffer_load_dwordx4 v247, s[36:39], s16 offen offset:3072 lds\n"
    "s_add_u32 m0, s55, 32768\n"
    "s_nop 0\n"
    "s_nop 0\n"
    "buffer_load_dwordx4 v248, s[36:39], s16 offen offset:0 lds\n"
    "buffer_load_dwordx4 v249, s[36:39], s16 offen offset:1024 lds\n"
    "buffer_load_dwordx4 v250, s[36:39], s16 offen offset:2048 lds\n"
    ".Lk_gemm1:\n"
    "s_memrealtime s[78:79]\n"
    "s_memtime s[4:5]\n"
    "s_add_u32 s3, s62, 20480\n"
    "buffer_load_dwordx4 a[88:91], v251, s[32:35], s3 offen offset:2048 sc1\n"
    "buffer_load_dwordx4 a[92:95], v251, s[32:35], s3 offen offset:3072 sc1\n"
    "s_add_u32 s3, s62, 24576\n"
    "buffer_load_dwordx4 a[96:99], v251, s[32:35], s3 offen offset:0 sc1\n"
    "buffer_load_dwordx4 a[100:103], v251, s[32:35], s3 offen offset:1024 sc1\n"
    "buffer_load_dwordx4 a[104:107], v251, s[32:35], s3 offen offset:2048 sc1\n"
    "buffer_load_dwordx4 a[108:111], v251, s[32:35], s3 offen offset:3072 sc1\n"
    "v_cmp_eq_u32 vcc, s51, v197\n"
    "v_subrev_u32 v196, s0, v196\n"
    "v_bfrev_b32 v197, 3\n"
    "v_cndmask_b32 v196, v197, v196, vcc\n"
    "v_cmp_eq_u32 vcc, s51, v199\n"
    "v_subrev_u32 v198, s0, v198\n"
    "v_bfrev_b32 v199, 3\n"
    "v_cndmask_b32 v198, v199, v198, vcc\n"
    "v_cmp_eq_u32 vcc, s51, v201\n"
    "v_subrev_u32 v200, s0, v200\n"
    "v_bfrev_b32 v201, 3\n"
    "v_cndmask_b32 v200, v201, v200, vcc\n"
    "v_max3_i32 v196, v196, v198, v200\n"
    "s_nop 1\n"
    "v_max_i32_dpp v196, v196, v196 row_ror:1 row_mask:0xf bank_mask:0xf\n"
    "s_waitcnt lgkmcnt(0)\n"
    "s_sub_u32 s78, s78, s0\n"
    "v_max_i32_dpp v196, v196, v196 row_ror:2 row_mask:0xf bank_mask:0xf\n"
    "s_sub_u32 s4, s4, s76\n"
    "s_nop 0\n"
    "v_max_i32_dpp v196, v196, v196 row_ror:4 row_mask:0xf bank_mask:0xf\n"
    "v_cvt_f32_u32 v202, s4\n"
    "s_nop 0\n"
    "v_max_i32_dpp v196, v196, v196 row_ror:8 row_mask:0xf bank_mask:0xf\n"
    "v_cvt_f32_u32 v203, s78\n"
    "s_nop 0\n"
    "v_max_i32_dpp v196, v196, v196 row_bcast:15 row_mask:0xa bank_mask:0xf\n"
    "v_rcp_f32_e64 v202, v202\n"
    "s_nop 0\n"
    "v_max_i32_dpp v196, v196, v196 row_bcast:31 row_mask:0xc bank_mask:0xf\n"
    "v_mul_f32 v203, 5650.0, v203\n"
    "v_readlane_b32 s17, v196, 63\n"
    "v_mul_f32_e64 v202, v202, v203\n"
    "s_min_i32 s17, s17, 300\n"
    "v_cvt_u32_f32 v202, v202\n"
    "s_add_u32 s17, s17, 35\n"
    "v_readfirstlane_b32 s78, v202\n"
    "s_add_u32 s17, s17, s0\n"
    "s_min_u32 s78, s78, 600\n"
    "s_add_u32 s17, s17, s78\n"
    ".Lk_pace_13:\n"
    "s_memrealtime s[0:1]\n"
    "s_waitcnt lgkmcnt(0)\n"
    "s_sub_u32 s0, s0, s17\n"
    "s_cmp_lt_i32 s0, 0\n"
    "s_cbranch_scc1 .Lk_pace_13\n"
    "buffer_load_dwordx4 v[20:23], v251, s[32:35], s64 offen offset:0 sc1\n"
    "s_and_b32 s3, s51, 15\n"
    "s_lshr_b32 s61, s48, 1\n"
    "s_mul_i32 s3, s3, 0x11111110\n"
    "s_lshl1_add_u32 s96, s7, s61\n"
    "s_xor_b32 s3, s3, s51\n"
    "s_and_b32 s97, s48, 1\n"
    "s_mov_b32 s56, 0x80008000\n"
    "s_mul_i32 s58, s52, 442368\n"
    "buffer_load_dwordx4 v[24:27], v251, s[32:35], s64 offen offset:1024 sc1\n"
    "v_and_b32_e64 v180, 3, v1\n"
    "s_add_u32 s58, s58, 2752512\n"
    "v_lshlrev_b32_e64 v180, 3, v180\n"
    "s_mul_i32 s59, s96, 110592\n"
    "v_lshrrev_b32 v180, v180, s3\n"
    "s_add_u32 s59, s59, s58\n"
    "v_bfe_u32 v182, v180, 0, 2\n"
    "s_add_u32 s44, s32, s59\n"
    "s_addc_u32 s45, s33, 0\n"
    "buffer_load_dwordx4 v[28:31], v251, s[32:35], s64 offen offset:2048 sc1\n"
    "v_bfe_u32 v184, v180, 2, 2\n"
    "s_xor_b32 s59, s96, 1\n"
    "v_bfe_u32 v186, v180, 4, 2\n"
    "s_mul_i32 s59, s59, 110592\n"
    "v_bfe_u32 v188, v180, 6, 2\n"
    "s_add_u32 s59, s59, s58\n"
    "v_lshlrev_b32 v183, 15, v182\n"
    "s_add_u32 s36, s32, s59\n"
    "s_addc_u32 s37, s33, 0\n"
    "buffer_load_dwordx4 v[32:35], v251, s[32:35], s64 offen offset:3072 sc1\n"
    "v_lshlrev_b32_e64 v185, 15, v184\n"
    "s_mov_b32 s38, 110592\n"
    "v_lshlrev_b32 v187, 15, v186\n"
    "s_mov_b32 s46, s38\n"
    "v_lshlrev_b32 v189, 15, v188\n"
    "s_lshl_b32 s23, s48, 12\n"
    "v_lshl_or_b32 v183, v182, 30, v183\n"
    "s_lshl_b32 s22, s48, 11\n"
    "buffer_load_dwordx4 v[36:39], v251, s[32:35], s65 offen offset:0 sc1\n"
    "v_lshl_or_b32 v185, v184, 30, v185\n"
    "s_add_u32 s22, s22, 2048\n"
    "v_lshl_or_b32 v187, v186, 30, v187\n"
    "s_min_u32 s22, s22, s23\n"
    "v_lshl_or_b32 v189, v188, 30, v189\n"
    "s_cmp_lt_u32 s48, 2\n"
    "s_cselect_b32 s99, s22, 0x70000000\n"
    "v_and_b32 v252, s56, v183\n"
    "s_add_u32 s24, s23, 28672\n"
    "buffer_load_dwordx4 v[40:43], v251, s[32:35], s65 offen offset:1024 sc1\n"
    "v_and_b32_e64 v253, s56, v185\n"
    "s_add_u32 s25, s23, 45056\n"
    "v_and_b32_e64 v254, s56, v187\n"
    "s_add_u32 s30, s23, 61440\n"
    "v_and_b32_e64 v255, s56, v189\n"
    "s_add_u32 s31, s23, 77824\n"
    "v_lshrrev_b32_e64 v4, 2, v0\n"
    "s_add_u32 s98, s23, 94208\n"
    "buffer_load_dwordx4 v[44:47], v251, s[32:35], s65 offen offset:2048 sc1\n"
    "v_and_b32_e64 v5, 3, v0\n"
    "s_add_u32 s23, s23, 12288\n"
    "v_mul_u32_u24 v4, 224, v4\n"
    "s_lshl2_add_u32 s59, s52, s96\n"
    "v_lshl_add_u32 v236, v5, 3, v4\n"
    "s_lshl_b32 s10, s59, 10\n"
    "v_min_u32 v190, 31, v0\n"
    "s_lshl3_add_u32 s10, s97, s10\n"
    "buffer_load_dwordx4 v[48:51], v251, s[32:35], s65 offen offset:3072 sc1\n"
    "v_add_u32 v237, 0x10000, v236\n"
    "s_add_u32 s10, s10, 12124160\n"
    "v_mul_u32_u24 v190, 224, v190\n"
    "s_mul_i32 s54, s59, 40960\n"
    "v_add_u32 v238, 0x20000, v236\n"
    "s_mul_i32 s59, s63, 5120\n"
    "v_add_u32 v190, s55, v190\n"
    "s_add_u32 s54, s54, s59\n"
    "buffer_load_dwordx4 v[52:55], v251, s[32:35], s66 offen offset:0 sc1\n"
    "v_mov_b32 v192, 0x3f803f80\n"
    "s_lshl3_add_u32 s54, s97, s54\n"
    "v_mov_b32 v193, 0x3f803f80\n"
    "s_add_u32 s54, s54, 131072\n"
    "v_lshl_add_u32 v3, v2, 4, s55\n"
    "s_mul_i32 s59, s48, 110592\n"
    "s_and_b32 s3, s53, 7\n"
    "s_add_u32 s58, s58, s59\n"
    "buffer_load_dwordx4 v[56:59], v251, s[32:35], s66 offen offset:1024 sc1\n"
    "v_min_u32 v173, 55, v0\n"
    "s_mul_i32 s60, s53, 768\n"
    "s_lshl3_add_u32 s3, s49, s3\n"
    "s_add_u32 s60, s60, s58\n"
    "s_lshl_b32 s3, s3, 10\n"
    "s_lshl_b32 s59, s53, 10\n"
    "s_add_u32 s3, s3, 12058624\n"
    "s_add_u32 s59, s59, s58\n"
    "s_nop 0\n"
    "buffer_load_dwordx4 v[60:63], v251, s[32:35], s66 offen offset:2048 sc1\n"
    "v_lshl_add_u32 v172, v173, 4, s3\n"
    "s_add_u32 s40, s59, 12288\n"
    "s_add_u32 s3, s55, 36352\n"
    "s_add_u32 s41, s59, 28672\n"
    "v_lshl_add_u32 v173, v173, 3, s3\n"
    "s_add_u32 s42, s59, 45056\n"
    "v_cmp_eq_u32_e64 vcc, 2, v2\n"
    "s_add_u32 s43, s59, 61440\n"
    "buffer_load_dwordx4 v[64:67], v251, s[32:35], s66 offen offset:3072 sc1\n"
    "v_add_u32 v239, -256, v251\n"
    "s_add_u32 s58, s59, 77824\n"
    "v_mov_b32 v191, 0x70000000\n"
    "s_add_u32 s59, s59, 94208\n"
    "v_cndmask_b32 v239, v239, v191, vcc\n"
    "v_cmp_gt_u32 vcc, 2, v2\n"
    "s_nop 0\n"
    "s_nop 0\n"
    "v_cndmask_b32_e64 v239, v239, v251, vcc\n"
    "ds_read_b64_tr_b16 v[4:5], v236 offset:0\n"
    "ds_read_b64_tr_b16 v[6:7], v236 offset:3584\n"
    "ds_read_b64_tr_b16 v[8:9], v236 offset:32\n"
    "ds_read_b64_tr_b16 v[10:11], v236 offset:3616\n"
    "ds_read_b64_tr_b16 v[12:13], v236 offset:64\n"
    "ds_read_b64_tr_b16 v[14:15], v236 offset:3648\n"
    "ds_read_b64_tr_b16 v[16:17], v236 offset:96\n"
    "ds_read_b64_tr_b16 v[18:19], v236 offset:3680\n"
    "s_waitcnt vmcnt(10)\n"
    "v_max3_u32 v250, v20, v22, v24\n"
    "v_max_u32 v250, v250, v26\n"
    "v_cmp_eq_u32_e64 vcc, -1, v250\n"
    "ds_write_b64 v190, v[192:193] offset:28880\n"
    "s_cbranch_vccnz .Lk_ool_14\n"
    ".Lk_ret_15:\n"
    "v_mov_b64 v[248:249], v[22:23]\n"
    "v_mov_b64 v[22:23], v[24:25]\n"
    "v_mov_b64_e64 v[24:25], v[248:249]\n"
    "s_waitcnt lgkmcnt(7)\n"
    "v_mfma_f32_16x16x32_bf16 v[180:183], v[4:7], v[20:23], 0\n"
    "s_waitcnt vmcnt(8)\n"
    "ds_read_b64_tr_b16 v[244:245], v236 offset:128\n"
    "v_max3_u32 v250, v28, v30, v32\n"
    "v_mfma_f32_16x16x32_bf16 v[184:187], v[4:7], v[24:27], 0\n"
    "s_waitcnt lgkmcnt(6)\n"
    "buffer_load_dwordx4 v[68:71], v251, s[32:35], s67 offen offset:0 sc1\n"
    "v_max_u32_e64 v250, v250, v34\n"
    "v_mfma_f32_16x16x32_bf16 v[192:195], v[8:11], v[24:27], 0\n"
    "ds_read_b64_tr_b16 v[246:247], v236 offset:3712\n"
    "ds_read_b64_tr_b16 v[4:5], v236 offset:160\n"
    "v_cmp_eq_u32_e64 vcc, -1, v250\n"
    "v_mfma_f32_16x16x32_bf16 v[188:191], v[8:11], v[20:23], 0\n"
    "s_waitcnt lgkmcnt(6)\n"
    "s_cbranch_vccnz .Lk_ool_16\n"
    ".Lk_ret_17:\n"
    "v_mov_b64_e64 v[248:249], v[30:31]\n"
    "v_mfma_f32_16x16x32_bf16 v[196:199], v[12:15], v[20:23], 0\n"
    "ds_read_b64_tr_b16 v[6:7], v236 offset:3744\n"
    "ds_read_b64_tr_b16 v[8:9], v236 offset:192\n"
    "v_mfma_f32_16x16x32_bf16 v[200:203], v[12:15], v[24:27], 0\n"
    "s_waitcnt lgkmcnt(6)\n"
    "buffer_load_dwordx4 v[72:75], v251, s[32:35], s67 offen offset:1024 sc1\n"
    "v_mov_b64 v[30:31], v[32:33]\n"
    "v_mfma_f32_16x16x32_bf16 v[208:211], v[16:19], v[24:27], 0\n"
    "ds_read_b64_tr_b16 v[10:11], v236 offset:3776\n"
    "ds_read_b64_tr_b16 v[12:13], v236 offset:7168\n"
    "v_mov_b64_e64 v[32:33], v[248:249]\n"
    "v_mfma_f32_16x16x32_bf16 v[204:207], v[16:19], v[20:23], 0\n"
    "s_waitcnt vmcnt(8) lgkmcnt(5)\n"
    "ds_read_b64_tr_b16 v[14:15], v236 offset:10752\n"
    "v_max3_u32 v250, v36, v38, v40\n"
    "v_mfma_f32_16x16x32_bf16 v[212:215], v[244:247], v[20:23], 0\n"
    "buffer_load_dwordx4 v[76:79], v251, s[32:35], s67 offen offset:2048 sc1\n"
    "v_max_u32 v250, v250, v42\n"
    "v_mfma_f32_16x16x32_bf16 v[216:219], v[244:247], v[24:27], 0\n"
    "s_waitcnt lgkmcnt(4)\n"
    "ds_read_b64_tr_b16 v[16:17], v236 offset:7200\n"
    "ds_read_b64_tr_b16 v[18:19], v236 offset:10784\n"
    "v_cmp_eq_u32 vcc, -1, v250\n"
    "v_mfma_f32_16x16x32_bf16 v[224:227], v[4:7], v[24:27], 0\n"
    "s_cbranch_vccnz .Lk_ool_18\n"
    ".Lk_ret_19:\n"
    "v_mov_b64 v[248:249], v[38:39]\n"
    "v_mfma_f32_16x16x32_bf16 v[220:223], v[4:7], v[20:23], 0\n"
    "s_waitcnt lgkmcnt(4)\n"
    "ds_read_b64_tr_b16 v[244:245], v236 offset:7232\n"
    "ds_read_b64_tr_b16 v[246:247], v236 offset:10816\n"
    "v_mfma_f32_16x16x32_bf16 v[228:231], v[8:11], v[20:23], 0\n"
    "buffer_load_dwordx4 v[80:83], v251, s[32:35], s67 offen offset:3072 sc1\n"
    "v_mfma_f32_16x16x32_bf16 v[232:235], v[8:11], v[24:27], 0\n"
    "s_waitcnt lgkmcnt(4)\n"
    "ds_read_b64_tr_b16 v[4:5], v236 offset:7264\n"
    "ds_read_b64_tr_b16 v[6:7], v236 offset:10848\n"
    "v_mov_b64_e64 v[38:39], v[40:41]\n"
    "v_mfma_f32_16x16x32_bf16 v[184:187], v[12:15], v[32:35], v[184:187]\n"
    "ds_read_b64_tr_b16 v[8:9], v236 offset:7296\n"
    "v_mov_b64_e64 v[40:41], v[248:249]\n"
    "v_mfma_f32_16x16x32_bf16 v[180:183], v[12:15], v[28:31], v[180:183]\n"
    "s_waitcnt vmcnt(8) lgkmcnt(5)\n"
    "buffer_load_dwordx4 v[84:87], v251, s[32:35], s68 offen offset:0 sc1\n"
    "v_max3_u32 v250, v44, v46, v48\n"
    "v_mfma_f32_16x16x32_bf16 v[188:191], v[16:19], v[28:31], v[188:191]\n"
    "ds_read_b64_tr_b16 v[10:11], v236 offset:10880\n"
    "ds_read_b64_tr_b16 v[12:13], v236 offset:7328\n"
    "v_max_u32 v250, v250, v50\n"
    "v_mfma_f32_16x16x32_bf16 v[192:195], v[16:19], v[32:35], v[192:195]\n"
    "s_waitcnt lgkmcnt(5)\n"
    "ds_read_b64_tr_b16 v[14:15], v236 offset:10912\n"
    "v_cmp_eq_u32 vcc, -1, v250\n"
    "v_mfma_f32_16x16x32_bf16 v[200:203], v[244:247], v[32:35], v[200:203]\n"
    "buffer_load_dwordx4 v[88:91], v251, s[32:35], s68 offen offset:1024 sc1\n"
    "s_cbranch_vccnz .Lk_ool_20\n"
    ".Lk_ret_21:\n"
    "v_mfma_f32_16x16x32_bf16 v[196:199], v[244:247], v[28:31], v[196:199]\n"
    "s_waitcnt lgkmcnt(4)\n"
    "ds_read_b64_tr_b16 v[16:17], v236 offset:7360\n"
    "ds_read_b64_tr_b16 v[18:19], v236 offset:10944\n"
    "v_mfma_f32_16x16x32_bf16 v[204:207], v[4:7], v[28:31], v[204:207]\n"
    "ds_read_b64_tr_b16 v[244:245], v236 offset:14336\n"
    "v_mov_b64_e64 v[248:249], v[46:47]\n"
    "v_mfma_f32_16x16x32_bf16 v[208:211], v[4:7], v[32:35], v[208:211]\n"
    "s_waitcnt lgkmcnt(5)\n"
    "ds_read_b64_tr_b16 v[246:247], v236 offset:17920\n"
    "v_mov_b64 v[46:47], v[48:49]\n"
    "v_mfma_f32_16x16x32_bf16 v[216:219], v[8:11], v[32:35], v[216:219]\n"
    "s_waitcnt vmcnt(8)\n"
    "buffer_load_dwordx4 v[92:95], v251, s[32:35], s68 offen offset:2048 sc1\n"
    "v_mov_b64 v[48:49], v[248:249]\n"
    "v_max3_u32 v250, v52, v54, v56\n"
    "v_mfma_f32_16x16x32_bf16 v[212:215], v[8:11], v[28:31], v[212:215]\n"
    "s_waitcnt vmcnt(8) lgkmcnt(4)\n"
    "ds_read_b64_tr_b16 v[4:5], v236 offset:14368\n"
    "ds_read_b64_tr_b16 v[6:7], v236 offset:17952\n"
    "v_max3_u32 v250, v250, v58, v60\n"
    "v_mfma_f32_16x16x32_bf16 v[220:223], v[12:15], v[28:31], v[220:223]\n"
    "s_waitcnt vmcnt(7)\n"
    "ds_read_b64_tr_b16 v[8:9], v236 offset:14400\n"
    "v_max3_u32 v250, v250, v62, v64\n"
    "v_mfma_f32_16x16x32_bf16 v[224:227], v[12:15], v[32:35], v[224:227]\n"
    "s_waitcnt lgkmcnt(5)\n"
    "buffer_load_dwordx4 v[96:99], v251, s[32:35], s68 offen offset:3072 sc1\n"
    "v_max_u32 v250, v250, v66\n"
    "v_mfma_f32_16x16x32_bf16 v[232:235], v[16:19], v[32:35], v[232:235]\n"
    "ds_read_b64_tr_b16 v[10:11], v236 offset:17984\n"
    "ds_read_b64_tr_b16 v[12:13], v236 offset:14432\n"
    "v_cmp_eq_u32_e64 vcc, -1, v250\n"
    "v_mfma_f32_16x16x32_bf16 v[228:231], v[16:19], v[28:31], v[228:231]\n"
    "s_waitcnt lgkmcnt(5)\n"
    "s_cbranch_vccnz .Lk_ool_22\n"
    ".Lk_ret_23:\n"
    "v_mov_b64_e64 v[248:249], v[54:55]\n"
    "v_mfma_f32_16x16x32_bf16 v[180:183], v[244:247], v[36:39], v[180:183]\n"
    "ds_read_b64_tr_b16 v[14:15], v236 offset:18016\n"
    "ds_read_b64_tr_b16 v[16:17], v236 offset:14464\n"
    "v_mfma_f32_16x16x32_bf16 v[184:187], v[244:247], v[40:43], v[184:187]\n"
    "s_waitcnt lgkmcnt(5)\n"
    "buffer_load_dwordx4 v[176:179], v172, s[32:35], 0 offen sc1\n"
    "v_mfma_f32_16x16x32_bf16 v[192:195], v[4:7], v[40:43], v[192:195]\n"
    "ds_read_b64_tr_b16 v[18:19], v236 offset:18048\n"
    "ds_read_b64_tr_b16 v[244:245], v236 offset:14496\n"
    "v_mov_b64 v[54:55], v[56:57]\n"
    "v_mfma_f32_16x16x32_bf16 v[188:191], v[4:7], v[36:39], v[188:191]\n"
    "s_waitcnt lgkmcnt(5)\n"
    "ds_read_b64_tr_b16 v[246:247], v236 offset:18080\n"
    "v_mov_b64 v[56:57], v[248:249]\n"
    "v_mfma_f32_16x16x32_bf16 v[196:199], v[8:11], v[36:39], v[196:199]\n"
    "buffer_load_dwordx4 v[100:103], v251, s[32:35], s69 offen offset:0 sc1\n"
    "v_mfma_f32_16x16x32_bf16 v[200:203], v[8:11], v[40:43], v[200:203]\n"
    "s_waitcnt lgkmcnt(4)\n"
    "ds_read_b64_tr_b16 v[4:5], v236 offset:14528\n"
    "ds_read_b64_tr_b16 v[6:7], v236 offset:18112\n"
    "v_mfma_f32_16x16x32_bf16 v[208:211], v[12:15], v[40:43], v[208:211]\n"
    "ds_read_b64_tr_b16 v[8:9], v236 offset:21504\n"
    "v_mov_b64 v[248:249], v[62:63]\n"
    "v_mfma_f32_16x16x32_bf16 v[204:207], v[12:15], v[36:39], v[204:207]\n"
    "s_waitcnt lgkmcnt(5)\n"
    "ds_read_b64_tr_b16 v[10:11], v236 offset:25088\n"
    "v_mov_b64 v[62:63], v[64:65]\n"
    "v_mfma_f32_16x16x32_bf16 v[212:215], v[16:19], v[36:39], v[212:215]\n"
    "buffer_load_dwordx4 v[104:107], v251, s[32:35], s69 offen offset:1024 sc1\n"
    "v_mfma_f32_16x16x32_bf16 v[216:219], v[16:19], v[40:43], v[216:219]\n"
    "s_waitcnt lgkmcnt(4)\n"
    "ds_read_b64_tr_b16 v[12:13], v236 offset:21536\n"
    "ds_read_b64_tr_b16 v[14:15], v236 offset:25120\n"
    "v_mfma_f32_16x16x32_bf16 v[224:227], v[244:247], v[40:43], v[224:227]\n"
    "s_waitcnt vmcnt(9)\n"
    "ds_read_b64_tr_b16 v[16:17], v236 offset:21568\n"
    "v_max3_u32 v250, v68, v70, v72\n"
    "v_mfma_f32_16x16x32_bf16 v[220:223], v[244:247], v[36:39], v[220:223]\n"
    "s_waitcnt vmcnt(8) lgkmcnt(5)\n"
    "buffer_load_dwordx4 v[108:111], v251, s[32:35], s69 offen offset:2048 sc1\n"
    "v_mov_b64 v[64:65], v[248:249]\n"
    "v_max3_u32 v250, v250, v74, v76\n"
    "v_mfma_f32_16x16x32_bf16 v[228:231], v[4:7], v[36:39], v[228:231]\n"
    "s_waitcnt vmcnt(8)\n"
    "ds_read_b64_tr_b16 v[18:19], v236 offset:25152\n"
    "ds_read_b64_tr_b16 v[244:245], v236 offset:21600\n"
    "v_max3_u32 v250, v250, v78, v80\n"
    "v_mfma_f32_16x16x32_bf16 v[232:235], v[4:7], v[40:43], v[232:235]\n"
    "s_waitcnt lgkmcnt(5)\n"
    "ds_read_b64_tr_b16 v[246:247], v236 offset:25184\n"
    "v_max_u32_e64 v250, v250, v82\n"
    "v_mfma_f32_16x16x32_bf16 v[184:187], v[8:11], v[48:51], v[184:187]\n"
    "ds_read_b64_tr_b16 v[4:5], v236 offset:21632\n"
    "v_cmp_eq_u32_e64 vcc, -1, v250\n"
    "v_mfma_f32_16x16x32_bf16 v[180:183], v[8:11], v[44:47], v[180:183]\n"
    "s_waitcnt lgkmcnt(5)\n"
    "buffer_load_dwordx4 v[112:115], v251, s[32:35], s69 offen offset:3072 sc1\n"
    "s_cbranch_vccnz .Lk_ool_24\n"
    ".Lk_ret_25:\n"
    "v_mfma_f32_16x16x32_bf16 v[188:191], v[12:15], v[44:47], v[188:191]\n"
    "ds_read_b64_tr_b16 v[6:7], v236 offset:25216\n"
    "ds_read_b64_tr_b16 v[8:9], v236 offset:21664\n"
    "v_mfma_f32_16x16x32_bf16 v[192:195], v[12:15], v[48:51], v[192:195]\n"
    "s_waitcnt lgkmcnt(5)\n"
    "ds_read_b64_tr_b16 v[10:11], v236 offset:25248\n"
    "v_mov_b64 v[248:249], v[70:71]\n"
    "v_mfma_f32_16x16x32_bf16 v[200:203], v[16:19], v[48:51], v[200:203]\n"
    "buffer_load_dwordx4 v[116:119], v251, s[32:35], s70 offen offset:0 sc1\n"
    "v_mov_b64_e64 v[70:71], v[72:73]\n"
    "v_mfma_f32_16x16x32_bf16 v[196:199], v[16:19], v[44:47], v[196:199]\n"
    "s_waitcnt lgkmcnt(4)\n"
    "ds_read_b64_tr_b16 v[12:13], v236 offset:21696\n"
    "ds_read_b64_tr_b16 v[14:15], v236 offset:25280\n"
    "v_mov_b64 v[72:73], v[248:249]\n"
    "v_mfma_f32_16x16x32_bf16 v[204:207], v[244:247], v[44:47], v[204:207]\n"
    "ds_read_b64_tr_b16 v[16:17], v236 offset:40960\n"
    "v_mov_b64_e64 v[248:249], v[78:79]\n"
    "v_mfma_f32_16x16x32_bf16 v[208:211], v[244:247], v[48:51], v[208:211]\n"
    "s_waitcnt lgkmcnt(5)\n"
    "ds_read_b64_tr_b16 v[18:19], v236 offset:44544\n"
    "v_mov_b64 v[78:79], v[80:81]\n"
    "v_mfma_f32_16x16x32_bf16 v[216:219], v[4:7], v[48:51], v[216:219]\n"
    "buffer_load_dwordx4 v[120:123], v251, s[32:35], s70 offen offset:1024 sc1\n"
    "v_mfma_f32_16x16x32_bf16 v[212:215], v[4:7], v[44:47], v[212:215]\n"
    "s_waitcnt lgkmcnt(4)\n"
    "ds_read_b64_tr_b16 v[244:245], v236 offset:48128\n"
    "ds_read_b64_tr_b16 v[246:247], v236 offset:51712\n"
    "v_mfma_f32_16x16x32_bf16 v[220:223], v[8:11], v[44:47], v[220:223]\n"
    "s_waitcnt vmcnt(9)\n"
    "ds_read_b64_tr_b16 v[4:5], v236 offset:40992\n"
    "v_max3_u32 v250, v84, v86, v88\n"
    "v_mfma_f32_16x16x32_bf16 v[224:227], v[8:11], v[48:51], v[224:227]\n"
    "s_waitcnt lgkmcnt(5)\n"
    "buffer_load_dwordx4 v[124:127], v251, s[32:35], s70 offen offset:2048 sc1\n"
    "v_mfma_f32_16x16x32_bf16 v[232:235], v[12:15], v[48:51], v[232:235]\n"
    "ds_read_b64_tr_b16 v[6:7], v236 offset:44576\n"
    "ds_read_b64_tr_b16 v[8:9], v236 offset:48160\n"
    "v_mfma_f32_16x16x32_bf16 v[228:231], v[12:15], v[44:47], v[228:231]\n"
    "s_waitcnt lgkmcnt(5)\n"
    "ds_read_b64_tr_b16 v[10:11], v236 offset:51744\n"
    "v_mov_b64_e64 v[80:81], v[248:249]\n"
    "v_mfma_f32_16x16x32_bf16 v[180:183], v[16:19], v[52:55], v[180:183]\n"
    "s_waitcnt vmcnt(9)\n"
    "ds_read_b64_tr_b16 v[12:13], v236 offset:41024\n"
    "v_max3_u32 v250, v250, v90, v92\n"
    "v_mfma_f32_16x16x32_bf16 v[184:187], v[16:19], v[56:59], v[184:187]\n"
    "s_waitcnt vmcnt(8) lgkmcnt(5)\n"
    "buffer_load_dwordx4 v[128:131], v251, s[32:35], s70 offen offset:3072 sc1\n"
    "v_max3_u32 v250, v250, v94, v96\n"
    "v_mfma_f32_16x16x32_bf16 v[184:187], v[244:247], v[64:67], v[184:187]\n"
    "ds_read_b64_tr_b16 v[14:15], v236 offset:44608\n"
    "ds_read_b64_tr_b16 v[16:17], v236 offset:48192\n"
    "v_max_u32_e64 v250, v250, v98\n"
    "v_mfma_f32_16x16x32_bf16 v[180:183], v[244:247], v[60:63], v[180:183]\n"
    "s_waitcnt lgkmcnt(5)\n"
    "ds_read_b64_tr_b16 v[18:19], v236 offset:51776\n"
    "v_cmp_eq_u32 vcc, -1, v250\n"
    "v_mfma_f32_16x16x32_bf16 v[188:191], v[4:7], v[52:55], v[188:191]\n"
    "buffer_load_dwordx4 v[132:135], v251, s[32:35], s71 offen offset:0 sc1\n"
    "s_cbranch_vccnz .Lk_ool_26\n"
    ".Lk_ret_27:\n"
    "v_mfma_f32_16x16x32_bf16 v[192:195], v[4:7], v[56:59], v[192:195]\n"
    "s_waitcnt lgkmcnt(4)\n"
    "ds_read_b64_tr_b16 v[244:245], v236 offset:41056\n"
    "ds_read_b64_tr_b16 v[246:247], v236 offset:44640\n"
    "v_mfma_f32_16x16x32_bf16 v[192:195], v[8:11], v[64:67], v[192:195]\n"
    "ds_read_b64_tr_b16 v[4:5], v236 offset:48224\n"
    "v_mov_b64_e64 v[248:249], v[86:87]\n"
    "v_mfma_f32_16x16x32_bf16 v[188:191], v[8:11], v[60:63], v[188:191]\n"
    "s_waitcnt lgkmcnt(5)\n"
    "ds_read_b64_tr_b16 v[6:7], v236 offset:51808\n"
    "v_mov_b64 v[86:87], v[88:89]\n"
    "v_mfma_f32_16x16x32_bf16 v[196:199], v[12:15], v[52:55], v[196:199]\n"
    "buffer_load_dwordx4 v[136:139], v251, s[32:35], s71 offen offset:1024 sc1\n"
    "v_mov_b64_e64 v[88:89], v[248:249]\n"
    "v_mfma_f32_16x16x32_bf16 v[200:203], v[12:15], v[56:59], v[200:203]\n"
    "s_waitcnt vmcnt(10) lgkmcnt(4)\n"
    "ds_read_b64_tr_b16 v[8:9], v236 offset:41088\n"
    "ds_read_b64_tr_b16 v[10:11], v236 offset:44672\n"
    "v_xor_b32 v250, s51, v177\n"
    "v_mfma_f32_16x16x32_bf16 v[200:203], v[16:19], v[64:67], v[200:203]\n"
    "ds_read_b64_tr_b16 v[12:13], v236 offset:48256\n"
    "v_bitop3_b32 v250, v179, s51, v250 bitop3:0xbe\n"
    "v_mfma_f32_16x16x32_bf16 v[196:199], v[16:19], v[60:63], v[196:199]\n"
    "s_waitcnt lgkmcnt(5)\n"
    "buffer_load_dwordx4 v[140:143], v251, s[32:35], s71 offen offset:2048 sc1\n"
    "v_cmp_ne_u32 vcc, 0, v250\n"
    "v_mfma_f32_16x16x32_bf16 v[204:207], v[244:247], v[52:55], v[204:207]\n"
    "ds_read_b64_tr_b16 v[14:15], v236 offset:51840\n"
    "ds_read_b64_tr_b16 v[16:17], v236 offset:41120\n"
    "s_cbranch_vccnz .Lk_ool_28\n"
    ".Lk_ret_29:\n"
    "v_mfma_f32_16x16x32_bf16 v[208:211], v[244:247], v[56:59], v[208:211]\n"
    "s_waitcnt lgkmcnt(5)\n"
    "ds_read_b64_tr_b16 v[18:19], v236 offset:44704\n"
    "v_mov_b64_e64 v[248:249], v[94:95]\n"
    "v_mfma_f32_16x16x32_bf16 v[208:211], v[4:7], v[64:67], v[208:211]\n"
    "ds_read_b64_tr_b16 v[244:245], v236 offset:48288\n"
    "v_mov_b64_e64 v[94:95], v[96:97]\n"
    "v_mfma_f32_16x16x32_bf16 v[204:207], v[4:7], v[60:63], v[204:207]\n"
    "s_waitcnt lgkmcnt(5)\n"
    "buffer_load_dwordx4 v[144:147], v251, s[32:35], s71 offen offset:3072 sc1\n"
    "v_mfma_f32_16x16x32_bf16 v[212:215], v[8:11], v[52:55], v[212:215]\n"
    "s_waitcnt vmcnt(10)\n"
    "ds_read_b64_tr_b16 v[246:247], v236 offset:51872\n"
    "ds_read_b64_tr_b16 v[4:5], v236 offset:41152\n"
    "v_mov_b64_e64 v[96:97], v[248:249]\n"
    "v_max3_u32 v250, v100, v102, v104\n"
    "v_mfma_f32_16x16x32_bf16 v[216:219], v[8:11], v[56:59], v[216:219]\n"
    "s_waitcnt vmcnt(9) lgkmcnt(5)\n"
    "ds_read_b64_tr_b16 v[6:7], v236 offset:44736\n"
    "v_max3_u32 v250, v250, v106, v108\n"
    "v_mfma_f32_16x16x32_bf16 v[216:219], v[12:15], v[64:67], v[216:219]\n"
    "s_waitcnt vmcnt(8)\n"
    "buffer_load_dwordx4 v[148:151], v251, s[32:35], s72 offen offset:0 sc1\n"
    "v_max3_u32 v250, v250, v110, v112\n"
    "v_mfma_f32_16x16x32_bf16 v[212:215], v[12:15], v[60:63], v[212:215]\n"
    "s_waitcnt lgkmcnt(4)\n"
    "ds_read_b64_tr_b16 v[8:9], v236 offset:48320\n"
    "ds_read_b64_tr_b16 v[10:11], v236 offset:51904\n"
    "v_max_u32 v250, v250, v114\n"
    "v_mfma_f32_16x16x32_bf16 v[220:223], v[16:19], v[52:55], v[220:223]\n"
    "ds_read_b64_tr_b16 v[12:13], v236 offset:55296\n"
    "v_cmp_eq_u32_e64 vcc, -1, v250\n"
    "v_mfma_f32_16x16x32_bf16 v[224:227], v[16:19], v[56:59], v[224:227]\n"
    "s_waitcnt lgkmcnt(5)\n"
    "s_cbranch_vccnz .Lk_ool_30\n"
    ".Lk_ret_31:\n"
    "v_mov_b64_e64 v[248:249], v[102:103]\n"
    "v_mfma_f32_16x16x32_bf16 v[224:227], v[244:247], v[64:67], v[224:227]\n"
    "ds_read_b64_tr_b16 v[14:15], v236 offset:58880\n"
    "ds_read_b64_tr_b16 v[16:17], v236 offset:62464\n"
    "v_mfma_f32_16x16x32_bf16 v[220:223], v[244:247], v[60:63], v[220:223]\n"
    "s_waitcnt lgkmcnt(5)\n"
    "buffer_load_dwordx4 v[152:155], v251, s[32:35], s72 offen offset:1024 sc1\n"
    "v_mfma_f32_16x16x32_bf16 v[228:231], v[4:7], v[52:55], v[228:231]\n"
    "ds_read_b64_tr_b16 v[18:19], v237 offset:512\n"
    "ds_read_b64_tr_b16 v[244:245], v236 offset:55328\n"
    "v_mov_b64 v[102:103], v[104:105]\n"
    "v_mfma_f32_16x16x32_bf16 v[232:235], v[4:7], v[56:59], v[232:235]\n"
    "s_waitcnt lgkmcnt(5)\n"
    "buffer_load_dwordx4 v[156:159], v251, s[32:35], s72 offen offset:2048 sc1\n"
    "v_mov_b64 v[104:105], v[248:249]\n"
    "v_mfma_f32_16x16x32_bf16 v[232:235], v[8:11], v[64:67], v[232:235]\n"
    "ds_read_b64_tr_b16 v[246:247], v236 offset:58912\n"
    "ds_read_b64_tr_b16 v[4:5], v236 offset:62496\n"
    "s_barrier\n"
    "v_mfma_f32_16x16x32_bf16 v[228:231], v[8:11], v[60:63], v[228:231]\n"
    "s_waitcnt lgkmcnt(5)\n"
    "ds_write2_b32 v173, v176, v178 offset0:0 offset1:1\n"
    "v_mfma_f32_16x16x32_bf16 v[180:183], v[12:15], v[68:71], v[180:183]\n"
    "ds_read_b64_tr_b16 v[6:7], v237 offset:544\n"
    "ds_read_b64_tr_b16 v[8:9], v236 offset:55360\n"
    "v_mfma_f32_16x16x32_bf16 v[184:187], v[12:15], v[72:75], v[184:187]\n"
    "s_waitcnt lgkmcnt(6)\n"
    "buffer_load_dwordx4 v[160:163], v251, s[32:35], s72 offen offset:3072 sc1\n"
    "v_mfma_f32_16x16x32_bf16 v[184:187], v[16:19], v[80:83], v[184:187]\n"
    "ds_read_b64_tr_b16 v[10:11], v236 offset:58944\n"
    "ds_read_b64_tr_b16 v[12:13], v236 offset:62528\n"
    "v_mfma_f32_16x16x32_bf16 v[180:183], v[16:19], v[76:79], v[180:183]\n"
    "s_waitcnt lgkmcnt(6)\n"
    "ds_read_b64_tr_b16 v[14:15], v237 offset:576\n"
    "v_mov_b64_e64 v[248:249], v[110:111]\n"
    "v_mfma_f32_16x16x32_bf16 v[188:191], v[244:247], v[68:71], v[188:191]\n"
    "s_waitcnt vmcnt(10)\n"
    "buffer_load_dwordx4 v[164:167], v251, s[32:35], s73 offen offset:0 sc1\n"
    "v_mov_b64 v[110:111], v[112:113]\n"
    "v_max3_u32 v250, v116, v118, v120\n"
    "v_mfma_f32_16x16x32_bf16 v[192:195], v[244:247], v[72:75], v[192:195]\n"
    "s_waitcnt vmcnt(10) lgkmcnt(4)\n"
    "ds_read_b64_tr_b16 v[16:17], v236 offset:55392\n"
    "ds_read_b64_tr_b16 v[18:19], v236 offset:58976\n"
    "v_mov_b64 v[112:113], v[248:249]\n"
    "v_max3_u32 v250, v250, v122, v124\n"
    "v_mfma_f32_16x16x32_bf16 v[192:195], v[4:7], v[80:83], v[192:195]\n"
    "s_waitcnt vmcnt(9)\n"
    "ds_read_b64_tr_b16 v[244:245], v236 offset:62560\n"
    "v_max3_u32 v250, v250, v126, v128\n"
    "v_mfma_f32_16x16x32_bf16 v[188:191], v[4:7], v[76:79], v[188:191]\n"
    "s_waitcnt lgkmcnt(5)\n"
    "ds_read_b64_tr_b16 v[246:247], v237 offset:608\n"
    "v_max_u32_e64 v250, v250, v130\n"
    "v_mfma_f32_16x16x32_bf16 v[196:199], v[8:11], v[68:71], v[196:199]\n"
    "buffer_load_dwordx4 v[168:171], v251, s[32:35], s73 offen offset:1024 sc1\n"
    "v_cmp_eq_u32_e64 vcc, -1, v250\n"
    "v_mfma_f32_16x16x32_bf16 v[200:203], v[8:11], v[72:75], v[200:203]\n"
    "s_waitcnt lgkmcnt(4)\n"
    "ds_read_b64_tr_b16 v[4:5], v236 offset:55424\n"
    "ds_read_b64_tr_b16 v[6:7], v236 offset:59008\n"
    "s_cbranch_vccnz .Lk_ool_32\n"
    ".Lk_ret_33:\n"
    "v_mfma_f32_16x16x32_bf16 v[200:203], v[12:15], v[80:83], v[200:203]\n"
    "ds_read_b64_tr_b16 v[8:9], v236 offset:62592\n"
    "v_mov_b64_e64 v[248:249], v[118:119]\n"
    "v_mfma_f32_16x16x32_bf16 v[196:199], v[12:15], v[76:79], v[196:199]\n"
    "s_waitcnt lgkmcnt(5)\n"
    "buffer_load_dwordx4 v[172:175], v251, s[32:35], s73 offen offset:2048 sc1\n"
    "v_mfma_f32_16x16x32_bf16 v[204:207], v[16:19], v[68:71], v[204:207]\n"
    "ds_read_b64_tr_b16 v[10:11], v237 offset:640\n"
    "ds_read_b64_tr_b16 v[12:13], v236 offset:55456\n"
    "v_mov_b64 v[118:119], v[120:121]\n"
    "v_mfma_f32_16x16x32_bf16 v[208:211], v[16:19], v[72:75], v[208:211]\n"
    "s_waitcnt lgkmcnt(5)\n"
    "ds_read_b64_tr_b16 v[14:15], v236 offset:59040\n"
    "v_mov_b64 v[120:121], v[248:249]\n"
    "v_mfma_f32_16x16x32_bf16 v[208:211], v[244:247], v[80:83], v[208:211]\n"
    "ds_read_b64_tr_b16 v[16:17], v236 offset:62624\n"
    "v_mov_b64_e64 v[248:249], v[126:127]\n"
    "v_mfma_f32_16x16x32_bf16 v[204:207], v[244:247], v[76:79], v[204:207]\n"
    "s_waitcnt lgkmcnt(5)\n"
    "buffer_load_dwordx4 v[176:179], v251, s[32:35], s73 offen offset:3072 sc1\n"
    "v_mfma_f32_16x16x32_bf16 v[212:215], v[4:7], v[68:71], v[212:215]\n"
    "ds_read_b64_tr_b16 v[18:19], v237 offset:672\n"
    "ds_read_b64_tr_b16 v[244:245], v236 offset:55488\n"
    "s_add_u32 s3, s62, 28672\n"
    "v_mfma_f32_16x16x32_bf16 v[216:219], v[4:7], v[72:75], v[216:219]\n"
    "s_waitcnt lgkmcnt(5)\n"
    "ds_read_b64_tr_b16 v[246:247], v236 offset:59072\n"
    "v_mov_b64_e64 v[126:127], v[128:129]\n"
    "v_mfma_f32_16x16x32_bf16 v[216:219], v[8:11], v[80:83], v[216:219]\n"
    "buffer_load_dwordx4 a[112:115], v251, s[32:35], s3 offen offset:0 sc1\n"
    "v_mfma_f32_16x16x32_bf16 v[212:215], v[8:11], v[76:79], v[212:215]\n"
    "s_waitcnt lgkmcnt(4)\n"
    "ds_read_b64_tr_b16 v[4:5], v236 offset:62656\n"
    "ds_read_b64_tr_b16 v[6:7], v237 offset:704\n"
    "v_mfma_f32_16x16x32_bf16 v[220:223], v[12:15], v[68:71], v[220:223]\n"
    "s_waitcnt vmcnt(11)\n"
    "ds_read_b64_tr_b16 v[8:9], v237 offset:16384\n"
    "v_max3_u32 v250, v132, v134, v136\n"
    "v_mfma_f32_16x16x32_bf16 v[224:227], v[12:15], v[72:75], v[224:227]\n"
    "s_waitcnt lgkmcnt(5)\n"
    "buffer_load_dwordx4 a[116:119], v251, s[32:35], s3 offen offset:1024 sc1\n"
    "v_mfma_f32_16x16x32_bf16 v[224:227], v[16:19], v[80:83], v[224:227]\n"
    "s_waitcnt vmcnt(11)\n"
    "ds_read_b64_tr_b16 v[10:11], v237 offset:19968\n"
    "ds_read_b64_tr_b16 v[12:13], v237 offset:23552\n"
    "v_mov_b64_e64 v[128:129], v[248:249]\n"
    "v_max3_u32 v250, v250, v138, v140\n"
    "v_mfma_f32_16x16x32_bf16 v[220:223], v[16:19], v[76:79], v[220:223]\n"
    "s_waitcnt vmcnt(10) lgkmcnt(5)\n"
    "ds_read_b64_tr_b16 v[14:15], v237 offset:27136\n"
    "v_max3_u32 v250, v250, v142, v144\n"
    "v_mfma_f32_16x16x32_bf16 v[228:231], v[244:247], v[68:71], v[228:231]\n"
    "ds_read_b64_tr_b16 v[16:17], v237 offset:16416\n"
    "v_max_u32 v250, v250, v146\n"
    "v_mfma_f32_16x16x32_bf16 v[232:235], v[244:247], v[72:75], v[232:235]\n"
    "s_waitcnt lgkmcnt(5)\n"
    "buffer_load_dwordx4 a[120:123], v251, s[32:35], s3 offen offset:2048 sc1\n"
    "v_cmp_eq_u32 vcc, -1, v250\n"
    "v_mfma_f32_16x16x32_bf16 v[232:235], v[4:7], v[80:83], v[232:235]\n"
    "ds_read_b64_tr_b16 v[18:19], v237 offset:20000\n"
    "ds_read_b64_tr_b16 v[244:245], v237 offset:23584\n"
    "s_cbranch_vccnz .Lk_ool_34\n"
    ".Lk_ret_35:\n"
    "v_mfma_f32_16x16x32_bf16 v[228:231], v[4:7], v[76:79], v[228:231]\n"
    "s_waitcnt lgkmcnt(5)\n"
    "ds_read_b64_tr_b16 v[246:247], v237 offset:27168\n"
    "v_mov_b64_e64 v[248:249], v[134:135]\n"
    "v_mfma_f32_16x16x32_bf16 v[180:183], v[8:11], v[84:87], v[180:183]\n"
    "buffer_load_dwordx4 a[124:127], v251, s[32:35], s3 offen offset:3072 sc1\n"
    "v_mfma_f32_16x16x32_bf16 v[184:187], v[8:11], v[88:91], v[184:187]\n"
    "s_waitcnt lgkmcnt(4)\n"
    "ds_read_b64_tr_b16 v[4:5], v237 offset:16448\n"
    "ds_read_b64_tr_b16 v[6:7], v237 offset:20032\n"
    "v_mov_b64 v[134:135], v[136:137]\n"
    "v_mfma_f32_16x16x32_bf16 v[184:187], v[12:15], v[96:99], v[184:187]\n"
    "ds_read_b64_tr_b16 v[8:9], v237 offset:23616\n"
    "s_add_u32 s3, s62, 32768\n"
    "v_mov_b64_e64 v[136:137], v[248:249]\n"
    "v_mfma_f32_16x16x32_bf16 v[180:183], v[12:15], v[92:95], v[180:183]\n"
    "s_waitcnt lgkmcnt(5)\n"
    "ds_read_b64_tr_b16 v[10:11], v237 offset:27200\n"
    "v_mov_b64 v[248:249], v[142:143]\n"
    "v_mfma_f32_16x16x32_bf16 v[188:191], v[16:19], v[84:87], v[188:191]\n"
    "buffer_load_dwordx4 a[128:131], v251, s[32:35], s3 offen offset:0 sc1\n"
    "v_mfma_f32_16x16x32_bf16 v[192:195], v[16:19], v[88:91], v[192:195]\n"
    "s_waitcnt lgkmcnt(4)\n"
    "ds_read_b64_tr_b16 v[12:13], v237 offset:16480\n"
    "ds_read_b64_tr_b16 v[14:15], v237 offset:20064\n"
    "v_mfma_f32_16x16x32_bf16 v[192:195], v[244:247], v[96:99], v[192:195]\n"
    "ds_read_b64_tr_b16 v[16:17], v237 offset:23648\n"
    "v_mov_b64 v[142:143], v[144:145]\n"
    "v_mfma_f32_16x16x32_bf16 v[188:191], v[244:247], v[92:95], v[188:191]\n"
    "s_waitcnt lgkmcnt(5)\n"
    "buffer_load_dwordx4 a[132:135], v251, s[32:35], s3 offen offset:1024 sc1\n"
    "v_mfma_f32_16x16x32_bf16 v[196:199], v[4:7], v[84:87], v[196:199]\n"
    "ds_read_b64_tr_b16 v[18:19], v237 offset:27232\n"
    "ds_read_b64_tr_b16 v[244:245], v237 offset:16512\n"
    "v_mfma_f32_16x16x32_bf16 v[200:203], v[4:7], v[88:91], v[200:203]\n"
    "s_waitcnt lgkmcnt(5)\n"
    "ds_read_b64_tr_b16 v[246:247], v237 offset:20096\n"
    "v_mov_b64_e64 v[144:145], v[248:249]\n"
    "v_mfma_f32_16x16x32_bf16 v[200:203], v[8:11], v[96:99], v[200:203]\n"
    "s_waitcnt vmcnt(12)\n"
    "ds_read_b64_tr_b16 v[4:5], v237 offset:23680\n"
    "v_max3_u32 v250, v148, v150, v152\n"
    "v_mfma_f32_16x16x32_bf16 v[196:199], v[8:11], v[92:95], v[196:199]\n"
    "s_waitcnt vmcnt(11) lgkmcnt(5)\n"
    "buffer_load_dwordx4 a[136:139], v251, s[32:35], s3 offen offset:2048 sc1\n"
    "v_max3_u32 v250, v250, v154, v156\n"
    "v_mfma_f32_16x16x32_bf16 v[204:207], v[12:15], v[84:87], v[204:207]\n"
    "s_waitcnt vmcnt(11)\n"
    "ds_read_b64_tr_b16 v[6:7], v237 offset:27264\n"
    "ds_read_b64_tr_b16 v[8:9], v237 offset:16544\n"
    "v_max3_u32 v250, v250, v158, v160\n"
    "v_mfma_f32_16x16x32_bf16 v[208:211], v[12:15], v[88:91], v[208:211]\n"
    "s_waitcnt lgkmcnt(5)\n"
    "ds_read_b64_tr_b16 v[10:11], v237 offset:20128\n"
    "v_max_u32_e64 v250, v250, v162\n"
    "v_mfma_f32_16x16x32_bf16 v[208:211], v[16:19], v[96:99], v[208:211]\n"
    "buffer_load_dwordx4 a[140:143], v251, s[32:35], s3 offen offset:3072 sc1\n"
    "v_cmp_eq_u32_e64 vcc, -1, v250\n"
    "v_mfma_f32_16x16x32_bf16 v[204:207], v[16:19], v[92:95], v[204:207]\n"
    "s_waitcnt lgkmcnt(4)\n"
    "ds_read_b64_tr_b16 v[12:13], v237 offset:23712\n"
    "ds_read_b64_tr_b16 v[14:15], v237 offset:27296\n"
    "s_cbranch_vccnz .Lk_ool_36\n"
    ".Lk_ret_37:\n"
    "v_mfma_f32_16x16x32_bf16 v[212:215], v[244:247], v[84:87], v[212:215]\n"
    "ds_read_b64_tr_b16 v[16:17], v237 offset:16576\n"
    "s_add_u32 s3, s62, 36864\n"
    "v_mfma_f32_16x16x32_bf16 v[216:219], v[244:247], v[88:91], v[216:219]\n"
    "s_waitcnt lgkmcnt(5)\n"
    "ds_read_b64_tr_b16 v[18:19], v237 offset:20160\n"
    "v_mov_b64 v[248:249], v[150:151]\n"
    "v_mfma_f32_16x16x32_bf16 v[216:219], v[4:7], v[96:99], v[216:219]\n"
    "buffer_load_dwordx4 a[144:147], v251, s[32:35], s3 offen offset:0 sc1\n"
    "v_mov_b64_e64 v[150:151], v[152:153]\n"
    "v_mfma_f32_16x16x32_bf16 v[212:215], v[4:7], v[92:95], v[212:215]\n"
    "s_waitcnt lgkmcnt(4)\n"
    "ds_read_b64_tr_b16 v[244:245], v237 offset:23744\n"
    "ds_read_b64_tr_b16 v[246:247], v237 offset:27328\n"
    "v_mov_b64 v[152:153], v[248:249]\n"
    "v_mfma_f32_16x16x32_bf16 v[220:223], v[8:11], v[84:87], v[220:223]\n"
    "ds_read_b64_tr_b16 v[4:5], v237 offset:30912\n"
    "v_mov_b64_e64 v[248:249], v[158:159]\n"
    "v_mfma_f32_16x16x32_bf16 v[224:227], v[8:11], v[88:91], v[224:227]\n"
    "s_waitcnt lgkmcnt(5)\n"
    "buffer_load_dwordx4 a[148:151], v251, s[32:35], s3 offen offset:1024 sc1\n"
    "v_mfma_f32_16x16x32_bf16 v[224:227], v[12:15], v[96:99], v[224:227]\n"
    "ds_read_b64_tr_b16 v[6:7], v237 offset:34496\n"
    "ds_read_b64_tr_b16 v[8:9], v237 offset:38080\n"
    "v_mfma_f32_16x16x32_bf16 v[220:223], v[12:15], v[92:95], v[220:223]\n"
    "s_waitcnt lgkmcnt(5)\n"
    "ds_read_b64_tr_b16 v[10:11], v237 offset:41664\n"
    "v_mov_b64_e64 v[158:159], v[160:161]\n"
    "v_mfma_f32_16x16x32_bf16 v[228:231], v[16:19], v[84:87], v[228:231]\n"
    "s_waitcnt vmcnt(12)\n"
    "ds_read_b64_tr_b16 v[12:13], v237 offset:57536\n"
    "v_max3_u32 v250, v164, v166, v168\n"
    "v_mfma_f32_16x16x32_bf16 v[232:235], v[16:19], v[88:91], v[232:235]\n"
    "s_waitcnt lgkmcnt(5)\n"
    "buffer_load_dwordx4 a[152:155], v251, s[32:35], s3 offen offset:2048 sc1\n"
    "v_mfma_f32_16x16x32_bf16 v[232:235], v[244:247], v[96:99], v[232:235]\n"
    "ds_read_b64_tr_b16 v[14:15], v237 offset:61120\n"
    "ds_read_b64_tr_b16 v[16:17], v237 offset:64704\n"
    "v_mfma_f32_16x16x32_bf16 v[228:231], v[244:247], v[92:95], v[228:231]\n"
    "s_waitcnt vmcnt(12) lgkmcnt(5)\n"
    "ds_read_b64_tr_b16 v[18:19], v238 offset:2752\n"
    "v_mov_b64 v[160:161], v[248:249]\n"
    "v_max3_u32 v250, v250, v170, v172\n"
    "v_mfma_f32_16x16x32_bf16 v[228:231], v[4:7], v[100:103], v[228:231]\n"
    "s_waitcnt vmcnt(11)\n"
    "buffer_load_dwordx4 a[156:159], v251, s[32:35], s3 offen offset:3072 sc1\n"
    "v_max3_u32 v250, v250, v174, v176\n"
    "v_mfma_f32_16x16x32_bf16 v[232:235], v[4:7], v[104:107], v[232:235]\n"
    "s_waitcnt lgkmcnt(4)\n"
    "ds_read_b64_tr_b16 v[244:245], v238 offset:6336\n"
    "ds_read_b64_tr_b16 v[246:247], v238 offset:9920\n"
    "v_max_u32_e64 v250, v250, v178\n"
    "v_mfma_f32_16x16x32_bf16 v[232:235], v[8:11], v[112:115], v[232:235]\n"
    "ds_read_b64_tr_b16 v[4:5], v238 offset:13504\n"
    "s_add_u32 s3, s62, 40960\n"
    "v_cmp_eq_u32_e64 vcc, -1, v250\n"
    "v_mfma_f32_16x16x32_bf16 v[228:231], v[8:11], v[108:111], v[228:231]\n"
    "s_waitcnt lgkmcnt(5)\n"
    "s_cbranch_vccnz .Lk_ool_38\n"
    ".Lk_ret_39:\n"
    "v_mov_b64_e64 v[248:249], v[166:167]\n"
    "v_mfma_f32_16x16x32_bf16 v[228:231], v[12:15], v[116:119], v[228:231]\n"
    "ds_read_b64_tr_b16 v[6:7], v238 offset:17088\n"
    "ds_read_b64_tr_b16 v[8:9], v236 offset:28864\n"
    "v_mfma_f32_16x16x32_bf16 v[232:235], v[12:15], v[120:123], v[232:235]\n"
    "s_waitcnt lgkmcnt(5)\n"
    "buffer_load_dwordx4 a[160:163], v251, s[32:35], s3 offen offset:0 sc1\n"
    "v_mfma_f32_16x16x32_bf16 v[232:235], v[16:19], v[128:131], v[232:235]\n"
    "ds_read_b64_tr_b16 v[10:11], v236 offset:32448\n"
    "ds_read_b64_tr_b16 v[12:13], v237 offset:4288\n"
    "v_mov_b64 v[166:167], v[168:169]\n"
    "v_mfma_f32_16x16x32_bf16 v[228:231], v[16:19], v[124:127], v[228:231]\n"
    "s_waitcnt lgkmcnt(5)\n"
    "buffer_load_dwordx4 a[164:167], v251, s[32:35], s3 offen offset:1024 sc1\n"
    "v_mov_b64 v[168:169], v[248:249]\n"
    "v_mfma_f32_16x16x32_bf16 v[228:231], v[244:247], v[132:135], v[228:231]\n"
    "ds_read_b64_tr_b16 v[14:15], v237 offset:7872\n"
    "ds_read_b64_tr_b16 v[16:17], v237 offset:45248\n"
    "v_mfma_f32_16x16x32_bf16 v[232:235], v[244:247], v[136:139], v[232:235]\n"
    "s_waitcnt lgkmcnt(5)\n"
    "ds_read_b64_tr_b16 v[18:19], v237 offset:48832\n"
    "v_mov_b64 v[248:249], v[174:175]\n"
    "v_mfma_f32_16x16x32_bf16 v[232:235], v[4:7], v[144:147], v[232:235]\n"
    "ds_read_b64_tr_b16 v[244:245], v238 offset:20672\n"
    "v_mov_b64_e64 v[174:175], v[176:177]\n"
    "v_mfma_f32_16x16x32_bf16 v[228:231], v[4:7], v[140:143], v[228:231]\n"
    "s_waitcnt lgkmcnt(5)\n"
    "buffer_load_dwordx4 a[168:171], v251, s[32:35], s3 offen offset:2048 sc1\n"
    "v_mfma_f32_16x16x32_bf16 v[228:231], v[8:11], v[148:151], v[228:231]\n"
    "ds_read_b64_tr_b16 v[246:247], v238 offset:24256\n"
    "ds_read_b64_tr_b16 v[4:5], v237 offset:30720\n"
    "v_mfma_f32_16x16x32_bf16 v[232:235], v[8:11], v[152:155], v[232:235]\n"
    "s_waitcnt lgkmcnt(5)\n"
    "ds_read_b64_tr_b16 v[6:7], v237 offset:34304\n"
    "v_mov_b64_e64 v[176:177], v[248:249]\n"
    "v_mfma_f32_16x16x32_bf16 v[232:235], v[12:15], v[160:163], v[232:235]\n"
    "buffer_load_dwordx4 a[172:175], v251, s[32:35], s3 offen offset:3072 sc1\n"
    "v_mfma_f32_16x16x32_bf16 v[228:231], v[12:15], v[156:159], v[228:231]\n"
    "s_waitcnt lgkmcnt(4)\n"
    "ds_read_b64_tr_b16 v[8:9], v237 offset:37888\n"
    "ds_read_b64_tr_b16 v[10:11], v237 offset:41472\n"
    "v_mfma_f32_16x16x32_bf16 v[228:231], v[16:19], v[164:167], v[228:231]\n"
    "ds_read_b64_tr_b16 v[12:13], v237 offset:57344\n"
    "s_add_u32 s3, s62, 45056\n"
    "v_mfma_f32_16x16x32_bf16 v[232:235], v[16:19], v[168:171], v[232:235]\n"
    "s_waitcnt lgkmcnt(5)\n"
    "ds_read_b64_tr_b16 v[14:15], v237 offset:60928\n"
    "v_lshlrev_b32_e64 v250, 2, v1\n"
    "v_mfma_f32_16x16x32_bf16 v[232:235], v[244:247], v[176:179], v[232:235]\n"
    "buffer_load_dwordx4 a[176:179], v251, s[32:35], s3 offen offset:0 sc1\n"
    "v_mfma_f32_16x16x32_bf16 v[228:231], v[244:247], v[172:175], v[228:231]\n"
    "s_waitcnt lgkmcnt(4)\n"
    "ds_read_b64_tr_b16 v[16:17], v237 offset:64512\n"
    "ds_read_b64_tr_b16 v[18:19], v238 offset:2560\n"
    "v_mfma_f32_16x16x32_bf16 v[180:183], v[4:7], v[100:103], v[180:183]\n"
    "ds_read_b64_tr_b16 v[244:245], v238 offset:6144\n"
    "v_add_u32 v250, 128, v250\n"
    "v_mfma_f32_16x16x32_bf16 v[184:187], v[4:7], v[104:107], v[184:187]\n"
    "s_waitcnt lgkmcnt(5)\n"
    "buffer_load_dwordx4 a[180:183], v251, s[32:35], s3 offen offset:1024 sc1\n"
    "v_mfma_f32_16x16x32_bf16 v[184:187], v[8:11], v[112:115], v[184:187]\n"
    "ds_read_b64_tr_b16 v[246:247], v238 offset:9728\n"
    "ds_read_b64_tr_b16 v[4:5], v238 offset:13312\n"
    "v_mfma_f32_16x16x32_bf16 v[180:183], v[8:11], v[108:111], v[180:183]\n"
    "s_waitcnt lgkmcnt(5)\n"
    "ds_read_b64_tr_b16 v[6:7], v238 offset:16896\n"
    "ds_bpermute_b32 v248, v250, v228\n"
    "v_mfma_f32_16x16x32_bf16 v[180:183], v[12:15], v[116:119], v[180:183]\n"
    "ds_read_b64_tr_b16 v[8:9], v236 offset:28672\n"
    "ds_bpermute_b32 v249, v250, v232\n"
    "v_mfma_f32_16x16x32_bf16 v[184:187], v[12:15], v[120:123], v[184:187]\n"
    "s_waitcnt lgkmcnt(7)\n"
    "ds_read_b128 v[240:243], v3 offset:36736\n"
    "s_lshl_b32 s6, s48, 7\n"
    "v_mfma_f32_16x16x32_bf16 v[184:187], v[16:19], v[128:131], v[184:187]\n"
    "ds_read_b64_tr_b16 v[10:11], v236 offset:32256\n"
    "ds_read_b64_tr_b16 v[12:13], v237 offset:4096\n"
    "v_mfma_f32_16x16x32_bf16 v[180:183], v[16:19], v[124:127], v[180:183]\n"
    "s_waitcnt lgkmcnt(8)\n"
    "buffer_load_dwordx4 a[184:187], v251, s[32:35], s3 offen offset:2048 sc1\n"
    "v_mfma_f32_16x16x32_bf16 v[180:183], v[244:247], v[132:135], v[180:183]\n"
    "s_waitcnt lgkmcnt(3)\n"
    "ds_read_b64_tr_b16 v[14:15], v237 offset:7680\n"
    "ds_read_b64_tr_b16 v[16:17], v237 offset:45056\n"
    "v_mul_f32 v248, 0xbacccccd, v248\n"
    "v_mul_f32 v249, 0xbacccccd, v249\n"
    "v_mfma_f32_16x16x32_bf16 v[184:187], v[244:247], v[136:139], v[184:187]\n"
    "s_waitcnt lgkmcnt(4)\n"
    "buffer_load_dwordx4 a[188:191], v251, s[32:35], s3 offen offset:3072 sc1\n"
    "v_fmac_f32_e64 v228, v240, v248\n"
    "v_mfma_f32_16x16x32_bf16 v[184:187], v[4:7], v[144:147], v[184:187]\n"
    "ds_read_b64_tr_b16 v[18:19], v237 offset:48640\n"
    "ds_read_b64_tr_b16 v[244:245], v238 offset:20480\n"
    "s_add_u32 s3, s62, 49152\n"
    "v_mfma_f32_16x16x32_bf16 v[180:183], v[4:7], v[140:143], v[180:183]\n"
    "s_waitcnt lgkmcnt(5)\n"
    "ds_read_b64_tr_b16 v[246:247], v238 offset:24064\n"
    "v_fmac_f32 v229, v241, v248\n"
    "v_mfma_f32_16x16x32_bf16 v[180:183], v[8:11], v[148:151], v[180:183]\n"
    "buffer_load_dwordx4 a[192:195], v251, s[32:35], s3 offen offset:0 sc1\n"
    "v_fmac_f32_e64 v230, v242, v248\n"
    "v_mfma_f32_16x16x32_bf16 v[184:187], v[8:11], v[152:155], v[184:187]\n"
    "s_waitcnt lgkmcnt(4)\n"
    "ds_read_b64_tr_b16 v[4:5], v237 offset:30752\n"
    "ds_read_b64_tr_b16 v[6:7], v237 offset:34336\n"
    "v_fmac_f32 v231, v243, v248\n"
    "v_mfma_f32_16x16x32_bf16 v[184:187], v[12:15], v[160:163], v[184:187]\n"
    "ds_read_b64_tr_b16 v[8:9], v237 offset:37920\n"
    "v_fmac_f32_e64 v232, v240, v249\n"
    "v_mfma_f32_16x16x32_bf16 v[180:183], v[12:15], v[156:159], v[180:183]\n"
    "s_waitcnt lgkmcnt(5)\n"
    "buffer_load_dwordx4 a[196:199], v251, s[32:35], s3 offen offset:1024 sc1\n"
    "v_fmac_f32 v233, v241, v249\n"
    "v_mfma_f32_16x16x32_bf16 v[180:183], v[16:19], v[164:167], v[180:183]\n"
    "ds_read_b64_tr_b16 v[10:11], v237 offset:41504\n"
    "ds_read_b64_tr_b16 v[12:13], v237 offset:57376\n"
    "v_fmac_f32_e64 v234, v242, v249\n"
    "v_mfma_f32_16x16x32_bf16 v[184:187], v[16:19], v[168:171], v[184:187]\n"
    "s_waitcnt lgkmcnt(5)\n"
    "ds_read_b64_tr_b16 v[14:15], v237 offset:60960\n"
    "v_fmac_f32 v235, v243, v249\n"
    "v_mfma_f32_16x16x32_bf16 v[184:187], v[244:247], v[176:179], v[184:187]\n"
    "ds_read_b64_tr_b16 v[16:17], v237 offset:64544\n"
    "v_cvt_pk_bf16_f32 v228, v228, v229\n"
    "v_mfma_f32_16x16x32_bf16 v[180:183], v[244:247], v[172:175], v[180:183]\n"
    "s_waitcnt lgkmcnt(5)\n"
    "buffer_load_dwordx4 a[200:203], v251, s[32:35], s3 offen offset:2048 sc1\n"
    "v_cvt_pk_bf16_f32 v229, v230, v231\n"
    "v_mfma_f32_16x16x32_bf16 v[188:191], v[4:7], v[100:103], v[188:191]\n"
    "ds_read_b64_tr_b16 v[18:19], v238 offset:2592\n"
    "ds_read_b64_tr_b16 v[244:245], v238 offset:6176\n"
    "v_cvt_pk_bf16_f32 v230, v232, v233\n"
    "v_mfma_f32_16x16x32_bf16 v[192:195], v[4:7], v[104:107], v[192:195]\n"
    "s_waitcnt lgkmcnt(5)\n"
    "ds_read_b64_tr_b16 v[246:247], v238 offset:9760\n"
    "v_cvt_pk_bf16_f32 v231, v234, v235\n"
    "v_mfma_f32_16x16x32_bf16 v[192:195], v[8:11], v[112:115], v[192:195]\n"
    "buffer_load_dwordx4 a[204:207], v251, s[32:35], s3 offen offset:3072 sc1\n"
    "v_pk_max_i16 v228, v228, 0\n"
    "v_mfma_f32_16x16x32_bf16 v[188:191], v[8:11], v[108:111], v[188:191]\n"
    "s_waitcnt lgkmcnt(4)\n"
    "ds_read_b64_tr_b16 v[4:5], v238 offset:13344\n"
    "ds_read_b64_tr_b16 v[6:7], v238 offset:16928\n"
    "v_pk_max_i16 v229, v229, 0\n"
    "v_mfma_f32_16x16x32_bf16 v[188:191], v[12:15], v[116:119], v[188:191]\n"
    "ds_read_b64_tr_b16 v[8:9], v236 offset:28704\n"
    "s_add_u32 s3, s62, 53248\n"
    "v_pk_max_i16 v230, v230, 0\n"
    "v_mfma_f32_16x16x32_bf16 v[192:195], v[12:15], v[120:123], v[192:195]\n"
    "s_waitcnt lgkmcnt(5)\n"
    "ds_read_b128 v[240:243], v3 offset:36352\n"
    "v_pk_max_i16 v231, v231, 0\n"
    "v_mfma_f32_16x16x32_bf16 v[192:195], v[16:19], v[128:131], v[192:195]\n"
    "ds_read_b64_tr_b16 v[10:11], v236 offset:32288\n"
    "ds_read_b64_tr_b16 v[12:13], v237 offset:4128\n"
    "v_or_b32_e64 v228, v228, v252\n"
    "v_mfma_f32_16x16x32_bf16 v[188:191], v[16:19], v[124:127], v[188:191]\n"
    "s_waitcnt lgkmcnt(6)\n"
    "buffer_load_dwordx4 a[208:211], v251, s[32:35], s3 offen offset:0 sc1\n"
    "v_or_b32 v229, v229, v253\n"
    "v_mfma_f32_16x16x32_bf16 v[188:191], v[244:247], v[132:135], v[188:191]\n"
    "ds_read_b64_tr_b16 v[14:15], v237 offset:7712\n"
    "ds_read_b64_tr_b16 v[16:17], v237 offset:45088\n"
    "v_or_b32_e64 v230, v230, v254\n"
    "v_mfma_f32_16x16x32_bf16 v[192:195], v[244:247], v[136:139], v[192:195]\n"
    "s_waitcnt lgkmcnt(4)\n"
    "buffer_load_dwordx4 a[212:215], v251, s[32:35], s3 offen offset:1024 sc1\n"
    "v_or_b32 v231, v231, v255\n"
    "v_fmac_f32_e64 v180, v240, v248\n"
    "v_mfma_f32_16x16x32_bf16 v[192:195], v[4:7], v[144:147], v[192:195]\n"
    "ds_read_b64_tr_b16 v[18:19], v237 offset:48672\n"
    "ds_read_b64_tr_b16 v[244:245], v238 offset:20512\n"
    "v_mfma_f32_16x16x32_bf16 v[188:191], v[4:7], v[140:143], v[188:191]\n"
    "s_waitcnt lgkmcnt(5)\n"
    "buffer_store_dwordx4 v[228:231], v239, s[32:35], s60 offen sc1\n"
    "v_fmac_f32 v181, v241, v248\n"
    "v_mfma_f32_16x16x32_bf16 v[188:191], v[8:11], v[148:151], v[188:191]\n"
    "ds_read_b64_tr_b16 v[246:247], v238 offset:24096\n"
    "ds_read_b64_tr_b16 v[4:5], v237 offset:30784\n"
    "v_fmac_f32_e64 v182, v242, v248\n"
    "v_mfma_f32_16x16x32_bf16 v[192:195], v[8:11], v[152:155], v[192:195]\n"
    "s_waitcnt lgkmcnt(5)\n"
    "buffer_load_dwordx4 a[216:219], v251, s[32:35], s3 offen offset:2048 sc1\n"
    "v_fmac_f32 v183, v243, v248\n"
    "v_mfma_f32_16x16x32_bf16 v[192:195], v[12:15], v[160:163], v[192:195]\n"
    "ds_read_b64_tr_b16 v[6:7], v237 offset:34368\n"
    "ds_read_b64_tr_b16 v[8:9], v237 offset:37952\n"
    "v_fmac_f32_e64 v184, v240, v249\n"
    "v_mfma_f32_16x16x32_bf16 v[188:191], v[12:15], v[156:159], v[188:191]\n"
    "s_waitcnt lgkmcnt(5)\n"
    "ds_read_b64_tr_b16 v[10:11], v237 offset:41536\n"
    "v_fmac_f32 v185, v241, v249\n"
    "v_mfma_f32_16x16x32_bf16 v[188:191], v[16:19], v[164:167], v[188:191]\n"
    "buffer_load_dwordx4 a[220:223], v251, s[32:35], s3 offen offset:3072 sc1\n"
    "v_fmac_f32_e64 v186, v242, v249\n"
    "v_mfma_f32_16x16x32_bf16 v[192:195], v[16:19], v[168:171], v[192:195]\n"
    "s_waitcnt lgkmcnt(4)\n"
    "ds_read_b64_tr_b16 v[12:13], v237 offset:57408\n"
    "ds_read_b64_tr_b16 v[14:15], v237 offset:60992\n"
    "v_fmac_f32 v187, v243, v249\n"
    "v_mfma_f32_16x16x32_bf16 v[192:195], v[244:247], v[176:179], v[192:195]\n"
    "ds_read_b64_tr_b16 v[16:17], v237 offset:64576\n"
    "s_add_u32 s3, s62, 57344\n"
    "v_cvt_pk_bf16_f32 v180, v180, v181\n"
    "v_mfma_f32_16x16x32_bf16 v[188:191], v[244:247], v[172:175], v[188:191]\n"
    "s_waitcnt lgkmcnt(5)\n"
    "ds_read_b64_tr_b16 v[18:19], v238 offset:2624\n"
    "v_cvt_pk_bf16_f32 v181, v182, v183\n"
    "v_mfma_f32_16x16x32_bf16 v[196:199], v[4:7], v[100:103], v[196:199]\n"
    "buffer_load_dwordx4 a[224:227], v251, s[32:35], s3 offen offset:0 sc1\n"
    "v_cvt_pk_bf16_f32 v182, v184, v185\n"
    "v_mfma_f32_16x16x32_bf16 v[200:203], v[4:7], v[104:107], v[200:203]\n"
    "s_waitcnt lgkmcnt(4)\n"
    "ds_read_b64_tr_b16 v[244:245], v238 offset:6208\n"
    "ds_read_b64_tr_b16 v[246:247], v238 offset:9792\n"
    "v_cvt_pk_bf16_f32 v183, v186, v187\n"
    "v_mfma_f32_16x16x32_bf16 v[200:203], v[8:11], v[112:115], v[200:203]\n"
    "ds_read_b64_tr_b16 v[4:5], v238 offset:13376\n"
    "v_pk_max_i16 v180, v180, 0\n"
    "v_mfma_f32_16x16x32_bf16 v[196:199], v[8:11], v[108:111], v[196:199]\n"
    "s_waitcnt lgkmcnt(5)\n"
    "ds_read_b128 v[240:243], v3 offset:36416\n"
    "v_pk_max_i16 v181, v181, 0\n"
    "v_mfma_f32_16x16x32_bf16 v[196:199], v[12:15], v[116:119], v[196:199]\n"
    "ds_read_b64_tr_b16 v[6:7], v238 offset:16960\n"
    "ds_read_b64_tr_b16 v[8:9], v236 offset:28736\n"
    "v_pk_max_i16 v182, v182, 0\n"
    "v_mfma_f32_16x16x32_bf16 v[200:203], v[12:15], v[120:123], v[200:203]\n"
    "s_waitcnt lgkmcnt(6)\n"
    "buffer_load_dwordx4 a[228:231], v251, s[32:35], s3 offen offset:1024 sc1\n"
    "v_pk_max_i16 v183, v183, 0\n"
    "v_mfma_f32_16x16x32_bf16 v[200:203], v[16:19], v[128:131], v[200:203]\n"
    "ds_read_b64_tr_b16 v[10:11], v236 offset:32320\n"
    "ds_read_b64_tr_b16 v[12:13], v237 offset:4160\n"
    "v_or_b32_e64 v180, v180, v252\n"
    "v_mfma_f32_16x16x32_bf16 v[196:199], v[16:19], v[124:127], v[196:199]\n"
    "s_waitcnt lgkmcnt(6)\n"
    "buffer_load_dwordx4 a[232:235], v251, s[32:35], s3 offen offset:2048 sc1\n"
    "v_or_b32 v181, v181, v253\n"
    "v_mfma_f32_16x16x32_bf16 v[196:199], v[244:247], v[132:135], v[196:199]\n"
    "ds_read_b64_tr_b16 v[14:15], v237 offset:7744\n"
    "ds_read_b64_tr_b16 v[16:17], v237 offset:45120\n"
    "v_or_b32_e64 v182, v182, v254\n"
    "v_mfma_f32_16x16x32_bf16 v[200:203], v[244:247], v[136:139], v[200:203]\n"
    "s_waitcnt lgkmcnt(5)\n"
    "ds_read_b64_tr_b16 v[18:19], v237 offset:48704\n"
    "v_or_b32 v183, v183, v255\n"
    "v_fmac_f32 v188, v240, v248\n"
    "v_fmac_f32 v189, v241, v248\n"
    "v_mfma_f32_16x16x32_bf16 v[200:203], v[4:7], v[144:147], v[200:203]\n"
    "buffer_load_dwordx4 a[236:239], v251, s[32:35], s3 offen offset:3072 sc1\n"
    "v_fmac_f32_e64 v190, v242, v248\n"
    "v_mfma_f32_16x16x32_bf16 v[196:199], v[4:7], v[140:143], v[196:199]\n"
    "s_waitcnt lgkmcnt(4)\n"
    "ds_read_b64_tr_b16 v[244:245], v238 offset:20544\n"
    "ds_read_b64_tr_b16 v[246:247], v238 offset:24128\n"
    "v_fmac_f32 v191, v243, v248\n"
    "v_mfma_f32_16x16x32_bf16 v[196:199], v[8:11], v[148:151], v[196:199]\n"
    "buffer_store_dwordx4 v[180:183], v251, s[32:35], s40 offen sc1\n"
    "s_add_u32 s3, s62, 61440\n"
    "v_fmac_f32_e64 v192, v240, v249\n"
    "v_mfma_f32_16x16x32_bf16 v[200:203], v[8:11], v[152:155], v[200:203]\n"
    "s_waitcnt lgkmcnt(4)\n"
    "ds_read_b64_tr_b16 v[4:5], v237 offset:30816\n"
    "ds_read_b64_tr_b16 v[6:7], v237 offset:34400\n"
    "v_fmac_f32 v193, v241, v249\n"
    "v_mfma_f32_16x16x32_bf16 v[200:203], v[12:15], v[160:163], v[200:203]\n"
    "buffer_load_dwordx4 a[240:243], v251, s[32:35], s3 offen offset:0 sc1\n"
    "v_fmac_f32_e64 v194, v242, v249\n"
    "v_mfma_f32_16x16x32_bf16 v[196:199], v[12:15], v[156:159], v[196:199]\n"
    "s_waitcnt lgkmcnt(4)\n"
    "ds_read_b64_tr_b16 v[8:9], v237 offset:37984\n"
    "ds_read_b64_tr_b16 v[10:11], v237 offset:41568\n"
    "v_fmac_f32 v195, v243, v249\n"
    "v_mfma_f32_16x16x32_bf16 v[196:199], v[16:19], v[164:167], v[196:199]\n"
    "ds_read_b64_tr_b16 v[12:13], v237 offset:57440\n"
    "v_cvt_pk_bf16_f32 v188, v188, v189\n"
    "v_mfma_f32_16x16x32_bf16 v[200:203], v[16:19], v[168:171], v[200:203]\n"
    "s_waitcnt lgkmcnt(5)\n"
    "buffer_load_dwordx4 a[244:247], v251, s[32:35], s3 offen offset:1024 sc1\n"
    "v_cvt_pk_bf16_f32 v189, v190, v191\n"
    "v_mfma_f32_16x16x32_bf16 v[200:203], v[244:247], v[176:179], v[200:203]\n"
    "ds_read_b64_tr_b16 v[14:15], v237 offset:61024\n"
    "ds_read_b64_tr_b16 v[16:17], v237 offset:64608\n"
    "v_cvt_pk_bf16_f32 v190, v192, v193\n"
    "v_mfma_f32_16x16x32_bf16 v[196:199], v[244:247], v[172:175], v[196:199]\n"
    "s_waitcnt lgkmcnt(5)\n"
    "ds_read_b64_tr_b16 v[18:19], v238 offset:2656\n"
    "v_cvt_pk_bf16_f32 v191, v194, v195\n"
    "v_mfma_f32_16x16x32_bf16 v[204:207], v[4:7], v[100:103], v[204:207]\n"
    "ds_read_b64_tr_b16 v[244:245], v238 offset:6240\n"
    "v_pk_max_i16 v188, v188, 0\n"
    "v_mfma_f32_16x16x32_bf16 v[208:211], v[4:7], v[104:107], v[208:211]\n"
    "s_waitcnt lgkmcnt(5)\n"
    "buffer_load_dwordx4 a[248:251], v251, s[32:35], s3 offen offset:2048 sc1\n"
    "v_pk_max_i16 v189, v189, 0\n"
    "v_mfma_f32_16x16x32_bf16 v[208:211], v[8:11], v[112:115], v[208:211]\n"
    "ds_read_b64_tr_b16 v[246:247], v238 offset:9824\n"
    "ds_read_b64_tr_b16 v[4:5], v238 offset:13408\n"
    "v_pk_max_i16 v190, v190, 0\n"
    "v_mfma_f32_16x16x32_bf16 v[204:207], v[8:11], v[108:111], v[204:207]\n"
    "s_waitcnt lgkmcnt(5)\n"
    "ds_read_b128 v[240:243], v3 offset:36480\n"
    "v_pk_max_i16 v191, v191, 0\n"
    "v_mfma_f32_16x16x32_bf16 v[204:207], v[12:15], v[116:119], v[204:207]\n"
    "ds_read_b64_tr_b16 v[6:7], v238 offset:16992\n"
    "ds_read_b64_tr_b16 v[8:9], v236 offset:28768\n"
    "v_or_b32_e64 v188, v188, v252\n"
    "v_mfma_f32_16x16x32_bf16 v[208:211], v[12:15], v[120:123], v[208:211]\n"
    "s_waitcnt lgkmcnt(6)\n"
    "buffer_load_dwordx4 a[252:255], v251, s[32:35], s3 offen offset:3072 sc1\n"
    "v_or_b32 v189, v189, v253\n"
    "v_mfma_f32_16x16x32_bf16 v[208:211], v[16:19], v[128:131], v[208:211]\n"
    "ds_read_b64_tr_b16 v[10:11], v236 offset:32352\n"
    "ds_read_b64_tr_b16 v[12:13], v237 offset:4192\n"
    "v_or_b32_e64 v190, v190, v254\n"
    "v_mfma_f32_16x16x32_bf16 v[204:207], v[16:19], v[124:127], v[204:207]\n"
    "s_waitcnt lgkmcnt(4)\n"
    "ds_read_b64_tr_b16 v[14:15], v237 offset:7776\n"
    "v_or_b32 v191, v191, v255\n"
    "v_fmac_f32_e64 v196, v240, v248\n"
    "v_mfma_f32_16x16x32_bf16 v[204:207], v[244:247], v[132:135], v[204:207]\n"
    "buffer_store_dwordx4 v[188:191], v251, s[32:35], s41 offen sc1\n"
    "v_mfma_f32_16x16x32_bf16 v[208:211], v[244:247], v[136:139], v[208:211]\n"
    "s_waitcnt lgkmcnt(4)\n"
    "ds_read_b64_tr_b16 v[16:17], v237 offset:45152\n"
    "ds_read_b64_tr_b16 v[18:19], v237 offset:48736\n"
    "v_fmac_f32 v197, v241, v248\n"
    "v_mfma_f32_16x16x32_bf16 v[208:211], v[4:7], v[144:147], v[208:211]\n"
    "ds_read_b64_tr_b16 v[244:245], v238 offset:20576\n"
    "v_fmac_f32_e64 v198, v242, v248\n"
    "v_mfma_f32_16x16x32_bf16 v[204:207], v[4:7], v[140:143], v[204:207]\n"
    "s_waitcnt lgkmcnt(5)\n"
    "ds_read_b64_tr_b16 v[246:247], v238 offset:24160\n"
    "v_fmac_f32 v199, v243, v248\n"
    "v_mfma_f32_16x16x32_bf16 v[204:207], v[8:11], v[148:151], v[204:207]\n"
    "ds_read_b64_tr_b16 v[4:5], v237 offset:30848\n"
    "v_fmac_f32_e64 v200, v240, v249\n"
    "v_mfma_f32_16x16x32_bf16 v[208:211], v[8:11], v[152:155], v[208:211]\n"
    "s_waitcnt lgkmcnt(5)\n"
    "ds_read_b64_tr_b16 v[6:7], v237 offset:34432\n"
    "v_fmac_f32 v201, v241, v249\n"
    "v_mfma_f32_16x16x32_bf16 v[208:211], v[12:15], v[160:163], v[208:211]\n"
    "ds_read_b64_tr_b16 v[8:9], v237 offset:38016\n"
    "v_fmac_f32_e64 v202, v242, v249\n"
    "v_mfma_f32_16x16x32_bf16 v[204:207], v[12:15], v[156:159], v[204:207]\n"
    "s_waitcnt lgkmcnt(5)\n"
    "ds_read_b64_tr_b16 v[10:11], v237 offset:41600\n"
    "v_fmac_f32 v203, v243, v249\n"
    "v_mfma_f32_16x16x32_bf16 v[204:207], v[16:19], v[164:167], v[204:207]\n"
    "ds_read_b64_tr_b16 v[12:13], v237 offset:57472\n"
    "v_cvt_pk_bf16_f32 v196, v196, v197\n"
    "v_mfma_f32_16x16x32_bf16 v[208:211], v[16:19], v[168:171], v[208:211]\n"
    "s_waitcnt lgkmcnt(5)\n"
    "ds_read_b64_tr_b16 v[14:15], v237 offset:61056\n"
    "v_cvt_pk_bf16_f32 v197, v198, v199\n"
    "v_mfma_f32_16x16x32_bf16 v[208:211], v[244:247], v[176:179], v[208:211]\n"
    "ds_read_b64_tr_b16 v[16:17], v237 offset:64640\n"
    "v_cvt_pk_bf16_f32 v198, v200, v201\n"
    "v_mfma_f32_16x16x32_bf16 v[204:207], v[244:247], v[172:175], v[204:207]\n"
    "s_waitcnt lgkmcnt(5)\n"
    "ds_read_b64_tr_b16 v[18:19], v238 offset:2688\n"
    "v_cvt_pk_bf16_f32 v199, v202, v203\n"
    "v_mfma_f32_16x16x32_bf16 v[212:215], v[4:7], v[100:103], v[212:215]\n"
    "ds_read_b64_tr_b16 v[244:245], v238 offset:6272\n"
    "v_pk_max_i16 v196, v196, 0\n"
    "v_mfma_f32_16x16x32_bf16 v[216:219], v[4:7], v[104:107], v[216:219]\n"
    "s_waitcnt lgkmcnt(5)\n"
    "ds_read_b64_tr_b16 v[246:247], v238 offset:9856\n"
    "v_pk_max_i16 v197, v197, 0\n"
    "v_mfma_f32_16x16x32_bf16 v[216:219], v[8:11], v[112:115], v[216:219]\n"
    "ds_read_b64_tr_b16 v[4:5], v238 offset:13440\n"
    "v_pk_max_i16 v198, v198, 0\n"
    "v_mfma_f32_16x16x32_bf16 v[212:215], v[8:11], v[108:111], v[212:215]\n"
    "s_waitcnt lgkmcnt(5)\n"
    "ds_read_b128 v[240:243], v3 offset:36544\n"
    "v_pk_max_i16 v199, v199, 0\n"
    "v_mfma_f32_16x16x32_bf16 v[212:215], v[12:15], v[116:119], v[212:215]\n"
    "ds_read_b64_tr_b16 v[6:7], v238 offset:17024\n"
    "ds_read_b64_tr_b16 v[8:9], v236 offset:28800\n"
    "v_or_b32_e64 v196, v196, v252\n"
    "v_mfma_f32_16x16x32_bf16 v[216:219], v[12:15], v[120:123], v[216:219]\n"
    "s_waitcnt lgkmcnt(6)\n"
    "buffer_load_dwordx4 v[20:23], v251, s[36:39], s99 offen offset:0 sc1\n"
    "v_or_b32 v197, v197, v253\n"
    "v_mfma_f32_16x16x32_bf16 v[216:219], v[16:19], v[128:131], v[216:219]\n"
    "ds_read_b64_tr_b16 v[10:11], v236 offset:32384\n"
    "ds_read_b64_tr_b16 v[12:13], v237 offset:4224\n"
    "v_or_b32_e64 v198, v198, v254\n"
    "v_mfma_f32_16x16x32_bf16 v[212:215], v[16:19], v[124:127], v[212:215]\n"
    "s_waitcnt lgkmcnt(4)\n"
    "ds_read_b64_tr_b16 v[14:15], v237 offset:7808\n"
    "v_or_b32 v199, v199, v255\n"
    "v_fmac_f32_e64 v204, v240, v248\n"
    "v_mfma_f32_16x16x32_bf16 v[212:215], v[244:247], v[132:135], v[212:215]\n"
    "buffer_store_dwordx4 v[196:199], v251, s[32:35], s42 offen sc1\n"
    "v_mfma_f32_16x16x32_bf16 v[216:219], v[244:247], v[136:139], v[216:219]\n"
    "s_waitcnt lgkmcnt(4)\n"
    "ds_read_b64_tr_b16 v[16:17], v237 offset:45184\n"
    "ds_read_b64_tr_b16 v[18:19], v237 offset:48768\n"
    "v_fmac_f32 v205, v241, v248\n"
    "v_mfma_f32_16x16x32_bf16 v[216:219], v[4:7], v[144:147], v[216:219]\n"
    "buffer_load_dwordx4 v[24:27], v251, s[36:39], s99 offen offset:1024 sc1\n"
    "v_fmac_f32_e64 v206, v242, v248\n"
    "v_mfma_f32_16x16x32_bf16 v[212:215], v[4:7], v[140:143], v[212:215]\n"
    "s_waitcnt lgkmcnt(4)\n"
    "ds_read_b64_tr_b16 v[244:245], v238 offset:20608\n"
    "ds_read_b64_tr_b16 v[246:247], v238 offset:24192\n"
    "v_fmac_f32 v207, v243, v248\n"
    "v_mfma_f32_16x16x32_bf16 v[212:215], v[8:11], v[148:151], v[212:215]\n"
    "buffer_load_dwordx4 v[28:31], v251, s[44:47], s99 offen offset:0 sc1\n"
    "v_fmac_f32_e64 v208, v240, v249\n"
    "v_mfma_f32_16x16x32_bf16 v[216:219], v[8:11], v[152:155], v[216:219]\n"
    "s_waitcnt lgkmcnt(4)\n"
    "ds_read_b64_tr_b16 v[4:5], v237 offset:30880\n"
    "ds_read_b64_tr_b16 v[6:7], v237 offset:34464\n"
    "v_fmac_f32 v209, v241, v249\n"
    "v_mfma_f32_16x16x32_bf16 v[216:219], v[12:15], v[160:163], v[216:219]\n"
    "ds_read_b64_tr_b16 v[8:9], v237 offset:38048\n"
    "v_fmac_f32_e64 v210, v242, v249\n"
    "v_mfma_f32_16x16x32_bf16 v[212:215], v[12:15], v[156:159], v[212:215]\n"
    "s_waitcnt lgkmcnt(5)\n"
    "buffer_load_dwordx4 v[32:35], v251, s[44:47], s99 offen offset:1024 sc1\n"
    "v_fmac_f32 v211, v243, v249\n"
    "v_mfma_f32_16x16x32_bf16 v[212:215], v[16:19], v[164:167], v[212:215]\n"
    "ds_read_b64_tr_b16 v[10:11], v237 offset:41632\n"
    "ds_read_b64_tr_b16 v[12:13], v237 offset:57504\n"
    "v_cvt_pk_bf16_f32 v204, v204, v205\n"
    "v_mfma_f32_16x16x32_bf16 v[216:219], v[16:19], v[168:171], v[216:219]\n"
    "s_waitcnt lgkmcnt(5)\n"
    "ds_read_b64_tr_b16 v[14:15], v237 offset:61088\n"
    "v_cvt_pk_bf16_f32 v205, v206, v207\n"
    "v_mfma_f32_16x16x32_bf16 v[216:219], v[244:247], v[176:179], v[216:219]\n"
    "ds_read_b64_tr_b16 v[16:17], v237 offset:64672\n"
    "v_cvt_pk_bf16_f32 v206, v208, v209\n"
    "v_mfma_f32_16x16x32_bf16 v[212:215], v[244:247], v[172:175], v[212:215]\n"
    "s_waitcnt lgkmcnt(5)\n"
    "buffer_load_dwordx4 v[36:39], v251, s[36:39], s22 offen offset:2048 sc1\n"
    "v_cvt_pk_bf16_f32 v207, v210, v211\n"
    "v_mfma_f32_16x16x32_bf16 v[220:223], v[4:7], v[100:103], v[220:223]\n"
    "ds_read_b64_tr_b16 v[18:19], v238 offset:2720\n"
    "ds_read_b64_tr_b16 v[244:245], v238 offset:6304\n"
    "v_pk_max_i16 v204, v204, 0\n"
    "v_mfma_f32_16x16x32_bf16 v[224:227], v[4:7], v[104:107], v[224:227]\n"
    "s_waitcnt lgkmcnt(5)\n"
    "ds_read_b64_tr_b16 v[246:247], v238 offset:9888\n"
    "v_pk_max_i16 v205, v205, 0\n"
    "v_mfma_f32_16x16x32_bf16 v[224:227], v[8:11], v[112:115], v[224:227]\n"
    "buffer_load_dwordx4 v[40:43], v251, s[36:39], s22 offen offset:3072 sc1\n"
    "v_pk_max_i16 v206, v206, 0\n"
    "v_mfma_f32_16x16x32_bf16 v[220:223], v[8:11], v[108:111], v[220:223]\n"
    "s_waitcnt lgkmcnt(4)\n"
    "ds_read_b64_tr_b16 v[4:5], v238 offset:13472\n"
    "ds_read_b64_tr_b16 v[6:7], v238 offset:17056\n"
    "v_pk_max_i16 v207, v207, 0\n"
    "v_mfma_f32_16x16x32_bf16 v[220:223], v[12:15], v[116:119], v[220:223]\n"
    "ds_read_b64_tr_b16 v[8:9], v236 offset:28832\n"
    "v_or_b32_e64 v204, v204, v252\n"
    "v_mfma_f32_16x16x32_bf16 v[224:227], v[12:15], v[120:123], v[224:227]\n"
    "s_waitcnt lgkmcnt(5)\n"
    "ds_read_b64_tr_b16 v[10:11], v236 offset:32416\n"
    "v_or_b32 v205, v205, v253\n"
    "v_mfma_f32_16x16x32_bf16 v[224:227], v[16:19], v[128:131], v[224:227]\n"
    "buffer_load_dwordx4 v[44:47], v251, s[44:47], s22 offen offset:2048 sc1\n"
    "v_or_b32_e64 v206, v206, v254\n"
    "v_mfma_f32_16x16x32_bf16 v[220:223], v[16:19], v[124:127], v[220:223]\n"
    "s_waitcnt lgkmcnt(4)\n"
    "ds_read_b64_tr_b16 v[12:13], v237 offset:4256\n"
    "ds_read_b64_tr_b16 v[14:15], v237 offset:7840\n"
    "v_or_b32 v207, v207, v255\n"
    "v_mfma_f32_16x16x32_bf16 v[220:223], v[244:247], v[132:135], v[220:223]\n"
    "buffer_store_dwordx4 v[204:207], v251, s[32:35], s43 offen sc1\n"
    "v_mfma_f32_16x16x32_bf16 v[224:227], v[244:247], v[136:139], v[224:227]\n"
    "s_waitcnt lgkmcnt(4)\n"
    "ds_read_b64_tr_b16 v[16:17], v237 offset:45216\n"
    "ds_read_b64_tr_b16 v[18:19], v237 offset:48800\n"
    "v_mfma_f32_16x16x32_bf16 v[224:227], v[4:7], v[144:147], v[224:227]\n"
    "buffer_load_dwordx4 v[48:51], v251, s[44:47], s22 offen offset:3072 sc1\n"
    "v_lshl_add_u32 v250, v1, 3, s6\n"
    "v_mfma_f32_16x16x32_bf16 v[220:223], v[4:7], v[140:143], v[220:223]\n"
    "s_waitcnt lgkmcnt(4)\n"
    "ds_read_b64_tr_b16 v[244:245], v238 offset:20640\n"
    "ds_read_b64_tr_b16 v[246:247], v238 offset:24224\n"
    "v_mfma_f32_16x16x32_bf16 v[220:223], v[8:11], v[148:151], v[220:223]\n"
    "ds_write_b64 v250, v[248:249] offset:36864\n"
    "v_mfma_f32_16x16x32_bf16 v[224:227], v[8:11], v[152:155], v[224:227]\n"
    "s_waitcnt lgkmcnt(5)\n"
    "v_mfma_f32_16x16x32_bf16 v[224:227], v[12:15], v[160:163], v[224:227]\n"
    "v_mfma_f32_16x16x32_bf16 v[220:223], v[12:15], v[156:159], v[220:223]\n"
    "s_waitcnt lgkmcnt(3)\n"
    "v_mfma_f32_16x16x32_bf16 v[220:223], v[16:19], v[164:167], v[220:223]\n"
    "v_mfma_f32_16x16x32_bf16 v[224:227], v[16:19], v[168:171], v[224:227]\n"
    "s_waitcnt lgkmcnt(1)\n"
    "v_mfma_f32_16x16x32_bf16 v[224:227], v[244:247], v[176:179], v[224:227]\n"
    "v_mfma_f32_16x16x32_bf16 v[220:223], v[244:247], v[172:175], v[220:223]\n"
    "ds_read_b128 v[4:7], v3 offset:36608\n"
    "ds_read_b128 v[8:11], v3 offset:36672\n"
    "s_barrier\n"
    "buffer_load_dwordx4 v[52:55], v251, s[36:39], s23 offen offset:0 sc1\n"
    "buffer_load_dwordx4 v[56:59], v251, s[36:39], s23 offen offset:1024 sc1\n"
    "buffer_load_dwordx4 v[60:63], v251, s[36:39], s23 offen offset:2048 sc1\n"
    "buffer_load_dwordx4 v[64:67], v251, s[36:39], s23 offen offset:3072 sc1\n"
    "s_waitcnt lgkmcnt(1)\n"
    "s_nop 0\n"
    "v_pk_fma_f32 v[212:213], v[4:5], v[248:249], v[212:213] op_sel:[0,0,0] op_sel_hi:[1,0,1]\n"
    "v_pk_fma_f32 v[214:215], v[6:7], v[248:249], v[214:215] op_sel:[0,0,0] op_sel_hi:[1,0,1]\n"
    "v_pk_fma_f32 v[216:217], v[4:5], v[248:249], v[216:217] op_sel:[0,1,0] op_sel_hi:[1,1,1]\n"
    "v_pk_fma_f32 v[218:219], v[6:7], v[248:249], v[218:219] op_sel:[0,1,0] op_sel_hi:[1,1,1]\n"
    "v_cvt_pk_bf16_f32 v212, v212, v213\n"
    "v_cvt_pk_bf16_f32 v213, v214, v215\n"
    "v_cvt_pk_bf16_f32 v214, v216, v217\n"
    "v_cvt_pk_bf16_f32 v215, v218, v219\n"
    "v_pk_max_i16 v212, v212, 0\n"
    "v_pk_max_i16 v213, v213, 0\n"
    "v_pk_max_i16 v214, v214, 0\n"
    "v_pk_max_i16 v215, v215, 0\n"
    "v_or_b32 v212, v212, v252\n"
    "v_or_b32 v213, v213, v253\n"
    "v_or_b32 v214, v214, v254\n"
    "v_or_b32 v215, v215, v255\n"
    "buffer_store_dwordx4 v[212:215], v251, s[32:35], s58 offen sc1\n"
    "s_waitcnt lgkmcnt(0)\n"
    "s_nop 0\n"
    "v_pk_fma_f32 v[220:221], v[8:9], v[248:249], v[220:221] op_sel:[0,0,0] op_sel_hi:[1,0,1]\n"
    "v_pk_fma_f32 v[222:223], v[10:11], v[248:249], v[222:223] op_sel:[0,0,0] op_sel_hi:[1,0,1]\n"
    "v_pk_fma_f32 v[224:225], v[8:9], v[248:249], v[224:225] op_sel:[0,1,0] op_sel_hi:[1,1,1]\n"
    "v_pk_fma_f32 v[226:227], v[10:11], v[248:249], v[226:227] op_sel:[0,1,0] op_sel_hi:[1,1,1]\n"
    "v_cvt_pk_bf16_f32 v220, v220, v221\n"
    "v_cvt_pk_bf16_f32 v221, v222, v223\n"
    "v_cvt_pk_bf16_f32 v222, v224, v225\n"
    "v_cvt_pk_bf16_f32 v223, v226, v227\n"
    "v_pk_max_i16 v220, v220, 0\n"
    "v_pk_max_i16 v221, v221, 0\n"
    "v_pk_max_i16 v222, v222, 0\n"
    "v_pk_max_i16 v223, v223, 0\n"
    "v_or_b32 v220, v220, v252\n"
    "v_or_b32 v221, v221, v253\n"
    "v_or_b32 v222, v222, v254\n"
    "v_or_b32 v223, v223, v255\n"
    "buffer_store_dwordx4 v[220:223], v251, s[32:35], s59 offen sc1\n"
    "s_cmp_lt_u32 s48, 2\n"
    "s_cselect_b32 s6, -1, 0\n"
    "v_and_b32 v244, s6, v252\n"
    "v_and_b32 v245, s6, v253\n"
    "v_and_b32 v246, s6, v254\n"
    "v_and_b32 v247, s6, v255\n"
    "s_bfe_u32 s6, s53, 0x20001\n"
    "s_cmp_lg_u32 s6, s48\n"
    "s_cbranch_scc1 .Lk_noss_40\n"
    "v_mov_b32 v240, 0\n"
    "v_mov_b32 v242, 0\n"
    "s_and_b32 s4, s53, 1\n"
    "s_cbranch_scc1 .Lk_ss4_41\n"
    "v_dot2c_f32_bf16 v240, v100, v100\n"
    "v_dot2c_f32_bf16 v242, v104, v104\n"
    "v_dot2c_f32_bf16 v240, v101, v101\n"
    "v_dot2c_f32_bf16 v242, v105, v105\n"
    "v_dot2c_f32_bf16 v240, v102, v102\n"
    "v_dot2c_f32_bf16 v242, v106, v106\n"
    "v_dot2c_f32_bf16 v240, v103, v103\n"
    "v_dot2c_f32_bf16 v242, v107, v107\n"
    "v_dot2c_f32_bf16 v240, v108, v108\n"
    "v_dot2c_f32_bf16 v242, v112, v112\n"
    "v_dot2c_f32_bf16 v240, v109, v109\n"
    "v_dot2c_f32_bf16 v242, v113, v113\n"
    "v_dot2c_f32_bf16 v240, v110, v110\n"
    "v_dot2c_f32_bf16 v242, v114, v114\n"
    "v_dot2c_f32_bf16 v240, v111, v111\n"
    "v_dot2c_f32_bf16 v242, v115, v115\n"
    "v_dot2c_f32_bf16 v240, v116, v116\n"
    "v_dot2c_f32_bf16 v242, v120, v120\n"
    "v_dot2c_f32_bf16 v240, v117, v117\n"
    "v_dot2c_f32_bf16 v242, v121, v121\n"
    "v_dot2c_f32_bf16 v240, v118, v118\n"
    "v_dot2c_f32_bf16 v242, v122, v122\n"
    "v_dot2c_f32_bf16 v240, v119, v119\n"
    "v_dot2c_f32_bf16 v242, v123, v123\n"
    "v_dot2c_f32_bf16 v240, v124, v124\n"
    "v_dot2c_f32_bf16 v242, v128, v128\n"
    "v_dot2c_f32_bf16 v240, v125, v125\n"
    "v_dot2c_f32_bf16 v242, v129, v129\n"
    "v_dot2c_f32_bf16 v240, v126, v126\n"
    "v_dot2c_f32_bf16 v242, v130, v130\n"
    "v_dot2c_f32_bf16 v240, v127, v127\n"
    "v_dot2c_f32_bf16 v242, v131, v131\n"
    "v_dot2c_f32_bf16 v240, v132, v132\n"
    "v_dot2c_f32_bf16 v242, v136, v136\n"
    "v_dot2c_f32_bf16 v240, v133, v133\n"
    "v_dot2c_f32_bf16 v242, v137, v137\n"
    "v_dot2c_f32_bf16 v240, v134, v134\n"
    "v_dot2c_f32_bf16 v242, v138, v138\n"
    "v_dot2c_f32_bf16 v240, v135, v135\n"
    "v_dot2c_f32_bf16 v242, v139, v139\n"
    "s_branch .Lk_ss5_42\n"
    ".Lk_ss4_41:\n"
    "v_dot2c_f32_bf16 v240, v140, v140\n"
    "v_dot2c_f32_bf16 v242, v144, v144\n"
    "v_dot2c_f32_bf16 v240, v141, v141\n"
    "v_dot2c_f32_bf16 v242, v145, v145\n"
    "v_dot2c_f32_bf16 v240, v142, v142\n"
    "v_dot2c_f32_bf16 v242, v146, v146\n"
    "v_dot2c_f32_bf16 v240, v143, v143\n"
    "v_dot2c_f32_bf16 v242, v147, v147\n"
    "v_dot2c_f32_bf16 v240, v148, v148\n"
    "v_dot2c_f32_bf16 v242, v152, v152\n"
    "v_dot2c_f32_bf16 v240, v149, v149\n"
    "v_dot2c_f32_bf16 v242, v153, v153\n"
    "v_dot2c_f32_bf16 v240, v150, v150\n"
    "v_dot2c_f32_bf16 v242, v154, v154\n"
    "v_dot2c_f32_bf16 v240, v151, v151\n"
    "v_dot2c_f32_bf16 v242, v155, v155\n"
    "v_dot2c_f32_bf16 v240, v156, v156\n"
    "v_dot2c_f32_bf16 v242, v160, v160\n"
    "v_dot2c_f32_bf16 v240, v157, v157\n"
    "v_dot2c_f32_bf16 v242, v161, v161\n"
    "v_dot2c_f32_bf16 v240, v158, v158\n"
    "v_dot2c_f32_bf16 v242, v162, v162\n"
    "v_dot2c_f32_bf16 v240, v159, v159\n"
    "v_dot2c_f32_bf16 v242, v163, v163\n"
    "v_dot2c_f32_bf16 v240, v164, v164\n"
    "v_dot2c_f32_bf16 v242, v168, v168\n"
    "v_dot2c_f32_bf16 v240, v165, v165\n"
    "v_dot2c_f32_bf16 v242, v169, v169\n"
    "v_dot2c_f32_bf16 v240, v166, v166\n"
    "v_dot2c_f32_bf16 v242, v170, v170\n"
    "v_dot2c_f32_bf16 v240, v167, v167\n"
    "v_dot2c_f32_bf16 v242, v171, v171\n"
    "v_dot2c_f32_bf16 v240, v172, v172\n"
    "v_dot2c_f32_bf16 v242, v176, v176\n"
    "v_dot2c_f32_bf16 v240, v173, v173\n"
    "v_dot2c_f32_bf16 v242, v177, v177\n"
    "v_dot2c_f32_bf16 v240, v174, v174\n"
    "v_dot2c_f32_bf16 v242, v178, v178\n"
    "v_dot2c_f32_bf16 v240, v175, v175\n"
    "v_dot2c_f32_bf16 v242, v179, v179\n"
    ".Lk_ss5_42:\n"
    "s_lshl2_add_u32 s6, s52, s48\n"
    "s_lshr_b32 s5, s53, 3\n"
    "s_lshl1_add_u32 s6, s6, s5\n"
    "s_lshl1_add_u32 s6, s6, s4\n"
    "s_lshl_b32 s6, s6, 8\n"
    "s_add_u32 s6, s6, 12124160\n"
    "v_mov_b32 v241, v240\n"
    "v_mov_b32 v243, v242\n"
    "s_nop 0\n"
    "v_permlane32_swap_b32 v241, v240\n"
    "v_permlane32_swap_b32 v243, v242\n"
    "v_add_f32 v240, v240, v241\n"
    "v_add_f32 v242, v242, v243\n"
    "v_mov_b32 v241, v240\n"
    "v_mov_b32 v243, v242\n"
    "s_nop 0\n"
    "v_permlane16_swap_b32 v241, v240\n"
    "v_permlane16_swap_b32 v243, v242\n"
    "v_add_f32 v240, v240, v241\n"
    "v_add_f32 v242, v242, v243\n"
    "v_mov_b32 v241, s51\n"
    "v_mov_b32 v243, s51\n"
    "s_mov_b64 exec, 0xffff\n"
    "buffer_store_dwordx4 v[240:243], v251, s[32:35], s6 offen sc1\n"
    "s_mov_b64 exec, -1\n"
    ".Lk_noss_40:\n"
    "s_waitcnt vmcnt(15)\n"
    "v_xor_b32 v116, v20, v244\n"
    "v_xor_b32 v117, v21, v245\n"
    "v_xor_b32 v120, v22, v246\n"
    "v_xor_b32 v121, v23, v247\n"
    "s_waitcnt vmcnt(13)\n"
    "v_xor_b32 v118, v24, v244\n"
    "v_xor_b32 v119, v25, v245\n"
    "v_xor_b32 v122, v26, v246\n"
    "v_xor_b32 v123, v27, v247\n"
    "s_waitcnt vmcnt(12)\n"
    "v_xor_b32 v124, v28, v244\n"
    "v_xor_b32 v125, v29, v245\n"
    "v_xor_b32 v128, v30, v246\n"
    "v_xor_b32 v129, v31, v247\n"
    "s_waitcnt vmcnt(11)\n"
    "v_xor_b32 v126, v32, v244\n"
    "v_xor_b32 v127, v33, v245\n"
    "v_xor_b32 v130, v34, v246\n"
    "v_xor_b32_e64 v131, v35, v247\n"
    "v_or3_b32 v250, v116, v117, v118\n"
    "v_or3_b32 v250, v119, v120, v250\n"
    "v_or3_b32 v250, v121, v122, v250\n"
    "v_or3_b32 v250, v123, v124, v250\n"
    "v_or3_b32 v250, v125, v126, v250\n"
    "v_or3_b32 v250, v127, v128, v250\n"
    "v_or3_b32 v250, v129, v130, v250\n"
    "v_bitop3_b32 v250, v131, v250, s56 bitop3:0xa8\n"
    "v_cmp_ne_u32_e64 vcc, 0, v250\n"
    "s_nop 0\n"
    "s_cbranch_vccnz .Lk_ool_43\n"
    ".Lk_ret_44:\n"
    "v_mfma_f32_16x16x32_bf16 v[148:151], a[0:3], v[116:119], 0\n"
    "buffer_load_dwordx4 v[68:71], v251, s[44:47], s23 offen offset:0 sc1\n"
    "v_mfma_f32_16x16x32_bf16 v[168:171], a[0:3], v[120:123], 0\n"
    "s_waitcnt vmcnt(11)\n"
    "v_xor_b32 v132, v36, v252\n"
    "v_xor_b32_e64 v133, v37, v253\n"
    "v_mfma_f32_16x16x32_bf16 v[188:191], a[0:3], v[124:127], 0\n"
    "v_xor_b32 v136, v38, v254\n"
    "v_xor_b32 v137, v39, v255\n"
    "v_mfma_f32_16x16x32_bf16 v[208:211], a[0:3], v[128:131], 0\n"
    "s_waitcnt vmcnt(10)\n"
    "v_xor_b32 v134, v40, v252\n"
    "v_xor_b32_e64 v135, v41, v253\n"
    "v_mfma_f32_16x16x32_bf16 v[212:215], a[4:7], v[128:131], 0\n"
    "buffer_load_dwordx4 v[72:75], v251, s[44:47], s23 offen offset:1024 sc1\n"
    "v_mfma_f32_16x16x32_bf16 v[192:195], a[4:7], v[124:127], 0\n"
    "v_xor_b32 v138, v42, v254\n"
    "v_xor_b32 v139, v43, v255\n"
    "v_mfma_f32_16x16x32_bf16 v[172:175], a[4:7], v[120:123], 0\n"
    "s_waitcnt vmcnt(10)\n"
    "v_xor_b32 v140, v44, v252\n"
    "v_xor_b32_e64 v141, v45, v253\n"
    "v_mfma_f32_16x16x32_bf16 v[152:155], a[4:7], v[116:119], 0\n"
    "v_xor_b32 v144, v46, v254\n"
    "v_xor_b32 v145, v47, v255\n"
    "v_mfma_f32_16x16x32_bf16 v[156:159], a[8:11], v[116:119], 0\n"
    "buffer_load_dwordx4 v[76:79], v251, s[44:47], s23 offen offset:2048 sc1\n"
    "v_mfma_f32_16x16x32_bf16 v[176:179], a[8:11], v[120:123], 0\n"
    "s_waitcnt vmcnt(9)\n"
    "v_xor_b32 v142, v48, v252\n"
    "v_xor_b32_e64 v143, v49, v253\n"
    "v_mfma_f32_16x16x32_bf16 v[196:199], a[8:11], v[124:127], 0\n"
    "v_xor_b32 v146, v50, v254\n"
    "v_xor_b32 v147, v51, v255\n"
    "v_mfma_f32_16x16x32_bf16 v[216:219], a[8:11], v[128:131], 0\n"
    "v_or3_b32 v250, v132, v133, v134\n"
    "v_or3_b32 v250, v135, v136, v250\n"
    "v_mfma_f32_16x16x32_bf16 v[220:223], a[12:15], v[128:131], 0\n"
    "buffer_load_dwordx4 v[80:83], v251, s[44:47], s23 offen offset:3072 sc1\n"
    "v_mfma_f32_16x16x32_bf16 v[200:203], a[12:15], v[124:127], 0\n"
    "v_or3_b32 v250, v137, v138, v250\n"
    "v_or3_b32 v250, v139, v140, v250\n"
    "v_mfma_f32_16x16x32_bf16 v[180:183], a[12:15], v[120:123], 0\n"
    "v_or3_b32 v250, v141, v142, v250\n"
    "v_or3_b32 v250, v143, v144, v250\n"
    "v_mfma_f32_16x16x32_bf16 v[160:163], a[12:15], v[116:119], 0\n"
    "v_or3_b32 v250, v145, v146, v250\n"
    "v_bitop3_b32 v250, v147, v250, s56 bitop3:0xa8\n"
    "v_mfma_f32_16x16x32_bf16 v[164:167], a[16:19], v[116:119], 0\n"
    "v_cmp_ne_u32_e64 vcc, 0, v250\n"
    "v_add_u32 v236, 4096, v251\n"
    "v_mfma_f32_16x16x32_bf16 v[184:187], a[16:19], v[120:123], 0\n"
    "buffer_load_dwordx4 v[84:87], v251, s[36:39], s24 offen offset:0 sc1\n"
    "v_mfma_f32_16x16x32_bf16 v[204:207], a[16:19], v[124:127], 0\n"
    "s_cbranch_vccnz .Lk_ool_45\n"
    ".Lk_ret_46:\n"
    "s_xor_b32 s58, s61, 1\n"
    "v_mfma_f32_16x16x32_bf16 v[224:227], a[16:19], v[128:131], 0\n"
    "s_waitcnt vmcnt(10)\n"
    "v_xor_b32 v116, v52, v252\n"
    "v_xor_b32_e64 v117, v53, v253\n"
    "v_mfma_f32_16x16x32_bf16 v[148:151], a[20:23], v[132:135], v[148:151]\n"
    "buffer_load_dwordx4 v[88:91], v251, s[36:39], s24 offen offset:1024 sc1\n"
    "v_mfma_f32_16x16x32_bf16 v[168:171], a[20:23], v[136:139], v[168:171]\n"
    "s_barrier\n"
    "v_xor_b32 v120, v54, v254\n"
    "v_mfma_f32_16x16x32_bf16 v[188:191], a[20:23], v[140:143], v[188:191]\n"
    "s_waitcnt vmcnt(10)\n"
    "v_xor_b32 v121, v55, v255\n"
    "v_xor_b32_e64 v118, v56, v252\n"
    "v_mfma_f32_16x16x32_bf16 v[208:211], a[20:23], v[144:147], v[208:211]\n"
    "v_xor_b32 v119, v57, v253\n"
    "v_xor_b32 v122, v58, v254\n"
    "v_mfma_f32_16x16x32_bf16 v[212:215], a[24:27], v[144:147], v[212:215]\n"
    "buffer_load_dwordx4 v[92:95], v251, s[36:39], s24 offen offset:2048 sc1\n"
    "v_mfma_f32_16x16x32_bf16 v[192:195], a[24:27], v[140:143], v[192:195]\n"
    "s_waitcnt vmcnt(10)\n"
    "v_xor_b32 v123, v59, v255\n"
    "v_xor_b32_e64 v124, v60, v252\n"
    "v_mfma_f32_16x16x32_bf16 v[172:175], a[24:27], v[136:139], v[172:175]\n"
    "v_xor_b32 v125, v61, v253\n"
    "v_xor_b32 v128, v62, v254\n"
    "v_mfma_f32_16x16x32_bf16 v[152:155], a[24:27], v[132:135], v[152:155]\n"
    "s_waitcnt vmcnt(9)\n"
    "v_xor_b32 v129, v63, v255\n"
    "v_xor_b32_e64 v126, v64, v252\n"
    "v_mfma_f32_16x16x32_bf16 v[156:159], a[28:31], v[132:135], v[156:159]\n"
    "buffer_load_dwordx4 v[96:99], v251, s[36:39], s24 offen offset:3072 sc1\n"
    "v_mfma_f32_16x16x32_bf16 v[176:179], a[28:31], v[136:139], v[176:179]\n"
    "v_xor_b32 v127, v65, v253\n"
    "v_xor_b32 v130, v66, v254\n"
    "v_mfma_f32_16x16x32_bf16 v[196:199], a[28:31], v[140:143], v[196:199]\n"
    "v_xor_b32_e64 v131, v67, v255\n"
    "v_or3_b32 v250, v116, v117, v118\n"
    "v_mfma_f32_16x16x32_bf16 v[216:219], a[28:31], v[144:147], v[216:219]\n"
    "v_or3_b32 v250, v119, v120, v250\n"
    "v_or3_b32 v250, v121, v122, v250\n"
    "v_mfma_f32_16x16x32_bf16 v[220:223], a[32:35], v[144:147], v[220:223]\n"
    "buffer_load_dwordx4 v[100:103], v251, s[44:47], s24 offen offset:0 sc1\n"
    "v_mfma_f32_16x16x32_bf16 v[200:203], a[32:35], v[140:143], v[200:203]\n"
    "v_or3_b32 v250, v123, v124, v250\n"
    "v_or3_b32 v250, v125, v126, v250\n"
    "v_mfma_f32_16x16x32_bf16 v[180:183], a[32:35], v[136:139], v[180:183]\n"
    "v_or3_b32 v250, v127, v128, v250\n"
    "v_or3_b32 v250, v129, v130, v250\n"
    "v_mfma_f32_16x16x32_bf16 v[160:163], a[32:35], v[132:135], v[160:163]\n"
    "v_bitop3_b32 v250, v131, v250, s56 bitop3:0xa8\n"
    "v_cmp_ne_u32_e64 vcc, 0, v250\n"
    "v_mfma_f32_16x16x32_bf16 v[164:167], a[36:39], v[132:135], v[164:167]\n"
    "buffer_load_dwordx4 v[104:107], v251, s[44:47], s24 offen offset:1024 sc1\n"
    "v_mfma_f32_16x16x32_bf16 v[184:187], a[36:39], v[136:139], v[184:187]\n"
    "s_cbranch_vccnz .Lk_ool_47\n"
    ".Lk_ret_48:\n"
    "s_mul_i32 s58, s58, 10240\n"
    "v_mfma_f32_16x16x32_bf16 v[204:207], a[36:39], v[140:143], v[204:207]\n"
    "s_add_u32 s58, s58, s55\n"
    "v_add_u32_e64 v14, s58, v251\n"
    "v_mfma_f32_16x16x32_bf16 v[224:227], a[36:39], v[144:147], v[224:227]\n"
    "s_waitcnt vmcnt(9)\n"
    "v_xor_b32 v132, v68, v252\n"
    "v_xor_b32_e64 v133, v69, v253\n"
    "v_mfma_f32_16x16x32_bf16 v[148:151], a[40:43], v[116:119], v[148:151]\n"
    "buffer_load_dwordx4 v[108:111], v251, s[44:47], s24 offen offset:2048 sc1\n"
    "v_mfma_f32_16x16x32_bf16 v[148:151], a[60:63], v[124:127], v[148:151]\n"
    "v_xor_b32 v136, v70, v254\n"
    "v_xor_b32 v137, v71, v255\n"
    "v_mfma_f32_16x16x32_bf16 v[168:171], a[60:63], v[128:131], v[168:171]\n"
    "s_waitcnt vmcnt(9)\n"
    "v_xor_b32 v134, v72, v252\n"
    "v_xor_b32_e64 v135, v73, v253\n"
    "v_mfma_f32_16x16x32_bf16 v[168:171], a[40:43], v[120:123], v[168:171]\n"
    "v_xor_b32 v138, v74, v254\n"
    "v_xor_b32 v139, v75, v255\n"
    "v_mfma_f32_16x16x32_bf16 v[152:155], a[44:47], v[116:119], v[152:155]\n"
    "buffer_load_dwordx4 v[112:115], v251, s[44:47], s24 offen offset:3072 sc1\n"
    "v_mfma_f32_16x16x32_bf16 v[152:155], a[64:67], v[124:127], v[152:155]\n"
    "s_waitcnt vmcnt(9)\n"
    "v_xor_b32 v140, v76, v252\n"
    "v_xor_b32_e64 v141, v77, v253\n"
    "v_mfma_f32_16x16x32_bf16 v[172:175], a[64:67], v[128:131], v[172:175]\n"
    "v_xor_b32 v144, v78, v254\n"
    "v_xor_b32 v145, v79, v255\n"
    "v_mfma_f32_16x16x32_bf16 v[172:175], a[44:47], v[120:123], v[172:175]\n"
    "s_waitcnt vmcnt(8)\n"
    "v_xor_b32 v142, v80, v252\n"
    "v_xor_b32_e64 v143, v81, v253\n"
    "v_mfma_f32_16x16x32_bf16 v[156:159], a[48:51], v[116:119], v[156:159]\n"
    "buffer_load_dwordx4 v[20:23], v251, s[36:39], s25 offen offset:0 sc1\n"
    "v_mfma_f32_16x16x32_bf16 v[156:159], a[68:71], v[124:127], v[156:159]\n"
    "v_xor_b32 v146, v82, v254\n"
    "v_xor_b32 v147, v83, v255\n"
    "v_mfma_f32_16x16x32_bf16 v[176:179], a[68:71], v[128:131], v[176:179]\n"
    "v_or3_b32 v250, v132, v133, v134\n"
    "v_or3_b32 v250, v135, v136, v250\n"
    "v_mfma_f32_16x16x32_bf16 v[176:179], a[48:51], v[120:123], v[176:179]\n"
    "v_or3_b32 v250, v137, v138, v250\n"
    "v_or3_b32 v250, v139, v140, v250\n"
    "v_mfma_f32_16x16x32_bf16 v[160:163], a[52:55], v[116:119], v[160:163]\n"
    "buffer_load_dwordx4 v[24:27], v251, s[36:39], s25 offen offset:1024 sc1\n"
    "v_mfma_f32_16x16x32_bf16 v[160:163], a[72:75], v[124:127], v[160:163]\n"
    "v_or3_b32 v250, v141, v142, v250\n"
    "v_or3_b32 v250, v143, v144, v250\n"
    "v_mfma_f32_16x16x32_bf16 v[180:183], a[72:75], v[128:131], v[180:183]\n"
    "v_or3_b32 v250, v145, v146, v250\n"
    "v_bitop3_b32 v250, v147, v250, s56 bitop3:0xa8\n"
    "v_mfma_f32_16x16x32_bf16 v[180:183], a[52:55], v[120:123], v[180:183]\n"
    "v_cmp_ne_u32 vcc, 0, v250\n"
    "s_xor_b32 s58, s48, 1\n"
    "v_mfma_f32_16x16x32_bf16 v[164:167], a[56:59], v[116:119], v[164:167]\n"
    "buffer_load_dwordx4 v[28:31], v251, s[36:39], s25 offen offset:2048 sc1\n"
    "v_mfma_f32_16x16x32_bf16 v[164:167], a[76:79], v[124:127], v[164:167]\n"
    "s_cbranch_vccnz .Lk_ool_49\n"
    ".Lk_ret_50:\n"
    "s_mul_i32 s58, s58, 5120\n"
    "v_mfma_f32_16x16x32_bf16 v[184:187], a[76:79], v[128:131], v[184:187]\n"
    "s_add_u32 s58, s58, s55\n"
    "v_add_u32_e64 v15, s58, v251\n"
    "v_mfma_f32_16x16x32_bf16 v[184:187], a[56:59], v[120:123], v[184:187]\n"
    "s_waitcnt vmcnt(10)\n"
    "v_xor_b32 v116, v84, v252\n"
    "v_xor_b32_e64 v117, v85, v253\n"
    "v_mfma_f32_16x16x32_bf16 v[188:191], a[40:43], v[132:135], v[188:191]\n"
    "buffer_load_dwordx4 v[32:35], v251, s[36:39], s25 offen offset:3072 sc1\n"
    "v_mfma_f32_16x16x32_bf16 v[188:191], a[60:63], v[140:143], v[188:191]\n"
    "s_add_u32 s3, s62, 65536\n"
    "v_xor_b32_e64 v120, v86, v254\n"
    "v_mfma_f32_16x16x32_bf16 v[208:211], a[60:63], v[144:147], v[208:211]\n"
    "s_waitcnt vmcnt(10)\n"
    "v_xor_b32 v121, v87, v255\n"
    "v_xor_b32_e64 v118, v88, v252\n"
    "v_mfma_f32_16x16x32_bf16 v[208:211], a[40:43], v[136:139], v[208:211]\n"
    "v_xor_b32 v119, v89, v253\n"
    "v_xor_b32 v122, v90, v254\n"
    "v_mfma_f32_16x16x32_bf16 v[192:195], a[44:47], v[132:135], v[192:195]\n"
    "buffer_load_dwordx4 a[0:3], v251, s[32:35], s3 offen offset:0 sc1\n"
    "v_mfma_f32_16x16x32_bf16 v[192:195], a[64:67], v[140:143], v[192:195]\n"
    "s_waitcnt vmcnt(10)\n"
    "v_xor_b32 v123, v91, v255\n"
    "v_xor_b32_e64 v124, v92, v252\n"
    "v_mfma_f32_16x16x32_bf16 v[212:215], a[64:67], v[144:147], v[212:215]\n"
    "v_xor_b32 v125, v93, v253\n"
    "v_xor_b32 v128, v94, v254\n"
    "v_mfma_f32_16x16x32_bf16 v[212:215], a[44:47], v[136:139], v[212:215]\n"
    "s_waitcnt vmcnt(9)\n"
    "v_xor_b32 v129, v95, v255\n"
    "v_xor_b32_e64 v126, v96, v252\n"
    "v_mfma_f32_16x16x32_bf16 v[196:199], a[48:51], v[132:135], v[196:199]\n"
    "buffer_load_dwordx4 v[36:39], v251, s[44:47], s25 offen offset:0 sc1\n"
    "v_mfma_f32_16x16x32_bf16 v[196:199], a[68:71], v[140:143], v[196:199]\n"
    "v_xor_b32 v127, v97, v253\n"
    "v_xor_b32 v130, v98, v254\n"
    "v_mfma_f32_16x16x32_bf16 v[216:219], a[68:71], v[144:147], v[216:219]\n"
    "v_xor_b32_e64 v131, v99, v255\n"
    "v_or3_b32 v250, v116, v117, v118\n"
    "v_mfma_f32_16x16x32_bf16 v[216:219], a[48:51], v[136:139], v[216:219]\n"
    "v_or3_b32 v250, v119, v120, v250\n"
    "v_or3_b32 v250, v121, v122, v250\n"
    "v_mfma_f32_16x16x32_bf16 v[200:203], a[52:55], v[132:135], v[200:203]\n"
    "buffer_load_dwordx4 v[40:43], v251, s[44:47], s25 offen offset:1024 sc1\n"
    "v_mfma_f32_16x16x32_bf16 v[200:203], a[72:75], v[140:143], v[200:203]\n"
    "v_or3_b32 v250, v123, v124, v250\n"
    "v_or3_b32 v250, v125, v126, v250\n"
    "v_mfma_f32_16x16x32_bf16 v[220:223], a[72:75], v[144:147], v[220:223]\n"
    "v_or3_b32 v250, v127, v128, v250\n"
    "v_or3_b32 v250, v129, v130, v250\n"
    "v_mfma_f32_16x16x32_bf16 v[220:223], a[52:55], v[136:139], v[220:223]\n"
    "v_bitop3_b32 v250, v131, v250, s56 bitop3:0xa8\n"
    "v_cmp_ne_u32_e64 vcc, 0, v250\n"
    "v_mfma_f32_16x16x32_bf16 v[204:207], a[56:59], v[132:135], v[204:207]\n"
    "buffer_load_dwordx4 v[44:47], v251, s[44:47], s25 offen offset:2048 sc1\n"
    "v_mfma_f32_16x16x32_bf16 v[204:207], a[76:79], v[140:143], v[204:207]\n"
    "s_cbranch_vccnz .Lk_ool_51\n"
    ".Lk_ret_52:\n"
    "s_lshl_b32 s58, s96, 7\n"
    "v_mfma_f32_16x16x32_bf16 v[224:227], a[76:79], v[144:147], v[224:227]\n"
    "s_lshl2_add_u32 s58, s97, s58\n"
    "v_lshl_add_u32 v231, v1, 3, s58\n"
    "v_mfma_f32_16x16x32_bf16 v[224:227], a[56:59], v[136:139], v[224:227]\n"
    "s_waitcnt vmcnt(11)\n"
    "ds_read_b32 v231, v231 offset:36864\n"
    "v_xor_b32_e64 v132, v100, v252\n"
    "v_mfma_f32_16x16x32_bf16 v[148:151], a[80:83], v[116:119], v[148:151]\n"
    "buffer_load_dwordx4 v[48:51], v251, s[44:47], s25 offen offset:3072 sc1\n"
    "v_mfma_f32_16x16x32_bf16 v[148:151], a[100:103], v[124:127], v[148:151]\n"
    "v_xor_b32 v133, v101, v253\n"
    "v_xor_b32 v136, v102, v254\n"
    "v_mfma_f32_16x16x32_bf16 v[168:171], a[100:103], v[128:131], v[168:171]\n"
    "s_waitcnt vmcnt(11)\n"
    "v_xor_b32 v137, v103, v255\n"
    "v_xor_b32_e64 v134, v104, v252\n"
    "v_mfma_f32_16x16x32_bf16 v[168:171], a[80:83], v[120:123], v[168:171]\n"
    "v_xor_b32 v135, v105, v253\n"
    "v_xor_b32 v138, v106, v254\n"
    "v_mfma_f32_16x16x32_bf16 v[152:155], a[84:87], v[116:119], v[152:155]\n"
    "buffer_load_dwordx4 a[4:7], v251, s[32:35], s3 offen offset:1024 sc1\n"
    "v_mfma_f32_16x16x32_bf16 v[152:155], a[104:107], v[124:127], v[152:155]\n"
    "s_waitcnt vmcnt(11)\n"
    "v_xor_b32 v139, v107, v255\n"
    "v_xor_b32_e64 v140, v108, v252\n"
    "v_mfma_f32_16x16x32_bf16 v[172:175], a[104:107], v[128:131], v[172:175]\n"
    "v_xor_b32 v141, v109, v253\n"
    "v_xor_b32 v144, v110, v254\n"
    "v_mfma_f32_16x16x32_bf16 v[172:175], a[84:87], v[120:123], v[172:175]\n"
    "s_waitcnt vmcnt(10)\n"
    "v_xor_b32 v145, v111, v255\n"
    "v_xor_b32_e64 v142, v112, v252\n"
    "v_mfma_f32_16x16x32_bf16 v[156:159], a[88:91], v[116:119], v[156:159]\n"
    "buffer_load_dwordx4 v[52:55], v251, s[36:39], s30 offen offset:0 sc1\n"
    "v_mfma_f32_16x16x32_bf16 v[156:159], a[108:111], v[124:127], v[156:159]\n"
    "v_xor_b32 v143, v113, v253\n"
    "v_xor_b32 v146, v114, v254\n"
    "v_mfma_f32_16x16x32_bf16 v[176:179], a[108:111], v[128:131], v[176:179]\n"
    "v_xor_b32_e64 v147, v115, v255\n"
    "v_or3_b32 v250, v132, v133, v134\n"
    "v_mfma_f32_16x16x32_bf16 v[176:179], a[88:91], v[120:123], v[176:179]\n"
    "v_or3_b32 v250, v135, v136, v250\n"
    "v_or3_b32 v250, v137, v138, v250\n"
    "v_mfma_f32_16x16x32_bf16 v[160:163], a[92:95], v[116:119], v[160:163]\n"
    "buffer_load_dwordx4 v[56:59], v251, s[36:39], s30 offen offset:1024 sc1\n"
    "v_mfma_f32_16x16x32_bf16 v[160:163], a[112:115], v[124:127], v[160:163]\n"
    "v_or3_b32 v250, v139, v140, v250\n"
    "v_or3_b32 v250, v141, v142, v250\n"
    "v_mfma_f32_16x16x32_bf16 v[180:183], a[112:115], v[128:131], v[180:183]\n"
    "v_or3_b32 v250, v143, v144, v250\n"
    "v_or3_b32 v250, v145, v146, v250\n"
    "v_mfma_f32_16x16x32_bf16 v[180:183], a[92:95], v[120:123], v[180:183]\n"
    "v_bitop3_b32 v250, v147, v250, s56 bitop3:0xa8\n"
    "v_cmp_ne_u32_e64 vcc, 0, v250\n"
    "v_mfma_f32_16x16x32_bf16 v[164:167], a[96:99], v[116:119], v[164:167]\n"
    "buffer_load_dwordx4 v[60:63], v251, s[36:39], s30 offen offset:2048 sc1\n"
    "v_mfma_f32_16x16x32_bf16 v[164:167], a[116:119], v[124:127], v[164:167]\n"
    "s_cbranch_vccnz .Lk_ool_53\n"
    ".Lk_ret_54:\n"
    "s_mul_i32 s59, s48, 5120\n"
    "v_mfma_f32_16x16x32_bf16 v[184:187], a[116:119], v[128:131], v[184:187]\n"
    "s_xor_b32 s58, s48, 1\n"
    "s_mul_i32 s58, s58, 40960\n"
    "v_mfma_f32_16x16x32_bf16 v[184:187], a[96:99], v[120:123], v[184:187]\n"
    "s_waitcnt vmcnt(12)\n"
    "v_xor_b32 v116, v20, v252\n"
    "v_xor_b32_e64 v117, v21, v253\n"
    "v_mfma_f32_16x16x32_bf16 v[188:191], a[80:83], v[132:135], v[188:191]\n"
    "buffer_load_dwordx4 v[64:67], v251, s[36:39], s30 offen offset:3072 sc1\n"
    "v_mfma_f32_16x16x32_bf16 v[188:191], a[100:103], v[140:143], v[188:191]\n"
    "v_xor_b32 v120, v22, v254\n"
    "v_xor_b32 v121, v23, v255\n"
    "v_mfma_f32_16x16x32_bf16 v[208:211], a[100:103], v[144:147], v[208:211]\n"
    "s_waitcnt vmcnt(12)\n"
    "v_xor_b32 v118, v24, v252\n"
    "v_xor_b32_e64 v119, v25, v253\n"
    "v_mfma_f32_16x16x32_bf16 v[208:211], a[80:83], v[136:139], v[208:211]\n"
    "v_xor_b32 v122, v26, v254\n"
    "v_xor_b32 v123, v27, v255\n"
    "v_mfma_f32_16x16x32_bf16 v[192:195], a[84:87], v[132:135], v[192:195]\n"
    "buffer_load_dwordx4 a[8:11], v251, s[32:35], s3 offen offset:2048 sc1\n"
    "v_mfma_f32_16x16x32_bf16 v[192:195], a[104:107], v[140:143], v[192:195]\n"
    "s_waitcnt vmcnt(12)\n"
    "v_xor_b32 v124, v28, v252\n"
    "v_xor_b32_e64 v125, v29, v253\n"
    "v_mfma_f32_16x16x32_bf16 v[212:215], a[104:107], v[144:147], v[212:215]\n"
    "v_xor_b32 v128, v30, v254\n"
    "v_xor_b32 v129, v31, v255\n"
    "v_mfma_f32_16x16x32_bf16 v[212:215], a[84:87], v[136:139], v[212:215]\n"
    "s_waitcnt vmcnt(11)\n"
    "v_xor_b32 v126, v32, v252\n"
    "v_xor_b32_e64 v127, v33, v253\n"
    "v_mfma_f32_16x16x32_bf16 v[196:199], a[88:91], v[132:135], v[196:199]\n"
    "buffer_load_dwordx4 v[68:71], v251, s[44:47], s30 offen offset:0 sc1\n"
    "v_mfma_f32_16x16x32_bf16 v[196:199], a[108:111], v[140:143], v[196:199]\n"
    "v_xor_b32 v130, v34, v254\n"
    "v_xor_b32 v131, v35, v255\n"
    "v_mfma_f32_16x16x32_bf16 v[216:219], a[108:111], v[144:147], v[216:219]\n"
    "v_or3_b32 v250, v116, v117, v118\n"
    "v_or3_b32 v250, v119, v120, v250\n"
    "v_mfma_f32_16x16x32_bf16 v[216:219], a[88:91], v[136:139], v[216:219]\n"
    "v_or3_b32 v250, v121, v122, v250\n"
    "v_or3_b32 v250, v123, v124, v250\n"
    "v_mfma_f32_16x16x32_bf16 v[200:203], a[92:95], v[132:135], v[200:203]\n"
    "buffer_load_dwordx4 v[72:75], v251, s[44:47], s30 offen offset:1024 sc1\n"
    "v_mfma_f32_16x16x32_bf16 v[200:203], a[112:115], v[140:143], v[200:203]\n"
    "v_or3_b32 v250, v125, v126, v250\n"
    "v_or3_b32 v250, v127, v128, v250\n"
    "v_mfma_f32_16x16x32_bf16 v[220:223], a[112:115], v[144:147], v[220:223]\n"
    "v_or3_b32 v250, v129, v130, v250\n"
    "v_bitop3_b32 v250, v131, v250, s56 bitop3:0xa8\n"
    "v_mfma_f32_16x16x32_bf16 v[220:223], a[92:95], v[136:139], v[220:223]\n"
    "v_cmp_ne_u32 vcc, 0, v250\n"
    "s_add_u32 s58, s58, s59\n"
    "v_mfma_f32_16x16x32_bf16 v[204:207], a[96:99], v[132:135], v[204:207]\n"
    "buffer_load_dwordx4 v[76:79], v251, s[44:47], s30 offen offset:2048 sc1\n"
    "v_mfma_f32_16x16x32_bf16 v[204:207], a[116:119], v[140:143], v[204:207]\n"
    "s_cbranch_vccnz .Lk_ool_55\n"
    ".Lk_ret_56:\n"
    "v_add_u32 v228, s58, v251\n"
    "v_mfma_f32_16x16x32_bf16 v[224:227], a[116:119], v[144:147], v[224:227]\n"
    "s_xor_b32 s58, s48, 2\n"
    "s_mul_i32 s58, s58, 40960\n"
    "v_mfma_f32_16x16x32_bf16 v[224:227], a[96:99], v[136:139], v[224:227]\n"
    "s_waitcnt vmcnt(12)\n"
    "v_xor_b32 v132, v36, v252\n"
    "v_xor_b32 v133, v37, v253\n"
    "v_mfma_f32_16x16x32_bf16 v[148:151], a[120:123], v[116:119], v[148:151]\n"
    "buffer_load_dwordx4 v[80:83], v251, s[44:47], s30 offen offset:3072 sc1\n"
    "v_mfma_f32_16x16x32_bf16 v[148:151], a[140:143], v[124:127], v[148:151]\n"
    "v_xor_b32 v136, v38, v254\n"
    "v_xor_b32 v137, v39, v255\n"
    "v_mfma_f32_16x16x32_bf16 v[168:171], a[140:143], v[128:131], v[168:171]\n"
    "s_waitcnt vmcnt(12)\n"
    "v_xor_b32 v134, v40, v252\n"
    "v_xor_b32_e64 v135, v41, v253\n"
    "v_mfma_f32_16x16x32_bf16 v[168:171], a[120:123], v[120:123], v[168:171]\n"
    "v_xor_b32 v138, v42, v254\n"
    "v_xor_b32 v139, v43, v255\n"
    "v_mfma_f32_16x16x32_bf16 v[152:155], a[124:127], v[116:119], v[152:155]\n"
    "buffer_load_dwordx4 a[12:15], v251, s[32:35], s3 offen offset:3072 sc1\n"
    "v_mfma_f32_16x16x32_bf16 v[152:155], a[144:147], v[124:127], v[152:155]\n"
    "s_waitcnt vmcnt(12)\n"
    "v_xor_b32 v140, v44, v252\n"
    "v_xor_b32_e64 v141, v45, v253\n"
    "v_mfma_f32_16x16x32_bf16 v[172:175], a[144:147], v[128:131], v[172:175]\n"
    "v_xor_b32 v144, v46, v254\n"
    "v_xor_b32 v145, v47, v255\n"
    "v_mfma_f32_16x16x32_bf16 v[172:175], a[124:127], v[120:123], v[172:175]\n"
    "s_waitcnt vmcnt(11)\n"
    "v_xor_b32 v142, v48, v252\n"
    "v_xor_b32_e64 v143, v49, v253\n"
    "v_mfma_f32_16x16x32_bf16 v[156:159], a[128:131], v[116:119], v[156:159]\n"
    "buffer_load_dwordx4 v[84:87], v251, s[36:39], s31 offen offset:0 sc1\n"
    "v_mfma_f32_16x16x32_bf16 v[156:159], a[148:151], v[124:127], v[156:159]\n"
    "v_xor_b32 v146, v50, v254\n"
    "v_xor_b32 v147, v51, v255\n"
    "v_mfma_f32_16x16x32_bf16 v[176:179], a[148:151], v[128:131], v[176:179]\n"
    "v_or3_b32 v250, v132, v133, v134\n"
    "v_or3_b32 v250, v135, v136, v250\n"
    "v_mfma_f32_16x16x32_bf16 v[176:179], a[128:131], v[120:123], v[176:179]\n"
    "v_or3_b32 v250, v137, v138, v250\n"
    "v_or3_b32 v250, v139, v140, v250\n"
    "v_mfma_f32_16x16x32_bf16 v[160:163], a[132:135], v[116:119], v[160:163]\n"
    "buffer_load_dwordx4 v[88:91], v251, s[36:39], s31 offen offset:1024 sc1\n"
    "v_mfma_f32_16x16x32_bf16 v[160:163], a[152:155], v[124:127], v[160:163]\n"
    "v_or3_b32 v250, v141, v142, v250\n"
    "v_or3_b32 v250, v143, v144, v250\n"
    "v_mfma_f32_16x16x32_bf16 v[180:183], a[152:155], v[128:131], v[180:183]\n"
    "v_or3_b32 v250, v145, v146, v250\n"
    "v_bitop3_b32 v250, v147, v250, s56 bitop3:0xa8\n"
    "v_mfma_f32_16x16x32_bf16 v[180:183], a[132:135], v[120:123], v[180:183]\n"
    "v_cmp_ne_u32 vcc, 0, v250\n"
    "s_add_u32 s58, s58, s59\n"
    "v_mfma_f32_16x16x32_bf16 v[164:167], a[136:139], v[116:119], v[164:167]\n"
    "buffer_load_dwordx4 v[92:95], v251, s[36:39], s31 offen offset:2048 sc1\n"
    "v_mfma_f32_16x16x32_bf16 v[164:167], a[156:159], v[124:127], v[164:167]\n"
    "s_cbranch_vccnz .Lk_ool_57\n"
    ".Lk_ret_58:\n"
    "v_add_u32 v229, s58, v251\n"
    "v_mfma_f32_16x16x32_bf16 v[184:187], a[156:159], v[128:131], v[184:187]\n"
    "s_xor_b32 s58, s48, 3\n"
    "s_mul_i32 s58, s58, 40960\n"
    "v_mfma_f32_16x16x32_bf16 v[184:187], a[136:139], v[120:123], v[184:187]\n"
    "s_waitcnt vmcnt(12)\n"
    "v_xor_b32 v116, v52, v252\n"
    "v_xor_b32 v117, v53, v253\n"
    "v_mfma_f32_16x16x32_bf16 v[188:191], a[120:123], v[132:135], v[188:191]\n"
    "buffer_load_dwordx4 v[96:99], v251, s[36:39], s31 offen offset:3072 sc1\n"
    "v_mfma_f32_16x16x32_bf16 v[188:191], a[140:143], v[140:143], v[188:191]\n"
    "s_add_u32 s3, s62, 69632\n"
    "v_xor_b32_e64 v120, v54, v254\n"
    "v_mfma_f32_16x16x32_bf16 v[208:211], a[140:143], v[144:147], v[208:211]\n"
    "s_waitcnt vmcnt(12)\n"
    "v_xor_b32 v121, v55, v255\n"
    "v_xor_b32_e64 v118, v56, v252\n"
    "v_mfma_f32_16x16x32_bf16 v[208:211], a[120:123], v[136:139], v[208:211]\n"
    "v_xor_b32 v119, v57, v253\n"
    "v_xor_b32 v122, v58, v254\n"
    "v_mfma_f32_16x16x32_bf16 v[192:195], a[124:127], v[132:135], v[192:195]\n"
    "buffer_load_dwordx4 a[16:19], v251, s[32:35], s3 offen offset:0 sc1\n"
    "v_mfma_f32_16x16x32_bf16 v[192:195], a[144:147], v[140:143], v[192:195]\n"
    "s_waitcnt vmcnt(12)\n"
    "v_xor_b32 v123, v59, v255\n"
    "v_xor_b32_e64 v124, v60, v252\n"
    "v_mfma_f32_16x16x32_bf16 v[212:215], a[144:147], v[144:147], v[212:215]\n"
    "v_xor_b32 v125, v61, v253\n"
    "v_xor_b32 v128, v62, v254\n"
    "v_mfma_f32_16x16x32_bf16 v[212:215], a[124:127], v[136:139], v[212:215]\n"
    "s_waitcnt vmcnt(11)\n"
    "v_xor_b32 v129, v63, v255\n"
    "v_xor_b32_e64 v126, v64, v252\n"
    "v_mfma_f32_16x16x32_bf16 v[196:199], a[128:131], v[132:135], v[196:199]\n"
    "buffer_load_dwordx4 v[100:103], v251, s[44:47], s31 offen offset:0 sc1\n"
    "v_mfma_f32_16x16x32_bf16 v[196:199], a[148:151], v[140:143], v[196:199]\n"
    "v_xor_b32 v127, v65, v253\n"
    "v_xor_b32 v130, v66, v254\n"
    "v_mfma_f32_16x16x32_bf16 v[216:219], a[148:151], v[144:147], v[216:219]\n"
    "v_xor_b32_e64 v131, v67, v255\n"
    "v_or3_b32 v250, v116, v117, v118\n"
    "v_mfma_f32_16x16x32_bf16 v[216:219], a[128:131], v[136:139], v[216:219]\n"
    "v_or3_b32 v250, v119, v120, v250\n"
    "v_or3_b32 v250, v121, v122, v250\n"
    "v_mfma_f32_16x16x32_bf16 v[200:203], a[132:135], v[132:135], v[200:203]\n"
    "buffer_load_dwordx4 v[104:107], v251, s[44:47], s31 offen offset:1024 sc1\n"
    "v_mfma_f32_16x16x32_bf16 v[200:203], a[152:155], v[140:143], v[200:203]\n"
    "v_or3_b32 v250, v123, v124, v250\n"
    "v_or3_b32 v250, v125, v126, v250\n"
    "v_mfma_f32_16x16x32_bf16 v[220:223], a[152:155], v[144:147], v[220:223]\n"
    "v_or3_b32 v250, v127, v128, v250\n"
    "v_or3_b32 v250, v129, v130, v250\n"
    "v_mfma_f32_16x16x32_bf16 v[220:223], a[132:135], v[136:139], v[220:223]\n"
    "v_bitop3_b32 v250, v131, v250, s56 bitop3:0xa8\n"
    "v_cmp_ne_u32_e64 vcc, 0, v250\n"
    "v_mfma_f32_16x16x32_bf16 v[204:207], a[136:139], v[132:135], v[204:207]\n"
    "buffer_load_dwordx4 v[108:111], v251, s[44:47], s31 offen offset:2048 sc1\n"
    "v_mfma_f32_16x16x32_bf16 v[204:207], a[156:159], v[140:143], v[204:207]\n"
    "s_cbranch_vccnz .Lk_ool_59\n"
    ".Lk_ret_60:\n"
    "s_add_u32 s58, s58, s59\n"
    "v_mfma_f32_16x16x32_bf16 v[224:227], a[156:159], v[144:147], v[224:227]\n"
    "v_add_u32 v230, s58, v251\n"
    "s_lshl2_add_u32 s58, s52, s96\n"
    "v_mfma_f32_16x16x32_bf16 v[224:227], a[136:139], v[136:139], v[224:227]\n"
    "s_waitcnt vmcnt(12)\n"
    "v_xor_b32 v132, v68, v252\n"
    "v_xor_b32_e64 v133, v69, v253\n"
    "v_mfma_f32_16x16x32_bf16 v[148:151], a[160:163], v[116:119], v[148:151]\n"
    "buffer_load_dwordx4 v[112:115], v251, s[44:47], s31 offen offset:3072 sc1\n"
    "v_mfma_f32_16x16x32_bf16 v[148:151], a[180:183], v[124:127], v[148:151]\n"
    "v_xor_b32 v136, v70, v254\n"
    "v_xor_b32 v137, v71, v255\n"
    "v_mfma_f32_16x16x32_bf16 v[168:171], a[180:183], v[128:131], v[168:171]\n"
    "s_waitcnt vmcnt(12)\n"
    "v_xor_b32 v134, v72, v252\n"
    "v_xor_b32_e64 v135, v73, v253\n"
    "v_mfma_f32_16x16x32_bf16 v[168:171], a[160:163], v[120:123], v[168:171]\n"
    "v_xor_b32 v138, v74, v254\n"
    "v_xor_b32 v139, v75, v255\n"
    "v_mfma_f32_16x16x32_bf16 v[152:155], a[164:167], v[116:119], v[152:155]\n"
    "buffer_load_dwordx4 a[20:23], v251, s[32:35], s3 offen offset:1024 sc1\n"
    "v_mfma_f32_16x16x32_bf16 v[152:155], a[184:187], v[124:127], v[152:155]\n"
    "s_waitcnt vmcnt(12)\n"
    "v_xor_b32 v140, v76, v252\n"
    "v_xor_b32_e64 v141, v77, v253\n"
    "v_mfma_f32_16x16x32_bf16 v[172:175], a[184:187], v[128:131], v[172:175]\n"
    "v_xor_b32 v144, v78, v254\n"
    "v_xor_b32 v145, v79, v255\n"
    "v_mfma_f32_16x16x32_bf16 v[172:175], a[164:167], v[120:123], v[172:175]\n"
    "s_waitcnt vmcnt(11)\n"
    "v_xor_b32 v142, v80, v252\n"
    "v_xor_b32_e64 v143, v81, v253\n"
    "v_mfma_f32_16x16x32_bf16 v[156:159], a[168:171], v[116:119], v[156:159]\n"
    "buffer_load_dwordx4 v[20:23], v251, s[36:39], s98 offen offset:0 sc1\n"
    "v_mfma_f32_16x16x32_bf16 v[156:159], a[188:191], v[124:127], v[156:159]\n"
    "v_xor_b32 v146, v82, v254\n"
    "v_xor_b32 v147, v83, v255\n"
    "v_mfma_f32_16x16x32_bf16 v[176:179], a[188:191], v[128:131], v[176:179]\n"
    "v_or3_b32 v250, v132, v133, v134\n"
    "v_or3_b32 v250, v135, v136, v250\n"
    "v_mfma_f32_16x16x32_bf16 v[176:179], a[168:171], v[120:123], v[176:179]\n"
    "v_or3_b32 v250, v137, v138, v250\n"
    "v_or3_b32 v250, v139, v140, v250\n"
    "v_mfma_f32_16x16x32_bf16 v[160:163], a[172:175], v[116:119], v[160:163]\n"
    "buffer_load_dwordx4 v[24:27], v251, s[36:39], s98 offen offset:1024 sc1\n"
    "v_mfma_f32_16x16x32_bf16 v[160:163], a[192:195], v[124:127], v[160:163]\n"
    "v_or3_b32 v250, v141, v142, v250\n"
    "v_or3_b32 v250, v143, v144, v250\n"
    "v_mfma_f32_16x16x32_bf16 v[180:183], a[192:195], v[128:131], v[180:183]\n"
    "v_or3_b32 v250, v145, v146, v250\n"
    "v_bitop3_b32 v250, v147, v250, s56 bitop3:0xa8\n"
    "v_mfma_f32_16x16x32_bf16 v[180:183], a[172:175], v[120:123], v[180:183]\n"
    "v_cmp_ne_u32_e64 vcc, 0, v250\n"
    "v_mul_u32_u24 v233, 2560, v1\n"
    "v_mfma_f32_16x16x32_bf16 v[164:167], a[176:179], v[116:119], v[164:167]\n"
    "buffer_load_dwordx4 v[28:31], v251, s[36:39], s98 offen offset:2048 sc1\n"
    "v_mfma_f32_16x16x32_bf16 v[164:167], a[196:199], v[124:127], v[164:167]\n"
    "s_cbranch_vccnz .Lk_ool_61\n"
    ".Lk_ret_62:\n"
    "s_lshl_b32 s58, s58, 5\n"
    "v_mfma_f32_16x16x32_bf16 v[184:187], a[196:199], v[128:131], v[184:187]\n"
    "s_add_u32 s58, s58, s97\n"
    "s_mul_i32 s58, s58, 1280\n"
    "v_mfma_f32_16x16x32_bf16 v[184:187], a[176:179], v[120:123], v[184:187]\n"
    "s_waitcnt vmcnt(12)\n"
    "v_xor_b32 v116, v84, v252\n"
    "v_xor_b32 v117, v85, v253\n"
    "v_mfma_f32_16x16x32_bf16 v[188:191], a[160:163], v[132:135], v[188:191]\n"
    "buffer_load_dwordx4 v[32:35], v251, s[36:39], s98 offen offset:3072 sc1\n"
    "v_mfma_f32_16x16x32_bf16 v[188:191], a[180:183], v[140:143], v[188:191]\n"
    "v_xor_b32 v120, v86, v254\n"
    "v_xor_b32 v121, v87, v255\n"
    "v_mfma_f32_16x16x32_bf16 v[208:211], a[180:183], v[144:147], v[208:211]\n"
    "s_waitcnt vmcnt(12)\n"
    "v_xor_b32 v118, v88, v252\n"
    "v_xor_b32_e64 v119, v89, v253\n"
    "v_mfma_f32_16x16x32_bf16 v[208:211], a[160:163], v[136:139], v[208:211]\n"
    "v_xor_b32 v122, v90, v254\n"
    "v_xor_b32 v123, v91, v255\n"
    "v_mfma_f32_16x16x32_bf16 v[192:195], a[164:167], v[132:135], v[192:195]\n"
    "buffer_load_dwordx4 v[36:39], v251, s[44:47], s98 offen offset:0 sc1\n"
    "v_mfma_f32_16x16x32_bf16 v[192:195], a[184:187], v[140:143], v[192:195]\n"
    "s_waitcnt vmcnt(12)\n"
    "v_xor_b32 v124, v92, v252\n"
    "v_xor_b32_e64 v125, v93, v253\n"
    "v_mfma_f32_16x16x32_bf16 v[212:215], a[184:187], v[144:147], v[212:215]\n"
    "v_xor_b32 v128, v94, v254\n"
    "v_xor_b32 v129, v95, v255\n"
    "v_mfma_f32_16x16x32_bf16 v[212:215], a[164:167], v[136:139], v[212:215]\n"
    "s_waitcnt vmcnt(11)\n"
    "v_xor_b32 v126, v96, v252\n"
    "v_xor_b32_e64 v127, v97, v253\n"
    "v_mfma_f32_16x16x32_bf16 v[196:199], a[168:171], v[132:135], v[196:199]\n"
    "buffer_load_dwordx4 v[40:43], v251, s[44:47], s98 offen offset:1024 sc1\n"
    "v_mfma_f32_16x16x32_bf16 v[196:199], a[188:191], v[140:143], v[196:199]\n"
    "v_xor_b32 v130, v98, v254\n"
    "v_xor_b32 v131, v99, v255\n"
    "v_mfma_f32_16x16x32_bf16 v[216:219], a[188:191], v[144:147], v[216:219]\n"
    "v_or3_b32 v250, v116, v117, v118\n"
    "v_or3_b32 v250, v119, v120, v250\n"
    "v_mfma_f32_16x16x32_bf16 v[216:219], a[168:171], v[136:139], v[216:219]\n"
    "v_or3_b32 v250, v121, v122, v250\n"
    "v_or3_b32 v250, v123, v124, v250\n"
    "v_mfma_f32_16x16x32_bf16 v[200:203], a[172:175], v[132:135], v[200:203]\n"
    "buffer_load_dwordx4 v[44:47], v251, s[44:47], s98 offen offset:2048 sc1\n"
    "v_mfma_f32_16x16x32_bf16 v[200:203], a[192:195], v[140:143], v[200:203]\n"
    "v_or3_b32 v250, v125, v126, v250\n"
    "v_or3_b32 v250, v127, v128, v250\n"
    "v_mfma_f32_16x16x32_bf16 v[220:223], a[192:195], v[144:147], v[220:223]\n"
    "v_or3_b32 v250, v129, v130, v250\n"
    "v_bitop3_b32 v250, v131, v250, s56 bitop3:0xa8\n"
    "v_mfma_f32_16x16x32_bf16 v[220:223], a[172:175], v[136:139], v[220:223]\n"
    "v_cmp_ne_u32_e64 vcc, 0, v250\n"
    "s_mul_i32 s59, s63, 160\n"
    "v_mfma_f32_16x16x32_bf16 v[204:207], a[176:179], v[132:135], v[204:207]\n"
    "buffer_load_dwordx4 v[48:51], v251, s[44:47], s98 offen offset:3072 sc1\n"
    "v_mfma_f32_16x16x32_bf16 v[204:207], a[196:199], v[140:143], v[204:207]\n"
    "s_cbranch_vccnz .Lk_ool_63\n"
    ".Lk_ret_64:\n"
    "v_lshl_add_u32 v233, v2, 3, v233\n"
    "v_mfma_f32_16x16x32_bf16 v[224:227], a[196:199], v[144:147], v[224:227]\n"
    "s_add_u32 s58, s58, s59\n"
    "v_add_u32_e64 v233, s58, v233\n"
    "v_mfma_f32_16x16x32_bf16 v[224:227], a[176:179], v[136:139], v[224:227]\n"
    "s_waitcnt vmcnt(12)\n"
    "v_xor_b32 v132, v100, v252\n"
    "v_xor_b32_e64 v133, v101, v253\n"
    "v_mfma_f32_16x16x32_bf16 v[148:151], a[200:203], v[116:119], v[148:151]\n"
    "buffer_load_dwordx2 v[234:235], v251, s[32:35], s10 offen sc1\n"
    "v_mfma_f32_16x16x32_bf16 v[148:151], a[220:223], v[124:127], v[148:151]\n"
    "v_xor_b32 v136, v102, v254\n"
    "v_xor_b32 v137, v103, v255\n"
    "v_mfma_f32_16x16x32_bf16 v[168:171], a[220:223], v[128:131], v[168:171]\n"
    "s_waitcnt vmcnt(12)\n"
    "v_xor_b32 v134, v104, v252\n"
    "v_xor_b32_e64 v135, v105, v253\n"
    "v_mfma_f32_16x16x32_bf16 v[168:171], a[200:203], v[120:123], v[168:171]\n"
    "v_xor_b32 v138, v106, v254\n"
    "v_xor_b32 v139, v107, v255\n"
    "v_mfma_f32_16x16x32_bf16 v[152:155], a[204:207], v[116:119], v[152:155]\n"
    "buffer_load_dwordx2 v[4:5], v251, s[32:35], s54 offen offset:0 sc1\n"
    "v_mfma_f32_16x16x32_bf16 v[152:155], a[224:227], v[124:127], v[152:155]\n"
    "s_waitcnt vmcnt(12)\n"
    "v_xor_b32 v140, v108, v252\n"
    "v_xor_b32_e64 v141, v109, v253\n"
    "v_mfma_f32_16x16x32_bf16 v[172:175], a[224:227], v[128:131], v[172:175]\n"
    "v_xor_b32 v144, v110, v254\n"
    "v_xor_b32 v145, v111, v255\n"
    "v_mfma_f32_16x16x32_bf16 v[172:175], a[204:207], v[120:123], v[172:175]\n"
    "s_waitcnt vmcnt(11)\n"
    "v_xor_b32 v142, v112, v252\n"
    "v_xor_b32_e64 v143, v113, v253\n"
    "v_mfma_f32_16x16x32_bf16 v[156:159], a[208:211], v[116:119], v[156:159]\n"
    "buffer_load_dwordx2 v[6:7], v251, s[32:35], s54 offen offset:1024 sc1\n"
    "v_mfma_f32_16x16x32_bf16 v[156:159], a[228:231], v[124:127], v[156:159]\n"
    "v_xor_b32 v146, v114, v254\n"
    "v_xor_b32 v147, v115, v255\n"
    "v_mfma_f32_16x16x32_bf16 v[176:179], a[228:231], v[128:131], v[176:179]\n"
    "v_or3_b32 v250, v132, v133, v134\n"
    "v_or3_b32 v250, v135, v136, v250\n"
    "v_mfma_f32_16x16x32_bf16 v[176:179], a[208:211], v[120:123], v[176:179]\n"
    "v_or3_b32 v250, v137, v138, v250\n"
    "v_or3_b32 v250, v139, v140, v250\n"
    "v_mfma_f32_16x16x32_bf16 v[160:163], a[212:215], v[116:119], v[160:163]\n"
    "buffer_load_dwordx2 v[8:9], v251, s[32:35], s54 offen offset:2048 sc1\n"
    "v_mfma_f32_16x16x32_bf16 v[160:163], a[232:235], v[124:127], v[160:163]\n"
    "v_or3_b32 v250, v141, v142, v250\n"
    "v_or3_b32 v250, v143, v144, v250\n"
    "v_mfma_f32_16x16x32_bf16 v[180:183], a[232:235], v[128:131], v[180:183]\n"
    "v_or3_b32 v250, v145, v146, v250\n"
    "v_bitop3_b32 v250, v147, v250, s56 bitop3:0xa8\n"
    "v_mfma_f32_16x16x32_bf16 v[180:183], a[212:215], v[120:123], v[180:183]\n"
    "v_cmp_ne_u32_e64 vcc, 0, v250\n"
    "v_mfma_f32_16x16x32_bf16 v[164:167], a[216:219], v[116:119], v[164:167]\n"
    "buffer_load_dwordx2 v[10:11], v251, s[32:35], s54 offen offset:3072 sc1\n"
    "v_mfma_f32_16x16x32_bf16 v[164:167], a[236:239], v[124:127], v[164:167]\n"
    "s_cbranch_vccnz .Lk_ool_65\n"
    ".Lk_ret_66:\n"
    "v_mfma_f32_16x16x32_bf16 v[184:187], a[236:239], v[128:131], v[184:187]\n"
    "v_mfma_f32_16x16x32_bf16 v[184:187], a[216:219], v[120:123], v[184:187]\n"
    "s_waitcnt vmcnt(12)\n"
    "v_xor_b32 v116, v20, v252\n"
    "v_xor_b32 v117, v21, v253\n"
    "v_mfma_f32_16x16x32_bf16 v[188:191], a[200:203], v[132:135], v[188:191]\n"
    "buffer_load_dwordx2 v[12:13], v236, s[32:35], s54 offen offset:0 sc1\n"
    "v_mfma_f32_16x16x32_bf16 v[188:191], a[220:223], v[140:143], v[188:191]\n"
    "v_xor_b32 v120, v22, v254\n"
    "v_xor_b32 v121, v23, v255\n"
    "v_mfma_f32_16x16x32_bf16 v[208:211], a[220:223], v[144:147], v[208:211]\n"
    "s_waitcnt vmcnt(12)\n"
    "v_xor_b32 v118, v24, v252\n"
    "v_xor_b32_e64 v119, v25, v253\n"
    "v_mfma_f32_16x16x32_bf16 v[208:211], a[200:203], v[136:139], v[208:211]\n"
    "v_xor_b32 v122, v26, v254\n"
    "v_xor_b32 v123, v27, v255\n"
    "v_mfma_f32_16x16x32_bf16 v[192:195], a[204:207], v[132:135], v[192:195]\n"
    "s_waitcnt vmcnt(11)\n"
    "v_xor_b32 v124, v28, v252\n"
    "v_xor_b32_e64 v125, v29, v253\n"
    "v_mfma_f32_16x16x32_bf16 v[192:195], a[224:227], v[140:143], v[192:195]\n"
    "v_xor_b32 v128, v30, v254\n"
    "v_xor_b32 v129, v31, v255\n"
    "v_mfma_f32_16x16x32_bf16 v[212:215], a[224:227], v[144:147], v[212:215]\n"
    "s_waitcnt vmcnt(10)\n"
    "v_xor_b32 v126, v32, v252\n"
    "v_xor_b32_e64 v127, v33, v253\n"
    "v_mfma_f32_16x16x32_bf16 v[212:215], a[204:207], v[136:139], v[212:215]\n"
    "v_xor_b32 v130, v34, v254\n"
    "v_xor_b32 v131, v35, v255\n"
    "v_mfma_f32_16x16x32_bf16 v[196:199], a[208:211], v[132:135], v[196:199]\n"
    "v_or3_b32 v250, v116, v117, v118\n"
    "v_or3_b32 v250, v119, v120, v250\n"
    "v_mfma_f32_16x16x32_bf16 v[196:199], a[228:231], v[140:143], v[196:199]\n"
    "v_or3_b32 v250, v121, v122, v250\n"
    "v_or3_b32 v250, v123, v124, v250\n"
    "v_mfma_f32_16x16x32_bf16 v[216:219], a[228:231], v[144:147], v[216:219]\n"
    "v_or3_b32 v250, v125, v126, v250\n"
    "v_or3_b32 v250, v127, v128, v250\n"
    "v_mfma_f32_16x16x32_bf16 v[216:219], a[208:211], v[136:139], v[216:219]\n"
    "v_or3_b32 v250, v129, v130, v250\n"
    "v_bitop3_b32 v250, v131, v250, s56 bitop3:0xa8\n"
    "v_mfma_f32_16x16x32_bf16 v[200:203], a[212:215], v[132:135], v[200:203]\n"
    "v_cmp_ne_u32_e64 vcc, 0, v250\n"
    "v_mfma_f32_16x16x32_bf16 v[200:203], a[232:235], v[140:143], v[200:203]\n"
    "s_cbranch_vccnz .Lk_ool_67\n"
    ".Lk_ret_68:\n"
    "v_mfma_f32_16x16x32_bf16 v[220:223], a[232:235], v[144:147], v[220:223]\n"
    "v_mfma_f32_16x16x32_bf16 v[220:223], a[212:215], v[136:139], v[220:223]\n"
    "v_mfma_f32_16x16x32_bf16 v[204:207], a[216:219], v[132:135], v[204:207]\n"
    "v_mfma_f32_16x16x32_bf16 v[204:207], a[236:239], v[140:143], v[204:207]\n"
    "v_mfma_f32_16x16x32_bf16 v[224:227], a[236:239], v[144:147], v[224:227]\n"
    "v_mfma_f32_16x16x32_bf16 v[224:227], a[216:219], v[136:139], v[224:227]\n"
    "s_waitcnt vmcnt(9)\n"
    "v_xor_b32 v132, v36, v252\n"
    "v_xor_b32 v133, v37, v253\n"
    "v_mfma_f32_16x16x32_bf16 v[148:151], a[240:243], v[116:119], v[148:151]\n"
    "v_xor_b32 v136, v38, v254\n"
    "v_xor_b32 v137, v39, v255\n"
    "v_mfma_f32_16x16x32_bf16 v[148:151], a[4:7], v[124:127], v[148:151]\n"
    "s_waitcnt vmcnt(8)\n"
    "v_xor_b32 v134, v40, v252\n"
    "v_xor_b32_e64 v135, v41, v253\n"
    "v_mfma_f32_16x16x32_bf16 v[168:171], a[4:7], v[128:131], v[168:171]\n"
    "v_xor_b32 v138, v42, v254\n"
    "v_xor_b32 v139, v43, v255\n"
    "v_mfma_f32_16x16x32_bf16 v[168:171], a[240:243], v[120:123], v[168:171]\n"
    "s_waitcnt vmcnt(7)\n"
    "v_xor_b32 v140, v44, v252\n"
    "v_xor_b32_e64 v141, v45, v253\n"
    "v_mfma_f32_16x16x32_bf16 v[152:155], a[244:247], v[116:119], v[152:155]\n"
    "v_xor_b32 v144, v46, v254\n"
    "v_xor_b32 v145, v47, v255\n"
    "v_mfma_f32_16x16x32_bf16 v[152:155], a[8:11], v[124:127], v[152:155]\n"
    "ds_write_b128 v14, v[148:151] offset:0\n"
    "v_mfma_f32_16x16x32_bf16 v[172:175], a[8:11], v[128:131], v[172:175]\n"
    "s_waitcnt vmcnt(6)\n"
    "v_xor_b32 v142, v48, v252\n"
    "v_xor_b32_e64 v143, v49, v253\n"
    "v_mfma_f32_16x16x32_bf16 v[172:175], a[244:247], v[120:123], v[172:175]\n"
    "v_xor_b32 v146, v50, v254\n"
    "v_xor_b32 v147, v51, v255\n"
    "v_mfma_f32_16x16x32_bf16 v[156:159], a[248:251], v[116:119], v[156:159]\n"
    "ds_write_b128 v14, v[168:171] offset:5120\n"
    "v_mfma_f32_16x16x32_bf16 v[156:159], a[12:15], v[124:127], v[156:159]\n"
    "v_or3_b32 v250, v132, v133, v134\n"
    "v_or3_b32 v250, v135, v136, v250\n"
    "v_mfma_f32_16x16x32_bf16 v[176:179], a[12:15], v[128:131], v[176:179]\n"
    "v_or3_b32 v250, v137, v138, v250\n"
    "v_or3_b32 v250, v139, v140, v250\n"
    "v_mfma_f32_16x16x32_bf16 v[176:179], a[248:251], v[120:123], v[176:179]\n"
    "ds_write_b128 v14, v[152:155] offset:1024\n"
    "v_mfma_f32_16x16x32_bf16 v[160:163], a[252:255], v[116:119], v[160:163]\n"
    "v_or3_b32 v250, v141, v142, v250\n"
    "v_or3_b32 v250, v143, v144, v250\n"
    "v_mfma_f32_16x16x32_bf16 v[160:163], a[16:19], v[124:127], v[160:163]\n"
    "v_or3_b32 v250, v145, v146, v250\n"
    "v_bitop3_b32 v250, v147, v250, s56 bitop3:0xa8\n"
    "v_mfma_f32_16x16x32_bf16 v[180:183], a[16:19], v[128:131], v[180:183]\n"
    "ds_write_b128 v14, v[172:175] offset:6144\n"
    "v_mfma_f32_16x16x32_bf16 v[180:183], a[252:255], v[120:123], v[180:183]\n"
    "v_cmp_ne_u32_e64 vcc, 0, v250\n"
    "v_mfma_f32_16x16x32_bf16 v[164:167], a[0:3], v[116:119], v[164:167]\n"
    "s_cbranch_vccnz .Lk_ool_69\n"
    ".Lk_ret_70:\n"
    "v_mfma_f32_16x16x32_bf16 v[164:167], a[20:23], v[124:127], v[164:167]\n"
    "ds_write_b128 v14, v[156:159] offset:2048\n"
    "v_mfma_f32_16x16x32_bf16 v[184:187], a[20:23], v[128:131], v[184:187]\n"
    "s_waitcnt vmcnt(4)\n"
    "v_cmp_ne_u32 vcc, s51, v235\n"
    "v_lshlrev_b32 v90, 16, v4\n"
    "v_mfma_f32_16x16x32_bf16 v[184:187], a[0:3], v[120:123], v[184:187]\n"
    "s_cbranch_vccnz .Lk_ool_71\n"
    ".Lk_ret_72:\n"
    "v_mov_b32 v235, v234\n"
    "v_mfma_f32_16x16x32_bf16 v[188:191], a[240:243], v[132:135], v[188:191]\n"
    "ds_write_b128 v14, v[176:179] offset:7168\n"
    "v_mfma_f32_16x16x32_bf16 v[188:191], a[4:7], v[140:143], v[188:191]\n"
    "s_waitcnt lgkmcnt(6)\n"
    "v_mul_f32 v231, v231, v231\n"
    "v_and_b32 v91, 0xffff0000, v4\n"
    "v_mfma_f32_16x16x32_bf16 v[208:211], a[4:7], v[144:147], v[208:211]\n"
    "v_permlane32_swap_b32 v235, v234\n"
    "v_lshlrev_b32 v92, 16, v5\n"
    "v_mfma_f32_16x16x32_bf16 v[208:211], a[240:243], v[136:139], v[208:211]\n"
    "ds_write_b128 v14, v[160:163] offset:3072\n"
    "v_mfma_f32_16x16x32_bf16 v[192:195], a[244:247], v[132:135], v[192:195]\n"
    "v_add_f32_e64 v234, v234, v235\n"
    "v_and_b32 v93, 0xffff0000, v5\n"
    "v_mfma_f32_16x16x32_bf16 v[192:195], a[8:11], v[140:143], v[192:195]\n"
    "s_waitcnt vmcnt(3)\n"
    "v_mov_b32 v235, v234\n"
    "v_lshlrev_b32_e64 v94, 16, v6\n"
    "v_mfma_f32_16x16x32_bf16 v[212:215], a[8:11], v[144:147], v[212:215]\n"
    "ds_write_b128 v14, v[180:183] offset:8192\n"
    "v_mfma_f32_16x16x32_bf16 v[212:215], a[244:247], v[136:139], v[212:215]\n"
    "v_mov_b32 v232, 0x3acccccd\n"
    "v_and_b32 v95, 0xffff0000, v6\n"
    "v_mfma_f32_16x16x32_bf16 v[196:199], a[248:251], v[132:135], v[196:199]\n"
    "v_permlane16_swap_b32 v235, v234\n"
    "v_lshlrev_b32 v96, 16, v7\n"
    "v_mfma_f32_16x16x32_bf16 v[196:199], a[12:15], v[140:143], v[196:199]\n"
    "ds_write_b128 v14, v[164:167] offset:4096\n"
    "v_mfma_f32_16x16x32_bf16 v[216:219], a[12:15], v[144:147], v[216:219]\n"
    "v_add_f32_e64 v234, v234, v235\n"
    "v_and_b32 v97, 0xffff0000, v7\n"
    "v_mfma_f32_16x16x32_bf16 v[216:219], a[248:251], v[136:139], v[216:219]\n"
    "s_waitcnt vmcnt(2)\n"
    "v_fma_f32 v232, v234, v232, -v231\n"
    "v_lshlrev_b32 v98, 16, v8\n"
    "v_mfma_f32_16x16x32_bf16 v[200:203], a[252:255], v[132:135], v[200:203]\n"
    "ds_write_b128 v14, v[184:187] offset:9216\n"
    "v_mfma_f32_16x16x32_bf16 v[200:203], a[16:19], v[140:143], v[200:203]\n"
    "v_max_f32_e64 v232, 0, v232\n"
    "v_and_b32 v99, 0xffff0000, v8\n"
    "v_mfma_f32_16x16x32_bf16 v[220:223], a[16:19], v[144:147], v[220:223]\n"
    "v_add_f32 v232, 0x3727c5ac, v232\n"
    "v_lshlrev_b32_e64 v100, 16, v9\n"
    "v_mfma_f32_16x16x32_bf16 v[220:223], a[252:255], v[136:139], v[220:223]\n"
    "v_rsq_f32 v232, v232\n"
    "v_and_b32 v101, 0xffff0000, v9\n"
    "v_mfma_f32_16x16x32_bf16 v[204:207], a[0:3], v[132:135], v[204:207]\n"
    "s_waitcnt vmcnt(1)\n"
    "v_lshlrev_b32_e64 v102, 16, v10\n"
    "v_and_b32 v103, 0xffff0000, v10\n"
    "v_mfma_f32_16x16x32_bf16 v[204:207], a[20:23], v[140:143], v[204:207]\n"
    "v_lshlrev_b32_e64 v104, 16, v11\n"
    "v_and_b32 v105, 0xffff0000, v11\n"
    "v_mfma_f32_16x16x32_bf16 v[224:227], a[20:23], v[144:147], v[224:227]\n"
    "s_waitcnt vmcnt(0)\n"
    "v_lshlrev_b32 v106, 16, v12\n"
    "v_and_b32 v107, 0xffff0000, v12\n"
    "v_mfma_f32_16x16x32_bf16 v[224:227], a[0:3], v[136:139], v[224:227]\n"
    "v_lshlrev_b32_e64 v108, 16, v13\n"
    "v_and_b32 v109, 0xffff0000, v13\n"
    "s_cmp_lg_u32 s97, 0\n"
    "s_cbranch_scc1 .Lk_odd_73\n"
    "ds_write_b128 v15, v[208:211] offset:0\n"
    "ds_write_b128 v15, v[212:215] offset:1024\n"
    "ds_write_b128 v15, v[216:219] offset:2048\n"
    "ds_write_b128 v15, v[220:223] offset:3072\n"
    "ds_write_b128 v15, v[224:227] offset:4096\n"
    "s_branch .Lk_join_74\n"
    ".Lk_odd_73:\n"
    "s_nop 7\n"
    "ds_write_b128 v15, v[188:191] offset:0\n"
    "ds_write_b128 v15, v[192:195] offset:1024\n"
    "ds_write_b128 v15, v[196:199] offset:2048\n"
    "ds_write_b128 v15, v[200:203] offset:3072\n"
    "ds_write_b128 v15, v[204:207] offset:4096\n"
    "v_mov_b64 v[188:189], v[208:209]\n"
    "v_mov_b64 v[190:191], v[210:211]\n"
    "v_mov_b64 v[192:193], v[212:213]\n"
    "v_mov_b64 v[194:195], v[214:215]\n"
    "v_mov_b64 v[196:197], v[216:217]\n"
    "v_mov_b64 v[198:199], v[218:219]\n"
    "v_mov_b64 v[200:201], v[220:221]\n"
    "v_mov_b64 v[202:203], v[222:223]\n"
    "v_mov_b64 v[204:205], v[224:225]\n"
    "v_mov_b64 v[206:207], v[226:227]\n"
    ".Lk_join_74:\n"
    "s_waitcnt lgkmcnt(0)\n"
    "s_barrier\n"
    "ds_read_b128 v[20:23], v228 offset:0\n"
    "ds_read_b128 v[24:27], v229 offset:0\n"
    "ds_read_b128 v[28:31], v230 offset:0\n"
    "ds_read_b128 v[32:35], v228 offset:1024\n"
    "ds_read_b128 v[36:39], v229 offset:1024\n"
    "ds_read_b128 v[40:43], v230 offset:1024\n"
    "ds_read_b128 v[44:47], v228 offset:2048\n"
    "ds_read_b128 v[48:51], v229 offset:2048\n"
    "ds_read_b128 v[52:55], v230 offset:2048\n"
    "ds_read_b128 v[56:59], v228 offset:3072\n"
    "ds_read_b128 v[60:63], v229 offset:3072\n"
    "ds_read_b128 v[64:67], v230 offset:3072\n"
    "ds_read_b128 v[68:71], v228 offset:4096\n"
    "ds_read_b128 v[72:75], v229 offset:4096\n"
    "ds_read_b128 v[76:79], v230 offset:4096\n"
    "s_waitcnt lgkmcnt(12)\n"
    "s_nop 0\n"
    "v_pk_add_f32 v[24:25], v[24:25], v[28:29]\n"
    "v_pk_add_f32 v[26:27], v[26:27], v[30:31]\n"
    "v_pk_add_f32 v[188:189], v[188:189], v[24:25]\n"
    "v_pk_add_f32 v[190:191], v[190:191], v[26:27]\n"
    "v_pk_add_f32 v[20:21], v[20:21], v[188:189]\n"
    "v_pk_add_f32 v[22:23], v[22:23], v[190:191]\n"
    "v_pk_fma_f32 v[20:21], v[20:21], v[232:233], v[90:91] op_sel_hi:[1,0,1]\n"
    "v_pk_fma_f32 v[22:23], v[22:23], v[232:233], v[92:93] op_sel_hi:[1,0,1]\n"
    "v_cvt_pk_bf16_f32 v20, v20, v21\n"
    "v_cvt_pk_bf16_f32 v21, v22, v23\n"
    "global_store_dwordx2 v233, v[20:21], s[26:27] offset:0\n"
    "s_waitcnt lgkmcnt(9)\n"
    "s_nop 0\n"
    "v_pk_add_f32 v[36:37], v[36:37], v[40:41]\n"
    "v_pk_add_f32 v[38:39], v[38:39], v[42:43]\n"
    "v_pk_add_f32 v[192:193], v[192:193], v[36:37]\n"
    "v_pk_add_f32 v[194:195], v[194:195], v[38:39]\n"
    "v_pk_add_f32 v[32:33], v[32:33], v[192:193]\n"
    "v_pk_add_f32 v[34:35], v[34:35], v[194:195]\n"
    "v_pk_fma_f32 v[32:33], v[32:33], v[232:233], v[94:95] op_sel_hi:[1,0,1]\n"
    "v_pk_fma_f32 v[34:35], v[34:35], v[232:233], v[96:97] op_sel_hi:[1,0,1]\n"
    "v_cvt_pk_bf16_f32 v32, v32, v33\n"
    "v_cvt_pk_bf16_f32 v33, v34, v35\n"
    "global_store_dwordx2 v233, v[32:33], s[26:27] offset:32\n"
    "s_waitcnt lgkmcnt(6)\n"
    "s_nop 0\n"
    "v_pk_add_f32 v[48:49], v[48:49], v[52:53]\n"
    "v_pk_add_f32 v[50:51], v[50:51], v[54:55]\n"
    "v_pk_add_f32 v[196:197], v[196:197], v[48:49]\n"
    "v_pk_add_f32 v[198:199], v[198:199], v[50:51]\n"
    "v_pk_add_f32 v[44:45], v[44:45], v[196:197]\n"
    "v_pk_add_f32 v[46:47], v[46:47], v[198:199]\n"
    "v_pk_fma_f32 v[44:45], v[44:45], v[232:233], v[98:99] op_sel_hi:[1,0,1]\n"
    "v_pk_fma_f32 v[46:47], v[46:47], v[232:233], v[100:101] op_sel_hi:[1,0,1]\n"
    "v_cvt_pk_bf16_f32 v44, v44, v45\n"
    "v_cvt_pk_bf16_f32 v45, v46, v47\n"
    "global_store_dwordx2 v233, v[44:45], s[26:27] offset:64\n"
    "s_waitcnt lgkmcnt(3)\n"
    "s_nop 0\n"
    "v_pk_add_f32 v[60:61], v[60:61], v[64:65]\n"
    "v_pk_add_f32 v[62:63], v[62:63], v[66:67]\n"
    "v_pk_add_f32 v[200:201], v[200:201], v[60:61]\n"
    "v_pk_add_f32 v[202:203], v[202:203], v[62:63]\n"
    "v_pk_add_f32 v[56:57], v[56:57], v[200:201]\n"
    "v_pk_add_f32 v[58:59], v[58:59], v[202:203]\n"
    "v_pk_fma_f32 v[56:57], v[56:57], v[232:233], v[102:103] op_sel_hi:[1,0,1]\n"
    "v_pk_fma_f32 v[58:59], v[58:59], v[232:233], v[104:105] op_sel_hi:[1,0,1]\n"
    "v_cvt_pk_bf16_f32 v56, v56, v57\n"
    "v_cvt_pk_bf16_f32 v57, v58, v59\n"
    "global_store_dwordx2 v233, v[56:57], s[26:27] offset:96\n"
    "s_waitcnt lgkmcnt(0)\n"
    "s_nop 0\n"
    "v_pk_add_f32 v[72:73], v[72:73], v[76:77]\n"
    "v_pk_add_f32 v[74:75], v[74:75], v[78:79]\n"
    "v_pk_add_f32 v[204:205], v[204:205], v[72:73]\n"
    "v_pk_add_f32 v[206:207], v[206:207], v[74:75]\n"
    "v_pk_add_f32 v[68:69], v[68:69], v[204:205]\n"
    "v_pk_add_f32 v[70:71], v[70:71], v[206:207]\n"
    "v_pk_fma_f32 v[68:69], v[68:69], v[232:233], v[106:107] op_sel_hi:[1,0,1]\n"
    "v_pk_fma_f32 v[70:71], v[70:71], v[232:233], v[108:109] op_sel_hi:[1,0,1]\n"
    "v_cvt_pk_bf16_f32 v68, v68, v69\n"
    "v_cvt_pk_bf16_f32 v69, v70, v71\n"
    "global_store_dwordx2 v233, v[68:69], s[26:27] offset:128\n"
    "s_or_b32 s58, s2, s48\n"
    "s_cbranch_scc1 .Lk_bump_75\n"
    "v_mov_b32 v16, s51\n"
    "v_mov_b32_e64 v17, 0\n"
    "s_mov_b64 exec, 1\n"
    "buffer_store_dword v16, v17, s[32:35], 0 offen sc1\n"
    "s_mov_b64 exec, -1\n"
    ".Lk_bump_75:\n"
    "s_endpgm\n"
    ".Lk_helper:\n"
    "s_sub_u32 s54, s2, 160\n"
    "s_cmp_ge_u32 s96, 256\n"
    "s_cbranch_scc1 .Lk_w2task\n"
    "s_lshl_b32 s58, s96, 6\n"
    "s_add_u32 s58, s58, 65536\n"
    "v_lshl_add_u32 v7, v4, 3, s58\n"
    "v_mov_b32 v13, 0x3f803f80\n"
    "v_mov_b32 v8, 0\n"
    "v_mov_b32 v9, 0\n"
    "v_min_u32 v5, 33, v0\n"
    "v_lshlrev_b32 v5, 7, v5\n"
    "s_mul_i32 s11, s96, 8704\n"
    "s_mul_i32 s10, s96, 10240\n"
    "s_add_u32 s10, s10, 131072\n"
    "v_mov_b32 v124, -1\n"
    "v_mov_b32 v125, -1\n"
    "v_mov_b32 v126, -1\n"
    "v_mov_b32_e64 v127, -1\n"
    "s_waitcnt vmcnt(10)\n"
    "v_readfirstlane_b32 s51, v18\n"
    "s_nop 0\n"
    "s_add_u32 s51, s51, 1\n"
    "s_waitcnt vmcnt(9)\n"
    "v_dot2c_f32_bf16 v8, v20, v13\n"
    "v_dot2c_f32_bf16 v9, v20, v20\n"
    "v_dot2c_f32_bf16 v8, v21, v13\n"
    "v_dot2c_f32_bf16 v9, v21, v21\n"
    "v_dot2c_f32_bf16 v8, v22, v13\n"
    "v_dot2c_f32_bf16 v9, v22, v22\n"
    "v_dot2c_f32_bf16 v8, v23, v13\n"
    "v_dot2c_f32_bf16 v9, v23, v23\n"
    "s_waitcnt vmcnt(8)\n"
    "v_dot2c_f32_bf16 v8, v24, v13\n"
    "v_dot2c_f32_bf16 v9, v24, v24\n"
    "v_dot2c_f32_bf16 v8, v25, v13\n"
    "v_dot2c_f32_bf16 v9, v25, v25\n"
    "v_dot2c_f32_bf16 v8, v26, v13\n"
    "v_dot2c_f32_bf16 v9, v26, v26\n"
    "v_dot2c_f32_bf16 v8, v27, v13\n"
    "v_dot2c_f32_bf16 v9, v27, v27\n"
    "s_waitcnt vmcnt(7)\n"
    "v_dot2c_f32_bf16 v8, v28, v13\n"
    "v_dot2c_f32_bf16 v9, v28, v28\n"
    "v_dot2c_f32_bf16 v8, v29, v13\n"
    "v_dot2c_f32_bf16 v9, v29, v29\n"
    "v_dot2c_f32_bf16 v8, v30, v13\n"
    "v_dot2c_f32_bf16 v9, v30, v30\n"
    "v_dot2c_f32_bf16 v8, v31, v13\n"
    "v_dot2c_f32_bf16 v9, v31, v31\n"
    "s_waitcnt vmcnt(6)\n"
    "v_dot2c_f32_bf16 v8, v32, v13\n"
    "v_dot2c_f32_bf16 v9, v32, v32\n"
    "v_dot2c_f32_bf16 v8, v33, v13\n"
    "v_dot2c_f32_bf16 v9, v33, v33\n"
    "v_dot2c_f32_bf16 v8, v34, v13\n"
    "v_dot2c_f32_bf16 v9, v34, v34\n"
    "v_dot2c_f32_bf16 v8, v35, v13\n"
    "v_dot2c_f32_bf16 v9, v35, v35\n"
    "s_waitcnt vmcnt(5)\n"
    "v_dot2c_f32_bf16 v8, v36, v13\n"
    "v_dot2c_f32_bf16 v9, v36, v36\n"
    "v_dot2c_f32_bf16 v8, v37, v13\n"
    "v_dot2c_f32_bf16 v9, v37, v37\n"
    "v_dot2c_f32_bf16 v8, v38, v13\n"
    "v_dot2c_f32_bf16 v9, v38, v38\n"
    "v_dot2c_f32_bf16 v8, v39, v13\n"
    "v_dot2c_f32_bf16 v9, v39, v39\n"
    "s_waitcnt vmcnt(4)\n"
    "v_dot2c_f32_bf16 v8, v40, v13\n"
    "v_dot2c_f32_bf16 v9, v40, v40\n"
    "v_dot2c_f32_bf16 v8, v41, v13\n"
    "v_dot2c_f32_bf16 v9, v41, v41\n"
    "v_dot2c_f32_bf16 v8, v42, v13\n"
    "v_dot2c_f32_bf16 v9, v42, v42\n"
    "v_dot2c_f32_bf16 v8, v43, v13\n"
    "v_dot2c_f32_bf16 v9, v43, v43\n"
    "s_waitcnt vmcnt(3)\n"
    "v_dot2c_f32_bf16 v8, v44, v13\n"
    "v_dot2c_f32_bf16 v9, v44, v44\n"
    "v_dot2c_f32_bf16 v8, v45, v13\n"
    "v_dot2c_f32_bf16 v9, v45, v45\n"
    "v_dot2c_f32_bf16 v8, v46, v13\n"
    "v_dot2c_f32_bf16 v9, v46, v46\n"
    "v_dot2c_f32_bf16 v8, v47, v13\n"
    "v_dot2c_f32_bf16 v9, v47, v47\n"
    "s_waitcnt vmcnt(2)\n"
    "v_dot2c_f32_bf16 v8, v48, v13\n"
    "v_dot2c_f32_bf16 v9, v48, v48\n"
    "v_dot2c_f32_bf16 v8, v49, v13\n"
    "v_dot2c_f32_bf16 v9, v49, v49\n"
    "v_dot2c_f32_bf16 v8, v50, v13\n"
    "v_dot2c_f32_bf16 v9, v50, v50\n"
    "v_dot2c_f32_bf16 v8, v51, v13\n"
    "v_dot2c_f32_bf16 v9, v51, v51\n"
    "s_waitcnt vmcnt(1)\n"
    "v_dot2c_f32_bf16 v8, v52, v13\n"
    "v_dot2c_f32_bf16 v9, v52, v52\n"
    "v_dot2c_f32_bf16 v8, v53, v13\n"
    "v_dot2c_f32_bf16 v9, v53, v53\n"
    "v_dot2c_f32_bf16 v8, v54, v13\n"
    "v_dot2c_f32_bf16 v9, v54, v54\n"
    "v_dot2c_f32_bf16 v8, v55, v13\n"
    "v_dot2c_f32_bf16 v9, v55, v55\n"
    "s_waitcnt vmcnt(0)\n"
    "v_dot2c_f32_bf16 v8, v56, v13\n"
    "v_dot2c_f32_bf16 v9, v56, v56\n"
    "v_dot2c_f32_bf16 v8, v57, v13\n"
    "v_dot2c_f32_bf16 v9, v57, v57\n"
    "v_dot2c_f32_bf16 v8, v58, v13\n"
    "v_dot2c_f32_bf16 v9, v58, v58\n"
    "v_dot2c_f32_bf16 v8, v59, v13\n"
    "v_dot2c_f32_bf16 v9, v59, v59\n"
    "s_nop 1\n"
    "v_add_f32_dpp v8, v8, v8 quad_perm:[1,0,3,2] row_mask:0xf bank_mask:0xf\n"
    "v_add_f32_dpp v9, v9, v9 quad_perm:[1,0,3,2] row_mask:0xf bank_mask:0xf\n"
    "s_nop 0\n"
    "v_add_f32_dpp v8, v8, v8 quad_perm:[2,3,0,1] row_mask:0xf bank_mask:0xf\n"
    "v_add_f32_dpp v9, v9, v9 quad_perm:[2,3,0,1] row_mask:0xf bank_mask:0xf\n"
    "s_nop 0\n"
    "v_add_f32_dpp v8, v8, v8 row_half_mirror row_mask:0xf bank_mask:0xf\n"
    "v_add_f32_dpp v9, v9, v9 row_half_mirror row_mask:0xf bank_mask:0xf\n"
    "v_mul_f32 v8, 0x3acccccd, v8\n"
    "v_mul_f32 v9, 0x3acccccd, v9\n"
    "v_fma_f32 v9, -v8, v8, v9\n"
    "v_max_f32_e64 v9, 0, v9\n"
    "v_cvt_pk_bf16_f32 v9, 0, v9\n"
    "v_add_f32 v9, 0x3727c5ac, v9\n"
    "v_cvt_pk_bf16_f32 v9, 0, v9\n"
    "v_sqrt_f32 v9, v9\n"
    "s_mov_b32 exec_lo, 0x01010101\n"
    "s_mov_b32 exec_hi, 0x01010101\n"
    "v_cvt_pk_bf16_f32 v8, v8, v9\n"
    "v_mov_b32 v9, s51\n"
    "buffer_store_dwordx2 v[8:9], v7, s[32:35], 0 offen sc1\n"
    "s_mov_b64 exec, -1\n"
    "buffer_load_dword v254, v5, s[36:39], s11 offen\n"
    "s_add_u32 s11, s11, 4352\n"
    "buffer_load_dword v253, v5, s[36:39], s11 offen\n"
    "s_waitcnt vmcnt(0)\n"
    "buffer_store_dwordx4 v[124:127], v251, s[32:35], s10 offen offset:0 sc1\n"
    "buffer_store_dwordx4 v[124:127], v251, s[32:35], s10 offen offset:1024 sc1\n"
    "buffer_store_dwordx4 v[124:127], v251, s[32:35], s10 offen offset:2048 sc1\n"
    "buffer_store_dwordx4 v[124:127], v251, s[32:35], s10 offen offset:3072 sc1\n"
    "s_add_u32 s10, s10, 4096\n"
    "buffer_store_dwordx4 v[124:127], v251, s[32:35], s10 offen offset:0 sc1\n"
    "buffer_store_dwordx4 v[124:127], v251, s[32:35], s10 offen offset:1024 sc1\n"
    "buffer_store_dwordx4 v[124:127], v251, s[32:35], s10 offen offset:2048 sc1\n"
    "buffer_store_dwordx4 v[124:127], v251, s[32:35], s10 offen offset:3072 sc1\n"
    "s_add_u32 s10, s10, 4096\n"
    "buffer_store_dwordx4 v[124:127], v251, s[32:35], s10 offen offset:0 sc1\n"
    "buffer_store_dwordx4 v[124:127], v251, s[32:35], s10 offen offset:1024 sc1\n"
    "s_branch .Lk_hjoin\n"
    ".Lk_w2task:\n"
    "s_sub_u32 s96, 383, s96\n"
    "s_cmp_ge_u32 s96, 108\n"
    "s_cbranch_scc1 .Lk_idle\n"
    "s_lshr_b32 s58, s96, 1\n"
    "s_and_b32 s59, s96, 1\n"
    "s_cmp_ge_u32 s58, 14\n"
    "s_cselect_b32 s60, 1, 0\n"
    "s_cmp_ge_u32 s58, 28\n"
    "s_cselect_b32 s60, 2, s60\n"
    "s_cmp_ge_u32 s58, 41\n"
    "s_cselect_b32 s60, 3, s60\n"
    "s_mul_i32 s61, s60, 13\n"
    "s_min_u32 s3, s60, 1\n"
    "s_add_u32 s61, s61, s3\n"
    "s_sub_u32 s61, s58, s61\n"
    "s_lshl1_add_u32 s16, s60, s61\n"
    "s_add_u32 s3, s60, 2\n"
    "s_min_u32 s16, s16, s3\n"
    "s_and_b32 s3, s61, 1\n"
    "s_lshl1_add_u32 s3, s60, s3\n"
    "s_lshr_b32 s19, s61, 1\n"
    "s_sub_u32 s19, s19, 1\n"
    "s_movk_i32 s17, 4096\n"
    "s_mov_b32 s18, 117760\n"
    "s_cmp_lt_u32 s61, 2\n"
    "s_cselect_b32 s16, s16, s3\n"
    "s_cselect_b32 s19, 6, s19\n"
    "s_cselect_b32 s17, 5462, s17\n"
    "s_cselect_b32 s18, 122880, s18\n"
    "s_lshl_b32 s16, s16, 5\n"
    "s_lshl4_add_u32 s19, s19, s16\n"
    "s_mul_i32 s19, s19, 1280\n"
    "s_mul_i32 s3, s59, 640\n"
    "s_add_u32 s19, s19, s3\n"
    "v_add_u32 v30, 0, v0\n"
    "v_add_u32 v32, 64, v0\n"
    "v_add_u32 v34, 128, v0\n"
    "v_add_u32 v36, 192, v0\n"
    "v_add_u32 v38, 256, v0\n"
    "v_mul_u32_u24 v20, 1639, v30\n"
    "v_mul_u32_u24 v21, 1639, v32\n"
    "v_mul_u32_u24 v22, 1639, v34\n"
    "v_mul_u32_u24 v23, 1639, v36\n"
    "v_mul_u32_u24 v24, 1639, v38\n"
    "v_lshrrev_b32 v20, 16, v20\n"
    "v_lshrrev_b32 v21, 16, v21\n"
    "v_lshrrev_b32 v22, 16, v22\n"
    "v_lshrrev_b32 v23, 16, v23\n"
    "v_lshrrev_b32 v24, 16, v24\n"
    "v_mul_u32_u24 v31, 40, v20\n"
    "v_mul_u32_u24 v33, 40, v21\n"
    "v_mul_u32_u24 v35, 40, v22\n"
    "v_mul_u32_u24 v37, 40, v23\n"
    "v_mul_u32_u24 v39, 40, v24\n"
    "v_sub_u32 v30, v30, v31\n"
    "v_sub_u32 v32, v32, v33\n"
    "v_sub_u32 v34, v34, v35\n"
    "v_sub_u32 v36, v36, v37\n"
    "v_sub_u32_e64 v38, v38, v39\n"
    "v_bfe_u32 v31, v20, 1, 2\n"
    "v_bfe_u32 v33, v21, 1, 2\n"
    "v_bfe_u32 v35, v22, 1, 2\n"
    "v_bfe_u32 v37, v23, 1, 2\n"
    "v_bfe_u32 v39, v24, 1, 2\n"
    "v_lshlrev_b32 v31, 1, v31\n"
    "v_lshlrev_b32 v33, 1, v33\n"
    "v_lshlrev_b32 v35, 1, v35\n"
    "v_lshlrev_b32 v37, 1, v37\n"
    "v_lshlrev_b32 v39, 1, v39\n"
    "v_xor_b32 v30, v30, v31\n"
    "v_xor_b32 v32, v32, v33\n"
    "v_xor_b32 v34, v34, v35\n"
    "v_xor_b32 v36, v36, v37\n"
    "v_xor_b32 v38, v38, v39\n"
    "v_mul_u32_u24 v31, 1280, v20\n"
    "v_mul_u32_u24 v33, 1280, v21\n"
    "v_mul_u32_u24 v35, 1280, v22\n"
    "v_mul_u32_u24 v37, 1280, v23\n"
    "v_mul_u32_u24 v39, 1280, v24\n"
    "v_lshl_add_u32 v8, v30, 4, v31\n"
    "v_lshl_add_u32 v9, v32, 4, v33\n"
    "v_lshl_add_u32 v10, v34, 4, v35\n"
    "v_lshl_add_u32 v11, v36, 4, v37\n"
    "v_lshl_add_u32 v12, v38, 4, v39\n"
    "v_add_u32 v20, s16, v20\n"
    "v_add_u32 v21, s16, v21\n"
    "v_add_u32 v22, s16, v22\n"
    "v_add_u32 v23, s16, v23\n"
    "v_add_u32 v24, s16, v24\n"
    "s_mov_b64 s[40:41], s[24:25]\n"
    "s_mov_b32 s42, 2211840\n"
    "s_mov_b32 s43, 0x20000\n"
    "v_mul_u32_u24 v30, s17, v20\n"
    "v_mul_u32_u24 v31, s17, v21\n"
    "v_mul_u32_u24 v32, s17, v22\n"
    "v_mul_u32_u24 v33, s17, v23\n"
    "v_mul_u32_u24 v34, s17, v24\n"
    "v_lshrrev_b32 v30, 16, v30\n"
    "v_lshrrev_b32 v31, 16, v31\n"
    "v_lshrrev_b32 v32, 16, v32\n"
    "v_lshrrev_b32 v33, 16, v33\n"
    "v_lshrrev_b32 v34, 16, v34\n"
    "v_mad_u32_u24 v30, v30, s18, v8\n"
    "v_mad_u32_u24 v31, v31, s18, v9\n"
    "v_mad_u32_u24 v32, v32, s18, v10\n"
    "v_mad_u32_u24 v33, v33, s18, v11\n"
    "v_mad_u32_u24 v34, v34, s18, v12\n"
    "v_add_u32 v20, 8, v20\n"
    "v_add_u32 v21, 8, v21\n"
    "v_add_u32 v22, 8, v22\n"
    "v_add_u32 v23, 8, v23\n"
    "v_add_u32_e64 v24, 8, v24\n"
    "s_add_u32 m0, s55, 0\n"
    "s_nop 0\n"
    "buffer_load_dwordx4 v30, s[40:43], s19 offen lds\n"
    "s_add_u32 m0, s55, 1024\n"
    "s_nop 0\n"
    "buffer_load_dwordx4 v31, s[40:43], s19 offen lds\n"
    "s_add_u32 m0, s55, 2048\n"
    "s_nop 0\n"
    "buffer_load_dwordx4 v32, s[40:43], s19 offen lds\n"
    "s_add_u32 m0, s55, 3072\n"
    "s_nop 0\n"
    "buffer_load_dwordx4 v33, s[40:43], s19 offen lds\n"
    "s_add_u32 m0, s55, 4096\n"
    "s_nop 0\n"
    "buffer_load_dwordx4 v34, s[40:43], s19 offen lds\n"
    "s_add_u32 s19, s19, 10240\n"
    "v_mul_u32_u24 v30, s17, v20\n"
    "v_mul_u32_u24 v31, s17, v21\n"
    "v_mul_u32_u24 v32, s17, v22\n"
    "v_mul_u32_u24 v33, s17, v23\n"
    "v_mul_u32_u24 v34, s17, v24\n"
    "v_lshrrev_b32 v30, 16, v30\n"
    "v_lshrrev_b32 v31, 16, v31\n"
    "v_lshrrev_b32 v32, 16, v32\n"
    "v_lshrrev_b32 v33, 16, v33\n"
    "v_lshrrev_b32 v34, 16, v34\n"
    "v_mad_u32_u24 v30, v30, s18, v8\n"
    "v_mad_u32_u24 v31, v31, s18, v9\n"
    "v_mad_u32_u24 v32, v32, s18, v10\n"
    "v_mad_u32_u24 v33, v33, s18, v11\n"
    "v_mad_u32_u24 v34, v34, s18, v12\n"
    "v_add_u32 v20, 8, v20\n"
    "v_add_u32 v21, 8, v21\n"
    "v_add_u32 v22, 8, v22\n"
    "v_add_u32 v23, 8, v23\n"
    "v_add_u32_e64 v24, 8, v24\n"
    "s_add_u32 m0, s55, 5120\n"
    "s_nop 0\n"
    "buffer_load_dwordx4 v30, s[40:43], s19 offen lds\n"
    "s_add_u32 m0, s55, 6144\n"
    "s_nop 0\n"
    "buffer_load_dwordx4 v31, s[40:43], s19 offen lds\n"
    "s_add_u32 m0, s55, 7168\n"
    "s_nop 0\n"
    "buffer_load_dwordx4 v32, s[40:43], s19 offen lds\n"
    "s_add_u32 m0, s55, 8192\n"
    "s_nop 0\n"
    "buffer_load_dwordx4 v33, s[40:43], s19 offen lds\n"
    "s_add_u32 m0, s55, 9216\n"
    "s_nop 0\n"
    "buffer_load_dwordx4 v34, s[40:43], s19 offen lds\n"
    "s_add_u32 s19, s19, 10240\n"
    "v_mul_u32_u24 v30, s17, v20\n"
    "v_mul_u32_u24 v31, s17, v21\n"
    "v_mul_u32_u24 v32, s17, v22\n"
    "v_mul_u32_u24 v33, s17, v23\n"
    "v_mul_u32_u24 v34, s17, v24\n"
    "v_lshrrev_b32 v30, 16, v30\n"
    "v_lshrrev_b32 v31, 16, v31\n"
    "v_lshrrev_b32 v32, 16, v32\n"
    "v_lshrrev_b32 v33, 16, v33\n"
    "v_lshrrev_b32_e64 v34, 16, v34\n"
    "v_mad_u32_u24 v30, v30, s18, v8\n"
    "v_mad_u32_u24 v31, v31, s18, v9\n"
    "v_mad_u32_u24 v32, v32, s18, v10\n"
    "v_mad_u32_u24 v33, v33, s18, v11\n"
    "v_mad_u32_u24 v34, v34, s18, v12\n"
    "v_add_u32 v20, 8, v20\n"
    "v_add_u32 v21, 8, v21\n"
    "v_add_u32 v22, 8, v22\n"
    "v_add_u32 v23, 8, v23\n"
    "v_add_u32_e64 v24, 8, v24\n"
    "s_add_u32 m0, s55, 10240\n"
    "s_nop 0\n"
    "buffer_load_dwordx4 v30, s[40:43], s19 offen lds\n"
    "s_add_u32 m0, s55, 11264\n"
    "s_nop 0\n"
    "buffer_load_dwordx4 v31, s[40:43], s19 offen lds\n"
    "s_add_u32 m0, s55, 12288\n"
    "s_nop 0\n"
    "buffer_load_dwordx4 v32, s[40:43], s19 offen lds\n"
    "s_add_u32 m0, s55, 13312\n"
    "s_nop 0\n"
    "buffer_load_dwordx4 v33, s[40:43], s19 offen lds\n"
    "s_add_u32 m0, s55, 14336\n"
    "s_nop 0\n"
    "buffer_load_dwordx4 v34, s[40:43], s19 offen lds\n"
    "s_add_u32 s19, s19, 10240\n"
    "v_mul_u32_u24 v30, s17, v20\n"
    "v_mul_u32_u24 v31, s17, v21\n"
    "v_mul_u32_u24 v32, s17, v22\n"
    "v_mul_u32_u24 v33, s17, v23\n"
    "v_mul_u32_u24 v34, s17, v24\n"
    "v_lshrrev_b32 v30, 16, v30\n"
    "v_lshrrev_b32 v31, 16, v31\n"
    "v_lshrrev_b32 v32, 16, v32\n"
    "v_lshrrev_b32 v33, 16, v33\n"
    "v_lshrrev_b32_e64 v34, 16, v34\n"
    "v_mad_u32_u24 v30, v30, s18, v8\n"
    "v_mad_u32_u24 v31, v31, s18, v9\n"
    "v_mad_u32_u24 v32, v32, s18, v10\n"
    "v_mad_u32_u24 v33, v33, s18, v11\n"
    "v_mad_u32_u24 v34, v34, s18, v12\n"
    "v_add_u32 v20, 8, v20\n"
    "v_add_u32 v21, 8, v21\n"
    "v_add_u32 v22, 8, v22\n"
    "v_add_u32 v23, 8, v23\n"
    "v_add_u32_e64 v24, 8, v24\n"
    "s_add_u32 m0, s55, 15360\n"
    "s_nop 0\n"
    "buffer_load_dwordx4 v30, s[40:43], s19 offen lds\n"
    "s_add_u32 m0, s55, 16384\n"
    "s_nop 0\n"
    "buffer_load_dwordx4 v31, s[40:43], s19 offen lds\n"
    "s_add_u32 m0, s55, 17408\n"
    "s_nop 0\n"
    "buffer_load_dwordx4 v32, s[40:43], s19 offen lds\n"
    "s_add_u32 m0, s55, 18432\n"
    "s_nop 0\n"
    "buffer_load_dwordx4 v33, s[40:43], s19 offen lds\n"
    "s_add_u32 m0, s55, 19456\n"
    "s_nop 0\n"
    "buffer_load_dwordx4 v34, s[40:43], s19 offen lds\n"
    "s_add_u32 s19, s19, 10240\n"
    "v_lshrrev_b32 v4, 2, v0\n"
    "v_mul_u32_u24 v4, 640, v4\n"
    "v_and_b32_e64 v5, 3, v0\n"
    "v_lshl_add_u32 v4, v5, 3, v4\n"
    "v_add_u32_e64 v4, s55, v4\n"
    "v_bfe_u32 v5, v0, 3, 2\n"
    "v_xor_b32_e64 v6, 0, v5\n"
    "v_lshl_add_u32 v8, v6, 5, v4\n"
    "v_xor_b32_e64 v6, 1, v5\n"
    "v_lshl_add_u32 v9, v6, 5, v4\n"
    "v_xor_b32_e64 v6, 2, v5\n"
    "v_lshl_add_u32 v10, v6, 5, v4\n"
    "v_xor_b32_e64 v6, 3, v5\n"
    "v_lshl_add_u32 v11, v6, 5, v4\n"
    "s_mul_i32 s59, s59, 216\n"
    "s_add_u32 s58, s58, s59\n"
    "s_mul_i32 s58, s58, 5120\n"
    "s_lshr_b32 s61, s61, 1\n"
    "s_add_u32 s12, s58, 9830400\n"
    "s_add_u32 s13, s58, 10106880\n"
    "s_add_u32 s14, s58, 10383360\n"
    "s_add_u32 s15, s58, 10659840\n"
    "s_waitcnt vmcnt(20)\n"
    "v_readfirstlane_b32 s51, v18\n"
    "s_nop 0\n"
    "s_add_u32 s51, s51, 1\n"
    "s_waitcnt vmcnt(0)\n"
    "s_nop 0\n"
    "ds_read_b64_tr_b16 v[20:21], v8 offset:0\n"
    "ds_read_b64_tr_b16 v[22:23], v8 offset:10240\n"
    "ds_read_b64_tr_b16 v[24:25], v9 offset:0\n"
    "ds_read_b64_tr_b16 v[26:27], v9 offset:10240\n"
    "ds_read_b64_tr_b16 v[28:29], v10 offset:0\n"
    "ds_read_b64_tr_b16 v[30:31], v10 offset:10240\n"
    "ds_read_b64_tr_b16 v[32:33], v11 offset:0\n"
    "ds_read_b64_tr_b16 v[34:35], v11 offset:10240\n"
    "ds_read_b64_tr_b16 v[36:37], v8 offset:128\n"
    "ds_read_b64_tr_b16 v[38:39], v8 offset:10368\n"
    "ds_read_b64_tr_b16 v[40:41], v9 offset:128\n"
    "ds_read_b64_tr_b16 v[42:43], v9 offset:10368\n"
    "s_waitcnt lgkmcnt(10)\n"
    "buffer_store_dwordx4 v[20:23], v251, s[32:35], s12 offen offset:0 sc1\n"
    "ds_read_b64_tr_b16 v[44:45], v10 offset:128\n"
    "ds_read_b64_tr_b16 v[46:47], v10 offset:10368\n"
    "s_waitcnt lgkmcnt(10)\n"
    "buffer_store_dwordx4 v[24:27], v251, s[32:35], s12 offen offset:1024 sc1\n"
    "ds_read_b64_tr_b16 v[48:49], v11 offset:128\n"
    "ds_read_b64_tr_b16 v[50:51], v11 offset:10368\n"
    "s_waitcnt lgkmcnt(10)\n"
    "buffer_store_dwordx4 v[28:31], v251, s[32:35], s12 offen offset:2048 sc1\n"
    "ds_read_b64_tr_b16 v[52:53], v8 offset:256\n"
    "ds_read_b64_tr_b16 v[54:55], v8 offset:10496\n"
    "s_waitcnt lgkmcnt(10)\n"
    "buffer_store_dwordx4 v[32:35], v251, s[32:35], s12 offen offset:3072 sc1\n"
    "ds_read_b64_tr_b16 v[56:57], v9 offset:256\n"
    "ds_read_b64_tr_b16 v[58:59], v9 offset:10496\n"
    "s_add_u32 s12, s12, 4096\n"
    "s_waitcnt lgkmcnt(10)\n"
    "buffer_store_dwordx4 v[36:39], v251, s[32:35], s12 offen offset:0 sc1\n"
    "ds_read_b64_tr_b16 v[60:61], v10 offset:256\n"
    "ds_read_b64_tr_b16 v[62:63], v10 offset:10496\n"
    "s_waitcnt lgkmcnt(10)\n"
    "buffer_store_dwordx4 v[40:43], v251, s[32:35], s13 offen offset:0 sc1\n"
    "ds_read_b64_tr_b16 v[64:65], v11 offset:256\n"
    "ds_read_b64_tr_b16 v[66:67], v11 offset:10496\n"
    "s_waitcnt lgkmcnt(10)\n"
    "buffer_store_dwordx4 v[44:47], v251, s[32:35], s13 offen offset:1024 sc1\n"
    "ds_read_b64_tr_b16 v[68:69], v8 offset:384\n"
    "ds_read_b64_tr_b16 v[70:71], v8 offset:10624\n"
    "s_waitcnt lgkmcnt(10)\n"
    "buffer_store_dwordx4 v[48:51], v251, s[32:35], s13 offen offset:2048 sc1\n"
    "ds_read_b64_tr_b16 v[72:73], v9 offset:384\n"
    "ds_read_b64_tr_b16 v[74:75], v9 offset:10624\n"
    "s_waitcnt lgkmcnt(10)\n"
    "buffer_store_dwordx4 v[52:55], v251, s[32:35], s13 offen offset:3072 sc1\n"
    "ds_read_b64_tr_b16 v[76:77], v10 offset:384\n"
    "ds_read_b64_tr_b16 v[78:79], v10 offset:10624\n"
    "s_add_u32 s13, s13, 4096\n"
    "s_waitcnt lgkmcnt(10)\n"
    "buffer_store_dwordx4 v[56:59], v251, s[32:35], s13 offen offset:0 sc1\n"
    "ds_read_b64_tr_b16 v[80:81], v11 offset:384\n"
    "ds_read_b64_tr_b16 v[82:83], v11 offset:10624\n"
    "s_waitcnt lgkmcnt(10)\n"
    "buffer_store_dwordx4 v[60:63], v251, s[32:35], s14 offen offset:0 sc1\n"
    "ds_read_b64_tr_b16 v[84:85], v8 offset:512\n"
    "ds_read_b64_tr_b16 v[86:87], v8 offset:10752\n"
    "s_waitcnt lgkmcnt(10)\n"
    "buffer_store_dwordx4 v[64:67], v251, s[32:35], s14 offen offset:1024 sc1\n"
    "ds_read_b64_tr_b16 v[88:89], v9 offset:512\n"
    "ds_read_b64_tr_b16 v[90:91], v9 offset:10752\n"
    "s_waitcnt lgkmcnt(10)\n"
    "buffer_store_dwordx4 v[68:71], v251, s[32:35], s14 offen offset:2048 sc1\n"
    "ds_read_b64_tr_b16 v[92:93], v10 offset:512\n"
    "ds_read_b64_tr_b16 v[94:95], v10 offset:10752\n"
    "s_waitcnt lgkmcnt(10)\n"
    "buffer_store_dwordx4 v[72:75], v251, s[32:35], s14 offen offset:3072 sc1\n"
    "ds_read_b64_tr_b16 v[96:97], v11 offset:512\n"
    "ds_read_b64_tr_b16 v[98:99], v11 offset:10752\n"
    "s_add_u32 s14, s14, 4096\n"
    "s_waitcnt lgkmcnt(10)\n"
    "buffer_store_dwordx4 v[76:79], v251, s[32:35], s14 offen offset:0 sc1\n"
    "s_waitcnt lgkmcnt(8)\n"
    "buffer_store_dwordx4 v[80:83], v251, s[32:35], s15 offen offset:0 sc1\n"
    "s_waitcnt lgkmcnt(6)\n"
    "buffer_store_dwordx4 v[84:87], v251, s[32:35], s15 offen offset:1024 sc1\n"
    "s_waitcnt lgkmcnt(4)\n"
    "buffer_store_dwordx4 v[88:91], v251, s[32:35], s15 offen offset:2048 sc1\n"
    "s_waitcnt lgkmcnt(2)\n"
    "buffer_store_dwordx4 v[92:95], v251, s[32:35], s15 offen offset:3072 sc1\n"
    "s_add_u32 s15, s15, 4096\n"
    "s_waitcnt lgkmcnt(0)\n"
    "buffer_store_dwordx4 v[96:99], v251, s[32:35], s15 offen offset:0 sc1\n"
    "s_branch .Lk_hjoin\n"
    ".Lk_idle:\n"
    "s_waitcnt vmcnt(0)\n"
    "v_readfirstlane_b32 s51, v18\n"
    "s_nop 0\n"
    "s_add_u32 s51, s51, 1\n"
    ".Lk_hjoin:\n"
    "s_mul_i32 s58, s63, 54\n"
    "s_mul_i32 s62, s48, 13\n"
    "s_min_u32 s3, s48, 1\n"
    "s_add_u32 s58, s58, s3\n"
    "s_add_u32 s58, s58, s62\n"
    "s_mul_i32 s62, s58, 5120\n"
    "s_add_u32 s62, s62, 9830400\n"
    "v_min_u32 v15, 47, v0\n"
    "s_lshl_b32 s58, s49, 11\n"
    "s_add_u32 s58, s58, 4096\n"
    "v_lshl_add_u32 v15, v15, 4, s58\n"
    "v_mul_u32_u24 v118, 4682, v0\n"
    "s_movk_i32 s60, 3240\n"
    "v_add_u32 v119, 4096, v251\n"
    "v_add_u32 v104, 0, v118\n"
    "v_add_u32 v106, 299648, v118\n"
    "v_add_u32 v108, 599296, v118\n"
    "v_add_u32 v110, 898944, v118\n"
    "v_add_u32 v112, 1198592, v118\n"
    "v_add_u32 v114, 1498240, v118\n"
    "v_add_u32 v116, 1797888, v118\n"
    "v_add_u32 v105, 4682, v118\n"
    "v_add_u32 v107, 304330, v118\n"
    "v_add_u32 v109, 603978, v118\n"
    "v_add_u32 v111, 903626, v118\n"
    "v_add_u32 v113, 1203274, v118\n"
    "v_add_u32 v115, 1502922, v118\n"
    "v_add_u32 v117, 1802570, v118\n"
    "v_lshrrev_b32 v104, 16, v104\n"
    "v_lshrrev_b32 v106, 16, v106\n"
    "v_lshrrev_b32 v108, 16, v108\n"
    "v_lshrrev_b32 v110, 16, v110\n"
    "v_lshrrev_b32 v112, 16, v112\n"
    "v_lshrrev_b32 v114, 16, v114\n"
    "v_lshrrev_b32 v116, 16, v116\n"
    "v_lshrrev_b32 v105, 16, v105\n"
    "v_lshrrev_b32 v107, 16, v107\n"
    "v_lshrrev_b32 v109, 16, v109\n"
    "v_lshrrev_b32 v111, 16, v111\n"
    "v_lshrrev_b32 v113, 16, v113\n"
    "v_lshrrev_b32 v115, 16, v115\n"
    "v_lshrrev_b32 v117, 16, v117\n"
    "v_mad_u32_u24 v104, v104, s60, v251\n"
    "v_mad_u32_u24 v106, v106, s60, v251\n"
    "v_mad_u32_u24 v108, v108, s60, v251\n"
    "v_mad_u32_u24 v110, v110, s60, v251\n"
    "v_mad_u32_u24 v112, v112, s60, v119\n"
    "v_mad_u32_u24 v114, v114, s60, v119\n"
    "v_mad_u32_u24 v116, v116, s60, v119\n"
    "v_mad_i32_i24 v244, v105, -8, v104\n"
    "v_mad_i32_i24 v245, v107, -8, v106\n"
    "v_mad_i32_i24 v246, v109, -8, v108\n"
    "v_mad_i32_i24 v247, v111, -8, v110\n"
    "v_mad_i32_i24 v248, v113, -8, v112\n"
    "v_mad_i32_i24 v249, v115, -8, v114\n"
    "v_mad_i32_i24 v250, v117, -8, v116\n"
    "s_mul_i32 s60, s53, 216\n"
    "s_lshl2_add_u32 s12, s48, s74\n"
    "s_add_u32 s14, s12, 2\n"
    "s_add_u32 s16, s48, s74\n"
    "s_add_u32 s16, s16, 16\n"
    "s_sub_u32 s59, s12, 20\n"
    "s_min_u32 s12, s12, s59\n"
    "s_mul_i32 s12, s12, 110592\n"
    "s_add_u32 s12, s12, s60\n"
    "s_sub_u32 s59, s14, 20\n"
    "s_min_u32 s14, s14, s59\n"
    "s_mul_i32 s14, s14, 110592\n"
    "s_add_u32 s14, s14, s60\n"
    "s_sub_u32 s59, s16, 20\n"
    "s_min_u32 s16, s16, s59\n"
    "s_mul_i32 s16, s16, 110592\n"
    "s_add_u32 s16, s16, s60\n"
    "s_add_u32 s13, s12, 110592\n"
    "s_add_u32 s15, s14, 110592\n"
    "s_waitcnt vmcnt(0) lgkmcnt(0)\n"
    "s_barrier\n"
    "s_memrealtime s[0:1]\n"
    "s_memtime s[76:77]\n"
    "s_cmp_lg_u32 s48, 0\n"
    "s_cbranch_scc1 .Lk_fa_7\n"
    "v_lshlrev_b32 v16, 11, v0\n"
    "v_mov_b32 v17, s51\n"
    "s_lshl_b32 s3, s54, 3\n"
    "s_add_u32 s3, s3, 5120\n"
    "s_mov_b64 exec, 0xff\n"
    "buffer_store_dword v17, v16, s[32:35], s3 offen sc1\n"
    "s_mov_b64 exec, -1\n"
    ".Lk_fa_7:\n"
    "s_getpc_b64 s[40:41]\n"
    ".Lk_pc_8:\n"
    "s_sub_u32 s40, s40, .Lk_pc_8-.Lk_pf\n"
    "s_subb_u32 s41, s41, 0\n"
    "s_mov_b32 s42, .Lk_end-.Lk_pf\n"
    "s_mov_b32 s43, 0x20000\n"
    "s_sub_u32 s3, s50, 20\n"
    "s_lshl2_add_u32 s3, s3, s48\n"
    "s_lshl_b32 s3, s3, 7\n"
    "v_mul_u32_u24 v20, 6144, v0\n"
    "buffer_load_dword v21, v20, s[40:43], s3 offen\n"
    "s_add_u32 m0, s55, 28672\n"
    "s_nop 0\n"
    "buffer_load_dwordx4 v244, s[36:39], s16 offen offset:0 lds\n"
    "buffer_load_dwordx4 v245, s[36:39], s16 offen offset:1024 lds\n"
    "buffer_load_dwordx4 v246, s[36:39], s16 offen offset:2048 lds\n"
    "buffer_load_dwordx4 v247, s[36:39], s16 offen offset:3072 lds\n"
    "s_add_u32 m0, s55, 32768\n"
    "s_nop 0\n"
    "buffer_load_dwordx4 v248, s[36:39], s16 offen offset:0 lds\n"
    "buffer_load_dwordx4 v249, s[36:39], s16 offen offset:1024 lds\n"
    "buffer_load_dwordx4 v250, s[36:39], s16 offen offset:2048 lds\n"
    "s_add_u32 m0, s55, 0\n"
    "s_nop 0\n"
    "buffer_load_dwordx4 v244, s[36:39], s12 offen offset:0 lds\n"
    "buffer_load_dwordx4 v245, s[36:39], s12 offen offset:1024 lds\n"
    "buffer_load_dwordx4 v246, s[36:39], s12 offen offset:2048 lds\n"
    "buffer_load_dwordx4 v247, s[36:39], s12 offen offset:3072 lds\n"
    "s_add_u32 m0, s55, 4096\n"
    "s_nop 0\n"
    "buffer_load_dwordx4 v248, s[36:39], s12 offen offset:0 lds\n"
    "buffer_load_dwordx4 v249, s[36:39], s12 offen offset:1024 lds\n"
    "buffer_load_dwordx4 v250, s[36:39], s12 offen offset:2048 lds\n"
    "s_add_u32 m0, s55, 7168\n"
    "s_nop 0\n"
    "buffer_load_dwordx4 v244, s[36:39], s13 offen offset:0 lds\n"
    "buffer_load_dwordx4 v245, s[36:39], s13 offen offset:1024 lds\n"
    "buffer_load_dwordx4 v246, s[36:39], s13 offen offset:2048 lds\n"
    "buffer_load_dwordx4 v247, s[36:39], s13 offen offset:3072 lds\n"
    "s_add_u32 m0, s55, 11264\n"
    "s_nop 0\n"
    "buffer_load_dwordx4 v248, s[36:39], s13 offen offset:0 lds\n"
    "buffer_load_dwordx4 v249, s[36:39], s13 offen offset:1024 lds\n"
    "buffer_load_dwordx4 v250, s[36:39], s13 offen offset:2048 lds\n"
    "s_add_u32 m0, s55, 14336\n"
    "s_nop 0\n"
    "buffer_load_dwordx4 v244, s[36:39], s14 offen offset:0 lds\n"
    "buffer_load_dwordx4 v245, s[36:39], s14 offen offset:1024 lds\n"
    "buffer_load_dwordx4 v246, s[36:39], s14 offen offset:2048 lds\n"
    "buffer_load_dwordx4 v247, s[36:39], s14 offen offset:3072 lds\n"
    "s_add_u32 m0, s55, 18432\n"
    "s_nop 0\n"
    "buffer_load_dwordx4 v248, s[36:39], s14 offen offset:0 lds\n"
    "buffer_load_dwordx3 v[16:18], v15, s[32:35], 0 offen offset:1024 sc1\n"
    "buffer_load_dwordx4 v249, s[36:39], s14 offen offset:1024 lds\n"
    "buffer_load_dwordx4 v250, s[36:39], s14 offen offset:2048 lds\n"
    "s_add_u32 m0, s55, 21504\n"
    "s_nop 0\n"
    "buffer_load_dwordx4 v244, s[36:39], s15 offen offset:0 lds\n"
    "buffer_load_dwordx4 v245, s[36:39], s15 offen offset:1024 lds\n"
    "buffer_load_dwordx4 v246, s[36:39], s15 offen offset:2048 lds\n"
    "buffer_load_dwordx4 v247, s[36:39], s15 offen offset:3072 lds\n"
    "s_add_u32 m0, s55, 25600\n"
    "s_nop 0\n"
    "buffer_load_dwordx4 v248, s[36:39], s15 offen offset:0 lds\n"
    "buffer_load_dwordx4 v249, s[36:39], s15 offen offset:1024 lds\n"
    "buffer_load_dwordx4 v250, s[36:39], s15 offen offset:2048 lds\n"
    "v_lshrrev_b32 v4, 2, v0\n"
    "v_and_b32_e64 v5, 3, v0\n"
    "v_mul_u32_u24 v4, 224, v4\n"
    "v_lshl_add_u32 v236, v5, 3, v4\n"
    "v_add_u32_e64 v6, s55, v236\n"
    "v_mul_u32_u24 v7, 224, v0\n"
    "v_add_u32_e64 v7, s55, v7\n"
    "v_mov_b32 v8, 0x3f803f80\n"
    "v_mov_b32 v9, 0x3f803f80\n"
    "v_mov_b32 v10, 0x3f803f80\n"
    "v_mov_b32 v11, 0x3f803f80\n"
    "v_lshl_add_u32 v4, v1, 2, s55\n"
    "v_min_u32 v19, 27, v0\n"
    "v_lshlrev_b32 v12, 5, v19\n"
    "v_lshlrev_b32_e64 v19, 4, v19\n"
    "v_add_u32 v240, 35840, v19\n"
    "v_add_u32 v241, 76800, v19\n"
    "v_add_u32 v242, 117760, v19\n"
    "v_add_u32 v243, 158720, v19\n"
    "s_waitcnt vmcnt(9)\n"
    "v_xor_b32 v16, s51, v16\n"
    "v_bitop3_b32 v16, v18, s51, v16 bitop3:0xbe\n"
    "v_cmp_ne_u32 vcc, 0, v16\n"
    "s_nop 0\n"
    "s_cbranch_vccnz .Lk_ool_9\n"
    ".Lk_ret_10:\n"
    "s_add_u32 s3, s62, 0\n"
    "buffer_load_dwordx4 a[0:3], v251, s[32:35], s3 offen offset:0 sc1\n"
    "buffer_load_dwordx4 a[4:7], v251, s[32:35], s3 offen offset:1024 sc1\n"
    "buffer_load_dwordx4 a[8:11], v251, s[32:35], s3 offen offset:2048 sc1\n"
    "buffer_load_dwordx4 a[12:15], v251, s[32:35], s3 offen offset:3072 sc1\n"
    "s_add_u32 s3, s62, 4096\n"
    "buffer_load_dwordx4 a[16:19], v251, s[32:35], s3 offen offset:0 sc1\n"
    "buffer_load_dwordx4 a[20:23], v251, s[32:35], s3 offen offset:1024 sc1\n"
    "buffer_load_dwordx4 a[24:27], v251, s[32:35], s3 offen offset:2048 sc1\n"
    "buffer_load_dwordx4 a[28:31], v251, s[32:35], s3 offen offset:3072 sc1\n"
    "s_waitcnt vmcnt(8)\n"
    "s_nop 0\n"
    "ds_write_b64 v7, v[8:9] offset:208\n"
    "ds_write_b64 v7, v[8:9] offset:14544\n"
    "ds_read_b64_tr_b16 v[148:149], v6 offset:0\n"
    "ds_read_b64_tr_b16 v[150:151], v6 offset:3584\n"
    "ds_read_b64_tr_b16 v[152:153], v6 offset:32\n"
    "ds_read_b64_tr_b16 v[154:155], v6 offset:3616\n"
    "ds_read_b64_tr_b16 v[156:157], v6 offset:64\n"
    "ds_read_b64_tr_b16 v[158:159], v6 offset:3648\n"
    "ds_read_b64_tr_b16 v[160:161], v6 offset:96\n"
    "ds_read_b64_tr_b16 v[162:163], v6 offset:3680\n"
    "ds_read_b64_tr_b16 v[164:165], v6 offset:128\n"
    "ds_read_b64_tr_b16 v[166:167], v6 offset:3712\n"
    "ds_read_b64_tr_b16 v[168:169], v6 offset:160\n"
    "ds_read_b64_tr_b16 v[170:171], v6 offset:3744\n"
    "ds_read_b64_tr_b16 v[172:173], v6 offset:192\n"
    "ds_read_b64_tr_b16 v[174:175], v6 offset:3776\n"
    "s_waitcnt lgkmcnt(0)\n"
    "v_mfma_f32_16x16x32_bf16 v[180:183], v[8:11], v[148:151], 0\n"
    "ds_read_b64_tr_b16 v[176:177], v6 offset:7168\n"
    "ds_read_b64_tr_b16 v[178:179], v6 offset:10752\n"
    "v_mfma_f32_16x16x32_bf16 v[184:187], v[8:11], v[152:155], 0\n"
    "s_add_u32 s3, s62, 8192\n"
    "buffer_load_dwordx4 a[32:35], v251, s[32:35], s3 offen offset:0 sc1\n"
    "ds_read_b64_tr_b16 v[148:149], v6 offset:7200\n"
    "ds_read_b64_tr_b16 v[150:151], v6 offset:10784\n"
    "v_mfma_f32_16x16x32_bf16 v[188:191], v[8:11], v[156:159], 0\n"
    "ds_read_b64_tr_b16 v[152:153], v6 offset:7232\n"
    "ds_read_b64_tr_b16 v[154:155], v6 offset:10816\n"
    "v_mfma_f32_16x16x32_bf16 v[192:195], v[8:11], v[160:163], 0\n"
    "ds_read_b64_tr_b16 v[156:157], v6 offset:7264\n"
    "ds_read_b64_tr_b16 v[158:159], v6 offset:10848\n"
    "v_mfma_f32_16x16x32_bf16 v[196:199], v[8:11], v[164:167], 0\n"
    "buffer_load_dwordx4 a[36:39], v251, s[32:35], s3 offen offset:1024 sc1\n"
    "ds_read_b64_tr_b16 v[160:161], v6 offset:7296\n"
    "ds_read_b64_tr_b16 v[162:163], v6 offset:10880\n"
    "v_mfma_f32_16x16x32_bf16 v[200:203], v[8:11], v[168:171], 0\n"
    "ds_read_b64_tr_b16 v[164:165], v6 offset:7328\n"
    "ds_read_b64_tr_b16 v[166:167], v6 offset:10912\n"
    "v_mfma_f32_16x16x32_bf16 v[204:207], v[8:11], v[172:175], 0\n"
    "s_waitcnt lgkmcnt(10)\n"
    "ds_read_b64_tr_b16 v[168:169], v6 offset:7360\n"
    "ds_read_b64_tr_b16 v[170:171], v6 offset:10944\n"
    "v_mfma_f32_16x16x32_bf16 v[180:183], v[8:11], v[176:179], v[180:183]\n"
    "s_waitcnt lgkmcnt(10)\n"
    "buffer_load_dwordx4 a[40:43], v251, s[32:35], s3 offen offset:2048 sc1\n"
    "ds_read_b64_tr_b16 v[172:173], v6 offset:14336\n"
    "ds_read_b64_tr_b16 v[174:175], v6 offset:17920\n"
    "v_mfma_f32_16x16x32_bf16 v[184:187], v[8:11], v[148:151], v[184:187]\n"
    "s_waitcnt lgkmcnt(10)\n"
    "ds_read_b64_tr_b16 v[176:177], v6 offset:14368\n"
    "ds_read_b64_tr_b16 v[178:179], v6 offset:17952\n"
    "v_mfma_f32_16x16x32_bf16 v[188:191], v[8:11], v[152:155], v[188:191]\n"
    "s_waitcnt lgkmcnt(10)\n"
    "ds_read_b64_tr_b16 v[148:149], v6 offset:14400\n"
    "ds_read_b64_tr_b16 v[150:151], v6 offset:17984\n"
    "v_mfma_f32_16x16x32_bf16 v[192:195], v[8:11], v[156:159], v[192:195]\n"
    "s_waitcnt lgkmcnt(10)\n"
    "buffer_load_dwordx4 a[44:47], v251, s[32:35], s3 offen offset:3072 sc1\n"
    "ds_read_b64_tr_b16 v[152:153], v6 offset:14432\n"
    "ds_read_b64_tr_b16 v[154:155], v6 offset:18016\n"
    "v_mfma_f32_16x16x32_bf16 v[196:199], v[8:11], v[160:163], v[196:199]\n"
    "s_waitcnt lgkmcnt(10)\n"
    "ds_read_b64_tr_b16 v[156:157], v6 offset:14464\n"
    "ds_read_b64_tr_b16 v[158:159], v6 offset:18048\n"
    "v_mfma_f32_16x16x32_bf16 v[200:203], v[8:11], v[164:167], v[200:203]\n"
    "s_waitcnt lgkmcnt(10)\n"
    "ds_read_b64_tr_b16 v[160:161], v6 offset:14496\n"
    "ds_read_b64_tr_b16 v[162:163], v6 offset:18080\n"
    "v_mfma_f32_16x16x32_bf16 v[204:207], v[8:11], v[168:171], v[204:207]\n"
    "s_waitcnt lgkmcnt(10)\n"
    "s_add_u32 s3, s62, 12288\n"
    "buffer_load_dwordx4 a[48:51], v251, s[32:35], s3 offen offset:0 sc1\n"
    "ds_read_b64_tr_b16 v[164:165], v6 offset:14528\n"
    "ds_read_b64_tr_b16 v[166:167], v6 offset:18112\n"
    "v_mfma_f32_16x16x32_bf16 v[180:183], v[8:11], v[172:175], v[180:183]\n"
    "s_waitcnt lgkmcnt(10)\n"
    "ds_read_b64_tr_b16 v[168:169], v6 offset:21504\n"
    "ds_read_b64_tr_b16 v[170:171], v6 offset:25088\n"
    "v_mfma_f32_16x16x32_bf16 v[184:187], v[8:11], v[176:179], v[184:187]\n"
    "s_waitcnt lgkmcnt(10)\n"
    "ds_read_b64_tr_b16 v[172:173], v6 offset:21536\n"
    "ds_read_b64_tr_b16 v[174:175], v6 offset:25120\n"
    "v_mfma_f32_16x16x32_bf16 v[188:191], v[8:11], v[148:151], v[188:191]\n"
    "s_waitcnt lgkmcnt(10)\n"
    "buffer_load_dwordx4 a[52:55], v251, s[32:35], s3 offen offset:1024 sc1\n"
    "ds_read_b64_tr_b16 v[176:177], v6 offset:21568\n"
    "ds_read_b64_tr_b16 v[178:179], v6 offset:25152\n"
    "v_mfma_f32_16x16x32_bf16 v[192:195], v[8:11], v[152:155], v[192:195]\n"
    "s_waitcnt lgkmcnt(10)\n"
    "ds_read_b64_tr_b16 v[148:149], v6 offset:21600\n"
    "ds_read_b64_tr_b16 v[150:151], v6 offset:25184\n"
    "v_mfma_f32_16x16x32_bf16 v[196:199], v[8:11], v[156:159], v[196:199]\n"
    "s_waitcnt lgkmcnt(10)\n"
    "ds_read_b64_tr_b16 v[152:153], v6 offset:21632\n"
    "ds_read_b64_tr_b16 v[154:155], v6 offset:25216\n"
    "v_mfma_f32_16x16x32_bf16 v[200:203], v[8:11], v[160:163], v[200:203]\n"
    "s_waitcnt lgkmcnt(10)\n"
    "buffer_load_dwordx4 a[56:59], v251, s[32:35], s3 offen offset:2048 sc1\n"
    "ds_read_b64_tr_b16 v[156:157], v6 offset:21664\n"
    "ds_read_b64_tr_b16 v[158:159], v6 offset:25248\n"
    "v_mfma_f32_16x16x32_bf16 v[204:207], v[8:11], v[164:167], v[204:207]\n"
    "s_waitcnt lgkmcnt(10)\n"
    "ds_read_b64_tr_b16 v[160:161], v6 offset:21696\n"
    "ds_read_b64_tr_b16 v[162:163], v6 offset:25280\n"
    "v_mfma_f32_16x16x32_bf16 v[180:183], v[8:11], v[168:171], v[180:183]\n"
    "s_waitcnt lgkmcnt(10)\n"
    "ds_read_b64_tr_b16 v[164:165], v6 offset:28672\n"
    "ds_read_b64_tr_b16 v[166:167], v6 offset:32256\n"
    "v_mfma_f32_16x16x32_bf16 v[184:187], v[8:11], v[172:175], v[184:187]\n"
    "s_waitcnt lgkmcnt(10)\n"
    "buffer_load_dwordx4 a[60:63], v251, s[32:35], s3 offen offset:3072 sc1\n"
    "ds_read_b64_tr_b16 v[168:169], v6 offset:28704\n"
    "ds_read_b64_tr_b16 v[170:171], v6 offset:32288\n"
    "v_mfma_f32_16x16x32_bf16 v[188:191], v[8:11], v[176:179], v[188:191]\n"
    "s_waitcnt lgkmcnt(10)\n"
    "ds_read_b64_tr_b16 v[172:173], v6 offset:28736\n"
    "ds_read_b64_tr_b16 v[174:175], v6 offset:32320\n"
    "v_mfma_f32_16x16x32_bf16 v[192:195], v[8:11], v[148:151], v[192:195]\n"
    "s_waitcnt lgkmcnt(10)\n"
    "ds_read_b64_tr_b16 v[176:177], v6 offset:28768\n"
    "ds_read_b64_tr_b16 v[178:179], v6 offset:32352\n"
    "v_mfma_f32_16x16x32_bf16 v[196:199], v[8:11], v[152:155], v[196:199]\n"
    "s_waitcnt lgkmcnt(10)\n"
    "s_add_u32 s3, s62, 16384\n"
    "buffer_load_dwordx4 a[64:67], v251, s[32:35], s3 offen offset:0 sc1\n"
    "ds_read_b64_tr_b16 v[148:149], v6 offset:28800\n"
    "ds_read_b64_tr_b16 v[150:151], v6 offset:32384\n"
    "v_mfma_f32_16x16x32_bf16 v[200:203], v[8:11], v[156:159], v[200:203]\n"
    "s_waitcnt lgkmcnt(10)\n"
    "ds_read_b64_tr_b16 v[152:153], v6 offset:28832\n"
    "ds_read_b64_tr_b16 v[154:155], v6 offset:32416\n"
    "v_mfma_f32_16x16x32_bf16 v[204:207], v[8:11], v[160:163], v[204:207]\n"
    "s_waitcnt lgkmcnt(10)\n"
    "ds_read_b64_tr_b16 v[156:157], v6 offset:28864\n"
    "ds_read_b64_tr_b16 v[158:159], v6 offset:32448\n"
    "v_mfma_f32_16x16x32_bf16 v[180:183], v[8:11], v[164:167], v[180:183]\n"
    "s_waitcnt lgkmcnt(10)\n"
    "buffer_load_dwordx4 a[68:71], v251, s[32:35], s3 offen offset:1024 sc1\n"
    "v_mfma_f32_16x16x32_bf16 v[184:187], v[8:11], v[168:171], v[184:187]\n"
    "s_waitcnt lgkmcnt(8)\n"
    "v_mfma_f32_16x16x32_bf16 v[188:191], v[8:11], v[172:175], v[188:191]\n"
    "s_waitcnt lgkmcnt(6)\n"
    "v_mfma_f32_16x16x32_bf16 v[192:195], v[8:11], v[176:179], v[192:195]\n"
    "s_waitcnt lgkmcnt(4)\n"
    "buffer_load_dwordx4 a[72:75], v251, s[32:35], s3 offen offset:2048 sc1\n"
    "v_mfma_f32_16x16x32_bf16 v[196:199], v[8:11], v[148:151], v[196:199]\n"
    "s_waitcnt lgkmcnt(2)\n"
    "v_mfma_f32_16x16x32_bf16 v[200:203], v[8:11], v[152:155], v[200:203]\n"
    "s_waitcnt lgkmcnt(0)\n"
    "v_mfma_f32_16x16x32_bf16 v[204:207], v[8:11], v[156:159], v[204:207]\n"
    "s_nop 0\n"
    "buffer_load_dwordx4 a[76:79], v251, s[32:35], s3 offen offset:3072 sc1\n"
    "ds_write_b32 v4, v180 offset:35840\n"
    "ds_write_b32 v4, v184 offset:35904\n"
    "ds_write_b32 v4, v188 offset:35968\n"
    "ds_write_b32 v4, v192 offset:36032\n"
    "ds_write_b32 v4, v196 offset:36096\n"
    "ds_write_b32 v4, v200 offset:36160\n"
    "s_nop 0\n"
    "ds_write_b32 v4, v204 offset:36224\n"
    "s_waitcnt lgkmcnt(0)\n"
    "s_barrier\n"
    "s_cmp_lg_u32 s48, 0\n"
    "s_cbranch_scc1 .Lk_nocs_11\n"
    "s_cmp_lt_u32 s50, 24\n"
    "s_cbranch_scc1 .Lk_nocs_11\n"
    "ds_read_b128 v[180:183], v240\n"
    "ds_read_b128 v[184:187], v241\n"
    "ds_read_b128 v[188:191], v242\n"
    "ds_read_b128 v[192:195], v243\n"
    "s_lshl3_add_u32 s58, s49, s50\n"
    "s_lshl_b32 s58, s58, 10\n"
    "s_add_u32 s58, s58, 12034048\n"
    "s_waitcnt lgkmcnt(2)\n"
    "v_pk_add_f32 v[180:181], v[180:181], v[184:185]\n"
    "s_waitcnt lgkmcnt(0)\n"
    "v_pk_add_f32 v[188:189], v[188:189], v[192:193]\n"
    "v_pk_add_f32 v[182:183], v[182:183], v[186:187]\n"
    "v_pk_add_f32 v[190:191], v[190:191], v[194:195]\n"
    "v_add_f32 v196, v180, v188\n"
    "v_mov_b32 v197, s51\n"
    "v_add_f32 v198, v181, v189\n"
    "v_mov_b32 v199, s51\n"
    "v_add_f32 v200, v182, v190\n"
    "v_mov_b32 v201, s51\n"
    "v_add_f32 v202, v183, v191\n"
    "v_mov_b32_e64 v203, s51\n"
    "buffer_store_dwordx4 v[196:199], v12, s[32:35], s58 offen sc1\n"
    "buffer_store_dwordx4 v[200:203], v12, s[32:35], s58 offen offset:16 sc1\n"
    ".Lk_nocs_11:\n"
    "s_lshl_b32 s17, s49, 11\n"
    "s_nop 0\n"
    "s_add_u32 s17, s17, 32768\n"
    "buffer_load_dwordx4 v[196:199], v251, s[32:35], s17 offen sc1\n"
    "buffer_load_dwordx2 v[200:201], v251, s[32:35], s17 offen offset:1024 sc1\n"
    "s_add_u32 s3, s62, 20480\n"
    "buffer_load_dwordx4 a[80:83], v251, s[32:35], s3 offen offset:0 sc1\n"
    "buffer_load_dwordx4 a[84:87], v251, s[32:35], s3 offen offset:1024 sc1\n"
    "s_waitcnt vmcnt(2)\n"
    "s_getpc_b64 s[40:41]\n"
    ".Lk_pc_12:\n"
    "s_sub_u32 s40, s40, .Lk_pc_12-.Lk_pf\n"
    "s_subb_u32 s41, s41, 0\n"
    "s_mov_b32 s42, .Lk_end-.Lk_pf\n"
    "s_mov_b32 s43, 0x20000\n"
    "s_sub_u32 s3, s50, 20\n"
    "s_lshl2_add_u32 s3, s3, s48\n"
    "s_lshl_b32 s3, s3, 7\n"
    "v_mul_u32_u24 v20, 6144, v0\n"
    "buffer_load_dword v235, v20, s[40:43], s3 offen\n"
    "s_nop 4\n"
    "s_branch .Lk_gemm1\n"
    ".Lk_ool_3:\n"
    "s_nop 1\n"
    "s_mov_b32 s57, 0\n"
    "s_waitcnt vmcnt(0) lgkmcnt(0)\n"
    ".Lk_spin_76:\n"
    "s_lshl_b32 s58, s96, 10\n"
    "s_add_u32 s58, s58, 65536\n"
    "buffer_load_dwordx4 v[228:231], v251, s[32:35], s58 offen sc1\n"
    "s_lshl_b32 s58, s97, 10\n"
    "s_add_u32 s58, s58, 65536\n"
    "buffer_load_dwordx4 v[232:235], v251, s[32:35], s58 offen sc1\n"
    "s_lshl_b32 s58, s98, 10\n"
    "s_add_u32 s58, s58, 65536\n"
    "buffer_load_dwordx4 v[236:239], v251, s[32:35], s58 offen sc1\n"
    "s_lshl_b32 s58, s99, 10\n"
    "s_add_u32 s58, s58, 65536\n"
    "buffer_load_dwordx4 v[240:243], v251, s[32:35], s58 offen sc1\n"
    "s_waitcnt vmcnt(3)\n"
    "v_xor_b32 v60, s51, v229\n"
    "v_xor_b32_e64 v61, s51, v231\n"
    "s_waitcnt vmcnt(2)\n"
    "v_bitop3_b32 v60, v233, s51, v60 bitop3:0xbe\n"
    "v_bitop3_b32 v61, v235, s51, v61 bitop3:0xbe\n"
    "s_waitcnt vmcnt(1)\n"
    "v_bitop3_b32 v60, v237, s51, v60 bitop3:0xbe\n"
    "v_bitop3_b32 v61, v239, s51, v61 bitop3:0xbe\n"
    "s_waitcnt vmcnt(0)\n"
    "v_bitop3_b32 v60, v241, s51, v60 bitop3:0xbe\n"
    "v_bitop3_b32 v61, v243, s51, v61 bitop3:0xbe\n"
    "v_or_b32 v60, v60, v61\n"
    "v_cmp_ne_u32 vcc, 0, v60\n"
    "s_nop 0\n"
    "s_cbranch_vccz .Lk_done_77\n"
    "s_add_u32 s57, s57, 1\n"
    "s_sleep 4\n"
    "s_cmp_lt_u32 s57, 65536\n"
    "s_cbranch_scc1 .Lk_spin_76\n"
    ".Lk_done_77:\n"
    "s_nop 1\n"
    "s_branch .Lk_ret_4\n"
    ".Lk_ool_5:\n"
    "s_nop 1\n"
    "s_sleep 10\n"
    "s_mov_b32 s57, 0\n"
    "s_waitcnt vmcnt(0) lgkmcnt(0)\n"
    ".Lk_spin_78:\n"
    "buffer_load_dwordx3 v[16:18], v15, s[32:35], 0 offen offset:1024 sc1\n"
    "s_waitcnt vmcnt(0)\n"
    "v_xor_b32_e64 v16, s51, v16\n"
    "v_bitop3_b32 v16, v18, s51, v16 bitop3:0xbe\n"
    "v_cmp_ne_u32_e64 vcc, 0, v16\n"
    "s_nop 0\n"
    "s_cbranch_vccz .Lk_done_79\n"
    "s_add_u32 s57, s57, 1\n"
    "s_sleep 10\n"
    "s_cmp_lt_u32 s57, 65536\n"
    "s_cbranch_scc1 .Lk_spin_78\n"
    ".Lk_done_79:\n"
    "s_nop 1\n"
    "s_branch .Lk_ret_6\n"
    ".Lk_ool_9:\n"
    "s_nop 1\n"
    "s_sleep 10\n"
    "s_mov_b32 s57, 0\n"
    "s_waitcnt vmcnt(0) lgkmcnt(0)\n"
    ".Lk_spin_80:\n"
    "buffer_load_dwordx3 v[16:18], v15, s[32:35], 0 offen offset:1024 sc1\n"
    "s_waitcnt vmcnt(0)\n"
    "v_xor_b32_e64 v16, s51, v16\n"
    "v_bitop3_b32 v16, v18, s51, v16 bitop3:0xbe\n"
    "v_cmp_ne_u32_e64 vcc, 0, v16\n"
    "s_nop 0\n"
    "s_cbranch_vccz .Lk_done_81\n"
    "s_add_u32 s57, s57, 1\n"
    "s_sleep 10\n"
    "s_cmp_lt_u32 s57, 65536\n"
    "s_cbranch_scc1 .Lk_spin_80\n"
    ".Lk_done_81:\n"
    "s_nop 1\n"
    "s_branch .Lk_ret_10\n"
    ".Lk_x1_0:\n"
    "buffer_load_dwordx4 v[20:23], v251, s[32:35], s64 offen offset:0 sc1\n"
    "s_sub_u32 s6, s6, 1\n"
    "s_cbranch_scc1 .Lk_x1_ret\n"
    ".Lk_x1_1:\n"
    "buffer_load_dwordx4 v[24:27], v251, s[32:35], s64 offen offset:1024 sc1\n"
    "s_sub_u32 s6, s6, 1\n"
    "s_cbranch_scc1 .Lk_x1_ret\n"
    ".Lk_x1_2:\n"
    "buffer_load_dwordx4 v[28:31], v251, s[32:35], s64 offen offset:2048 sc1\n"
    "s_sub_u32 s6, s6, 1\n"
    "s_cbranch_scc1 .Lk_x1_ret\n"
    ".Lk_x1_3:\n"
    "buffer_load_dwordx4 v[32:35], v251, s[32:35], s64 offen offset:3072 sc1\n"
    "s_sub_u32 s6, s6, 1\n"
    "s_cbranch_scc1 .Lk_x1_ret\n"
    ".Lk_x1_4:\n"
    "buffer_load_dwordx4 v[36:39], v251, s[32:35], s65 offen offset:0 sc1\n"
    "s_sub_u32 s6, s6, 1\n"
    "s_cbranch_scc1 .Lk_x1_ret\n"
    ".Lk_x1_5:\n"
    "buffer_load_dwordx4 v[40:43], v251, s[32:35], s65 offen offset:1024 sc1\n"
    "s_sub_u32 s6, s6, 1\n"
    "s_cbranch_scc1 .Lk_x1_ret\n"
    ".Lk_x1_6:\n"
    "buffer_load_dwordx4 v[44:47], v251, s[32:35], s65 offen offset:2048 sc1\n"
    "s_sub_u32 s6, s6, 1\n"
    "s_cbranch_scc1 .Lk_x1_ret\n"
    ".Lk_x1_7:\n"
    "buffer_load_dwordx4 v[48:51], v251, s[32:35], s65 offen offset:3072 sc1\n"
    "s_sub_u32 s6, s6, 1\n"
    "s_cbranch_scc1 .Lk_x1_ret\n"
    ".Lk_x1_8:\n"
    "buffer_load_dwordx4 v[52:55], v251, s[32:35], s66 offen offset:0 sc1\n"
    "s_sub_u32 s6, s6, 1\n"
    "s_cbranch_scc1 .Lk_x1_ret\n"
    ".Lk_x1_9:\n"
    "buffer_load_dwordx4 v[56:59], v251, s[32:35], s66 offen offset:1024 sc1\n"
    "s_sub_u32 s6, s6, 1\n"
    "s_cbranch_scc1 .Lk_x1_ret\n"
    ".Lk_x1_10:\n"
    "buffer_load_dwordx4 v[60:63], v251, s[32:35], s66 offen offset:2048 sc1\n"
    "s_sub_u32 s6, s6, 1\n"
    "s_cbranch_scc1 .Lk_x1_ret\n"
    ".Lk_x1_11:\n"
    "buffer_load_dwordx4 v[64:67], v251, s[32:35], s66 offen offset:3072 sc1\n"
    "s_sub_u32 s6, s6, 1\n"
    "s_cbranch_scc1 .Lk_x1_ret\n"
    ".Lk_x1_12:\n"
    "buffer_load_dwordx4 v[68:71], v251, s[32:35], s67 offen offset:0 sc1\n"
    "s_sub_u32 s6, s6, 1\n"
    "s_cbranch_scc1 .Lk_x1_ret\n"
    ".Lk_x1_13:\n"
    "buffer_load_dwordx4 v[72:75], v251, s[32:35], s67 offen offset:1024 sc1\n"
    "s_sub_u32 s6, s6, 1\n"
    "s_cbranch_scc1 .Lk_x1_ret\n"
    ".Lk_x1_14:\n"
    "buffer_load_dwordx4 v[76:79], v251, s[32:35], s67 offen offset:2048 sc1\n"
    "s_sub_u32 s6, s6, 1\n"
    "s_cbranch_scc1 .Lk_x1_ret\n"
    ".Lk_x1_15:\n"
    "buffer_load_dwordx4 v[80:83], v251, s[32:35], s67 offen offset:3072 sc1\n"
    "s_sub_u32 s6, s6, 1\n"
    "s_cbranch_scc1 .Lk_x1_ret\n"
    ".Lk_x1_16:\n"
    "buffer_load_dwordx4 v[84:87], v251, s[32:35], s68 offen offset:0 sc1\n"
    "s_sub_u32 s6, s6, 1\n"
    "s_cbranch_scc1 .Lk_x1_ret\n"
    ".Lk_x1_17:\n"
    "buffer_load_dwordx4 v[88:91], v251, s[32:35], s68 offen offset:1024 sc1\n"
    "s_sub_u32 s6, s6, 1\n"
    "s_cbranch_scc1 .Lk_x1_ret\n"
    ".Lk_x1_18:\n"
    "buffer_load_dwordx4 v[92:95], v251, s[32:35], s68 offen offset:2048 sc1\n"
    "s_sub_u32 s6, s6, 1\n"
    "s_cbranch_scc1 .Lk_x1_ret\n"
    ".Lk_x1_19:\n"
    "buffer_load_dwordx4 v[96:99], v251, s[32:35], s68 offen offset:3072 sc1\n"
    "s_sub_u32 s6, s6, 1\n"
    "s_cbranch_scc1 .Lk_x1_ret\n"
    ".Lk_x1_20:\n"
    "buffer_load_dwordx4 v[176:179], v172, s[32:35], 0 offen sc1\n"
    "s_sub_u32 s6, s6, 1\n"
    "s_cbranch_scc1 .Lk_x1_ret\n"
    ".Lk_x1_21:\n"
    "buffer_load_dwordx4 v[100:103], v251, s[32:35], s69 offen offset:0 sc1\n"
    "s_sub_u32 s6, s6, 1\n"
    "s_cbranch_scc1 .Lk_x1_ret\n"
    ".Lk_x1_22:\n"
    "buffer_load_dwordx4 v[104:107], v251, s[32:35], s69 offen offset:1024 sc1\n"
    "s_sub_u32 s6, s6, 1\n"
    "s_cbranch_scc1 .Lk_x1_ret\n"
    ".Lk_x1_23:\n"
    "buffer_load_dwordx4 v[108:111], v251, s[32:35], s69 offen offset:2048 sc1\n"
    "s_sub_u32 s6, s6, 1\n"
    "s_cbranch_scc1 .Lk_x1_ret\n"
    ".Lk_x1_24:\n"
    "buffer_load_dwordx4 v[112:115], v251, s[32:35], s69 offen offset:3072 sc1\n"
    "s_sub_u32 s6, s6, 1\n"
    "s_cbranch_scc1 .Lk_x1_ret\n"
    ".Lk_x1_25:\n"
    "buffer_load_dwordx4 v[116:119], v251, s[32:35], s70 offen offset:0 sc1\n"
    "s_sub_u32 s6, s6, 1\n"
    "s_cbranch_scc1 .Lk_x1_ret\n"
    ".Lk_x1_26:\n"
    "buffer_load_dwordx4 v[120:123], v251, s[32:35], s70 offen offset:1024 sc1\n"
    "s_sub_u32 s6, s6, 1\n"
    "s_cbranch_scc1 .Lk_x1_ret\n"
    ".Lk_x1_27:\n"
    "buffer_load_dwordx4 v[124:127], v251, s[32:35], s70 offen offset:2048 sc1\n"
    "s_sub_u32 s6, s6, 1\n"
    "s_cbranch_scc1 .Lk_x1_ret\n"
    ".Lk_x1_28:\n"
    "buffer_load_dwordx4 v[128:131], v251, s[32:35], s70 offen offset:3072 sc1\n"
    "s_sub_u32 s6, s6, 1\n"
    "s_cbranch_scc1 .Lk_x1_ret\n"
    ".Lk_x1_29:\n"
    "buffer_load_dwordx4 v[132:135], v251, s[32:35], s71 offen offset:0 sc1\n"
    "s_sub_u32 s6, s6, 1\n"
    "s_cbranch_scc1 .Lk_x1_ret\n"
    ".Lk_x1_30:\n"
    "buffer_load_dwordx4 v[136:139], v251, s[32:35], s71 offen offset:1024 sc1\n"
    "s_sub_u32 s6, s6, 1\n"
    "s_cbranch_scc1 .Lk_x1_ret\n"
    ".Lk_x1_31:\n"
    "buffer_load_dwordx4 v[140:143], v251, s[32:35], s71 offen offset:2048 sc1\n"
    "s_sub_u32 s6, s6, 1\n"
    "s_cbranch_scc1 .Lk_x1_ret\n"
    ".Lk_x1_32:\n"
    "buffer_load_dwordx4 v[144:147], v251, s[32:35], s71 offen offset:3072 sc1\n"
    "s_sub_u32 s6, s6, 1\n"
    "s_cbranch_scc1 .Lk_x1_ret\n"
    ".Lk_x1_33:\n"
    "buffer_load_dwordx4 v[148:151], v251, s[32:35], s72 offen offset:0 sc1\n"
    "s_sub_u32 s6, s6, 1\n"
    "s_cbranch_scc1 .Lk_x1_ret\n"
    ".Lk_x1_34:\n"
    "buffer_load_dwordx4 v[152:155], v251, s[32:35], s72 offen offset:1024 sc1\n"
    "s_sub_u32 s6, s6, 1\n"
    "s_cbranch_scc1 .Lk_x1_ret\n"
    ".Lk_x1_35:\n"
    "buffer_load_dwordx4 v[156:159], v251, s[32:35], s72 offen offset:2048 sc1\n"
    "s_sub_u32 s6, s6, 1\n"
    "s_cbranch_scc1 .Lk_x1_ret\n"
    ".Lk_x1_36:\n"
    "buffer_load_dwordx4 v[160:163], v251, s[32:35], s72 offen offset:3072 sc1\n"
    "s_sub_u32 s6, s6, 1\n"
    "s_cbranch_scc1 .Lk_x1_ret\n"
    ".Lk_x1_37:\n"
    "buffer_load_dwordx4 v[164:167], v251, s[32:35], s73 offen offset:0 sc1\n"
    "s_sub_u32 s6, s6, 1\n"
    "s_cbranch_scc1 .Lk_x1_ret\n"
    ".Lk_x1_38:\n"
    "buffer_load_dwordx4 v[168:171], v251, s[32:35], s73 offen offset:1024 sc1\n"
    "s_sub_u32 s6, s6, 1\n"
    "s_cbranch_scc1 .Lk_x1_ret\n"
    ".Lk_x1_39:\n"
    "buffer_load_dwordx4 v[172:175], v251, s[32:35], s73 offen offset:2048 sc1\n"
    "s_sub_u32 s6, s6, 1\n"
    "s_cbranch_scc1 .Lk_x1_ret\n"
    ".Lk_x1_40:\n"
    "buffer_load_dwordx4 v[176:179], v251, s[32:35], s73 offen offset:3072 sc1\n"
    "s_sub_u32 s6, s6, 1\n"
    "s_cbranch_scc1 .Lk_x1_ret\n"
    ".Lk_x1_41:\n"
    "s_add_u32 s11, s62, 28672\n"
    "buffer_load_dwordx4 a[112:115], v251, s[32:35], s11 offen offset:0 sc1\n"
    "s_sub_u32 s6, s6, 1\n"
    "s_cbranch_scc1 .Lk_x1_ret\n"
    ".Lk_x1_42:\n"
    "s_add_u32 s11, s62, 28672\n"
    "buffer_load_dwordx4 a[116:119], v251, s[32:35], s11 offen offset:1024 sc1\n"
    "s_sub_u32 s6, s6, 1\n"
    "s_cbranch_scc1 .Lk_x1_ret\n"
    ".Lk_x1_43:\n"
    "s_add_u32 s11, s62, 28672\n"
    "buffer_load_dwordx4 a[120:123], v251, s[32:35], s11 offen offset:2048 sc1\n"
    "s_sub_u32 s6, s6, 1\n"
    "s_cbranch_scc1 .Lk_x1_ret\n"
    ".Lk_x1_44:\n"
    "s_add_u32 s11, s62, 28672\n"
    "buffer_load_dwordx4 a[124:127], v251, s[32:35], s11 offen offset:3072 sc1\n"
    "s_sub_u32 s6, s6, 1\n"
    "s_cbranch_scc1 .Lk_x1_ret\n"
    ".Lk_x1_45:\n"
    "s_add_u32 s11, s62, 32768\n"
    "buffer_load_dwordx4 a[128:131], v251, s[32:35], s11 offen offset:0 sc1\n"
    "s_sub_u32 s6, s6, 1\n"
    "s_cbranch_scc1 .Lk_x1_ret\n"
    ".Lk_x1_46:\n"
    "s_add_u32 s11, s62, 32768\n"
    "buffer_load_dwordx4 a[132:135], v251, s[32:35], s11 offen offset:1024 sc1\n"
    "s_sub_u32 s6, s6, 1\n"
    "s_cbranch_scc1 .Lk_x1_ret\n"
    ".Lk_x1_47:\n"
    "s_add_u32 s11, s62, 32768\n"
    "buffer_load_dwordx4 a[136:139], v251, s[32:35], s11 offen offset:2048 sc1\n"
    "s_sub_u32 s6, s6, 1\n"
    "s_cbranch_scc1 .Lk_x1_ret\n"
    ".Lk_x1_48:\n"
    "s_add_u32 s11, s62, 32768\n"
    "buffer_load_dwordx4 a[140:143], v251, s[32:35], s11 offen offset:3072 sc1\n"
    "s_sub_u32 s6, s6, 1\n"
    "s_cbranch_scc1 .Lk_x1_ret\n"
    ".Lk_x1_49:\n"
    "s_add_u32 s11, s62, 36864\n"
    "buffer_load_dwordx4 a[144:147], v251, s[32:35], s11 offen offset:0 sc1\n"
    "s_sub_u32 s6, s6, 1\n"
    "s_cbranch_scc1 .Lk_x1_ret\n"
    ".Lk_x1_50:\n"
    "s_add_u32 s11, s62, 36864\n"
    "buffer_load_dwordx4 a[148:151], v251, s[32:35], s11 offen offset:1024 sc1\n"
    "s_sub_u32 s6, s6, 1\n"
    "s_cbranch_scc1 .Lk_x1_ret\n"
    ".Lk_x1_51:\n"
    "s_add_u32 s11, s62, 36864\n"
    "buffer_load_dwordx4 a[152:155], v251, s[32:35], s11 offen offset:2048 sc1\n"
    "s_sub_u32 s6, s6, 1\n"
    "s_cbranch_scc1 .Lk_x1_ret\n"
    ".Lk_x1_52:\n"
    "s_add_u32 s11, s62, 36864\n"
    "buffer_load_dwordx4 a[156:159], v251, s[32:35], s11 offen offset:3072 sc1\n"
    "s_sub_u32 s6, s6, 1\n"
    "s_cbranch_scc1 .Lk_x1_ret\n"
    ".Lk_x1_53:\n"
    "s_add_u32 s11, s62, 40960\n"
    "buffer_load_dwordx4 a[160:163], v251, s[32:35], s11 offen offset:0 sc1\n"
    "s_sub_u32 s6, s6, 1\n"
    "s_cbranch_scc1 .Lk_x1_ret\n"
    ".Lk_x1_54:\n"
    "s_add_u32 s11, s62, 40960\n"
    "buffer_load_dwordx4 a[164:167], v251, s[32:35], s11 offen offset:1024 sc1\n"
    "s_sub_u32 s6, s6, 1\n"
    "s_cbranch_scc1 .Lk_x1_ret\n"
    ".Lk_x1_55:\n"
    "s_add_u32 s11, s62, 40960\n"
    "buffer_load_dwordx4 a[168:171], v251, s[32:35], s11 offen offset:2048 sc1\n"
    "s_sub_u32 s6, s6, 1\n"
    "s_cbranch_scc1 .Lk_x1_ret\n"
    ".Lk_x1_56:\n"
    "s_add_u32 s11, s62, 40960\n"
    "buffer_load_dwordx4 a[172:175], v251, s[32:35], s11 offen offset:3072 sc1\n"
    "s_sub_u32 s6, s6, 1\n"
    "s_cbranch_scc1 .Lk_x1_ret\n"
    ".Lk_x1_57:\n"
    "s_add_u32 s11, s62, 45056\n"
    "buffer_load_dwordx4 a[176:179], v251, s[32:35], s11 offen offset:0 sc1\n"
    "s_sub_u32 s6, s6, 1\n"
    "s_cbranch_scc1 .Lk_x1_ret\n"
    ".Lk_x1_58:\n"
    "s_add_u32 s11, s62, 45056\n"
    "buffer_load_dwordx4 a[180:183], v251, s[32:35], s11 offen offset:1024 sc1\n"
    "s_sub_u32 s6, s6, 1\n"
    "s_cbranch_scc1 .Lk_x1_ret\n"
    ".Lk_x1_59:\n"
    "s_add_u32 s11, s62, 45056\n"
    "buffer_load_dwordx4 a[184:187], v251, s[32:35], s11 offen offset:2048 sc1\n"
    "s_sub_u32 s6, s6, 1\n"
    "s_cbranch_scc1 .Lk_x1_ret\n"
    ".Lk_x1_60:\n"
    "s_add_u32 s11, s62, 45056\n"
    "buffer_load_dwordx4 a[188:191], v251, s[32:35], s11 offen offset:3072 sc1\n"
    "s_sub_u32 s6, s6, 1\n"
    "s_cbranch_scc1 .Lk_x1_ret\n"
    ".Lk_x1_61:\n"
    "s_add_u32 s11, s62, 49152\n"
    "buffer_load_dwordx4 a[192:195], v251, s[32:35], s11 offen offset:0 sc1\n"
    "s_sub_u32 s6, s6, 1\n"
    "s_cbranch_scc1 .Lk_x1_ret\n"
    ".Lk_x1_62:\n"
    "s_add_u32 s11, s62, 49152\n"
    "buffer_load_dwordx4 a[196:199], v251, s[32:35], s11 offen offset:1024 sc1\n"
    "s_sub_u32 s6, s6, 1\n"
    "s_cbranch_scc1 .Lk_x1_ret\n"
    ".Lk_x1_63:\n"
    "s_add_u32 s11, s62, 49152\n"
    "buffer_load_dwordx4 a[200:203], v251, s[32:35], s11 offen offset:2048 sc1\n"
    "s_sub_u32 s6, s6, 1\n"
    "s_cbranch_scc1 .Lk_x1_ret\n"
    ".Lk_x1_64:\n"
    "s_add_u32 s11, s62, 49152\n"
    "buffer_load_dwordx4 a[204:207], v251, s[32:35], s11 offen offset:3072 sc1\n"
    "s_sub_u32 s6, s6, 1\n"
    "s_cbranch_scc1 .Lk_x1_ret\n"
    ".Lk_x1_65:\n"
    "s_add_u32 s11, s62, 53248\n"
    "buffer_load_dwordx4 a[208:211], v251, s[32:35], s11 offen offset:0 sc1\n"
    "s_sub_u32 s6, s6, 1\n"
    "s_cbranch_scc1 .Lk_x1_ret\n"
    ".Lk_x1_66:\n"
    "s_add_u32 s11, s62, 53248\n"
    "buffer_load_dwordx4 a[212:215], v251, s[32:35], s11 offen offset:1024 sc1\n"
    "s_sub_u32 s6, s6, 1\n"
    "s_cbranch_scc1 .Lk_x1_ret\n"
    ".Lk_x1_67:\n"
    "s_add_u32 s11, s62, 53248\n"
    "buffer_load_dwordx4 a[216:219], v251, s[32:35], s11 offen offset:2048 sc1\n"
    "s_sub_u32 s6, s6, 1\n"
    "s_cbranch_scc1 .Lk_x1_ret\n"
    ".Lk_x1_68:\n"
    "s_add_u32 s11, s62, 53248\n"
    "buffer_load_dwordx4 a[220:223], v251, s[32:35], s11 offen offset:3072 sc1\n"
    "s_sub_u32 s6, s6, 1\n"
    "s_cbranch_scc1 .Lk_x1_ret\n"
    ".Lk_x1_69:\n"
    "s_add_u32 s11, s62, 57344\n"
    "buffer_load_dwordx4 a[224:227], v251, s[32:35], s11 offen offset:0 sc1\n"
    "s_sub_u32 s6, s6, 1\n"
    "s_cbranch_scc1 .Lk_x1_ret\n"
    ".Lk_x1_70:\n"
    "s_add_u32 s11, s62, 57344\n"
    "buffer_load_dwordx4 a[228:231], v251, s[32:35], s11 offen offset:1024 sc1\n"
    "s_sub_u32 s6, s6, 1\n"
    "s_cbranch_scc1 .Lk_x1_ret\n"
    ".Lk_x1_71:\n"
    "s_add_u32 s11, s62, 57344\n"
    "buffer_load_dwordx4 a[232:235], v251, s[32:35], s11 offen offset:2048 sc1\n"
    "s_sub_u32 s6, s6, 1\n"
    "s_cbranch_scc1 .Lk_x1_ret\n"
    ".Lk_x1_72:\n"
    "s_add_u32 s11, s62, 57344\n"
    "buffer_load_dwordx4 a[236:239], v251, s[32:35], s11 offen offset:3072 sc1\n"
    "s_sub_u32 s6, s6, 1\n"
    "s_cbranch_scc1 .Lk_x1_ret\n"
    ".Lk_x1_73:\n"
    "s_add_u32 s11, s62, 61440\n"
    "buffer_load_dwordx4 a[240:243], v251, s[32:35], s11 offen offset:0 sc1\n"
    "s_sub_u32 s6, s6, 1\n"
    "s_cbranch_scc1 .Lk_x1_ret\n"
    ".Lk_x1_74:\n"
    "s_add_u32 s11, s62, 61440\n"
    "buffer_load_dwordx4 a[244:247], v251, s[32:35], s11 offen offset:1024 sc1\n"
    "s_sub_u32 s6, s6, 1\n"
    "s_cbranch_scc1 .Lk_x1_ret\n"
    ".Lk_x1_75:\n"
    "s_add_u32 s11, s62, 61440\n"
    "buffer_load_dwordx4 a[248:251], v251, s[32:35], s11 offen offset:2048 sc1\n"
    "s_sub_u32 s6, s6, 1\n"
    "s_cbranch_scc1 .Lk_x1_ret\n"
    ".Lk_x1_76:\n"
    "s_add_u32 s11, s62, 61440\n"
    "buffer_load_dwordx4 a[252:255], v251, s[32:35], s11 offen offset:3072 sc1\n"
    "s_sub_u32 s6, s6, 1\n"
    "s_cbranch_scc1 .Lk_x1_ret\n"
    ".Lk_x1_ret:\n"
    "s_setpc_b64 s[4:5]\n"
    ".Lk_ool_14:\n"
    "s_nop 1\n"
    "s_mov_b32 s57, 0\n"
    ".Lk_again_82:\n"
    "s_mov_b32 s6, 11\n"
    "s_call_b64 s[4:5], .Lk_x1_0\n"
    "s_waitcnt vmcnt(10)\n"
    "v_max3_u32 v250, v20, v22, v24\n"
    "v_max_u32 v250, v250, v26\n"
    "v_cmp_eq_u32 vcc, -1, v250\n"
    "s_nop 0\n"
    "s_cbranch_vccz .Lk_there_83\n"
    "s_add_u32 s57, s57, 1\n"
    "s_cmp_lt_u32 s57, 65536\n"
    "s_cbranch_scc1 .Lk_again_82\n"
    ".Lk_there_83:\n"
    "s_nop 1\n"
    "s_branch .Lk_ret_15\n"
    ".Lk_ool_16:\n"
    "s_nop 1\n"
    "s_mov_b32 s57, 0\n"
    ".Lk_again_84:\n"
    "s_mov_b32 s6, 10\n"
    "s_call_b64 s[4:5], .Lk_x1_2\n"
    "s_waitcnt vmcnt(9)\n"
    "v_max3_u32 v250, v28, v30, v32\n"
    "v_max_u32 v250, v250, v34\n"
    "v_cmp_eq_u32_e64 vcc, -1, v250\n"
    "s_nop 0\n"
    "s_cbranch_vccz .Lk_there_85\n"
    "s_add_u32 s57, s57, 1\n"
    "s_cmp_lt_u32 s57, 65536\n"
    "s_cbranch_scc1 .Lk_again_84\n"
    ".Lk_there_85:\n"
    "s_nop 1\n"
    "s_branch .Lk_ret_17\n"
    ".Lk_ool_18:\n"
    "s_nop 1\n"
    "s_mov_b32 s57, 0\n"
    ".Lk_again_86:\n"
    "s_mov_b32 s6, 10\n"
    "s_call_b64 s[4:5], .Lk_x1_4\n"
    "s_waitcnt vmcnt(9)\n"
    "v_max3_u32 v250, v36, v38, v40\n"
    "v_max_u32 v250, v250, v42\n"
    "v_cmp_eq_u32_e64 vcc, -1, v250\n"
    "s_nop 0\n"
    "s_cbranch_vccz .Lk_there_87\n"
    "s_add_u32 s57, s57, 1\n"
    "s_cmp_lt_u32 s57, 65536\n"
    "s_cbranch_scc1 .Lk_again_86\n"
    ".Lk_there_87:\n"
    "s_nop 1\n"
    "s_branch .Lk_ret_19\n"
    ".Lk_ool_20:\n"
    "s_nop 1\n"
    "s_mov_b32 s57, 0\n"
    ".Lk_again_88:\n"
    "s_mov_b32 s6, 11\n"
    "s_call_b64 s[4:5], .Lk_x1_6\n"
    "s_waitcnt vmcnt(10)\n"
    "v_max3_u32 v250, v44, v46, v48\n"
    "v_max_u32 v250, v250, v50\n"
    "v_cmp_eq_u32_e64 vcc, -1, v250\n"
    "s_nop 0\n"
    "s_cbranch_vccz .Lk_there_89\n"
    "s_add_u32 s57, s57, 1\n"
    "s_cmp_lt_u32 s57, 65536\n"
    "s_cbranch_scc1 .Lk_again_88\n"
    ".Lk_there_89:\n"
    "s_nop 1\n"
    "s_branch .Lk_ret_21\n"
    ".Lk_ool_22:\n"
    "s_nop 1\n"
    "s_mov_b32 s57, 0\n"
    ".Lk_again_90:\n"
    "s_mov_b32 s6, 11\n"
    "s_call_b64 s[4:5], .Lk_x1_8\n"
    "s_waitcnt vmcnt(8)\n"
    "v_max3_u32 v250, v52, v54, v56\n"
    "v_max3_u32 v250, v250, v58, v60\n"
    "v_max3_u32 v250, v250, v62, v64\n"
    "v_max_u32 v250, v250, v66\n"
    "v_cmp_eq_u32_e64 vcc, -1, v250\n"
    "s_nop 0\n"
    "s_cbranch_vccz .Lk_there_91\n"
    "s_add_u32 s57, s57, 1\n"
    "s_cmp_lt_u32 s57, 65536\n"
    "s_cbranch_scc1 .Lk_again_90\n"
    ".Lk_there_91:\n"
    "s_nop 1\n"
    "s_branch .Lk_ret_23\n"
    ".Lk_ool_24:\n"
    "s_nop 1\n"
    "s_mov_b32 s57, 0\n"
    ".Lk_again_92:\n"
    "s_mov_b32 s6, 12\n"
    "s_call_b64 s[4:5], .Lk_x1_12\n"
    "s_waitcnt vmcnt(9)\n"
    "v_max3_u32 v250, v68, v70, v72\n"
    "v_max3_u32 v250, v250, v74, v76\n"
    "v_max3_u32 v250, v250, v78, v80\n"
    "v_max_u32 v250, v250, v82\n"
    "v_cmp_eq_u32_e64 vcc, -1, v250\n"
    "s_nop 0\n"
    "s_cbranch_vccz .Lk_there_93\n"
    "s_add_u32 s57, s57, 1\n"
    "s_cmp_lt_u32 s57, 65536\n"
    "s_cbranch_scc1 .Lk_again_92\n"
    ".Lk_there_93:\n"
    "s_nop 1\n"
    "s_branch .Lk_ret_25\n"
    ".Lk_ool_26:\n"
    "s_nop 1\n"
    "s_mov_b32 s57, 0\n"
    ".Lk_again_94:\n"
    "s_mov_b32 s6, 13\n"
    "s_call_b64 s[4:5], .Lk_x1_16\n"
    "s_waitcnt vmcnt(10)\n"
    "v_max3_u32 v250, v84, v86, v88\n"
    "v_max3_u32 v250, v250, v90, v92\n"
    "v_max3_u32 v250, v250, v94, v96\n"
    "v_max_u32 v250, v250, v98\n"
    "v_cmp_eq_u32_e64 vcc, -1, v250\n"
    "s_nop 0\n"
    "s_cbranch_vccz .Lk_there_95\n"
    "s_add_u32 s57, s57, 1\n"
    "s_cmp_lt_u32 s57, 65536\n"
    "s_cbranch_scc1 .Lk_again_94\n"
    ".Lk_there_95:\n"
    "s_nop 1\n"
    "s_branch .Lk_ret_27\n"
    ".Lk_ool_28:\n"
    "s_nop 1\n"
    "s_mov_b32 s57, 0\n"
    "s_waitcnt vmcnt(0) lgkmcnt(0)\n"
    ".Lk_spin_96:\n"
    "buffer_load_dwordx4 v[176:179], v172, s[32:35], 0 offen sc1\n"
    "s_waitcnt vmcnt(0)\n"
    "v_xor_b32 v250, s51, v177\n"
    "v_bitop3_b32 v250, v179, s51, v250 bitop3:0xbe\n"
    "v_cmp_ne_u32_e64 vcc, 0, v250\n"
    "s_nop 0\n"
    "s_cbranch_vccz .Lk_done_97\n"
    "s_add_u32 s57, s57, 1\n"
    "s_sleep 4\n"
    "s_cmp_lt_u32 s57, 65536\n"
    "s_cbranch_scc1 .Lk_spin_96\n"
    ".Lk_done_97:\n"
    "s_nop 1\n"
    "s_branch .Lk_ret_29\n"
    ".Lk_ool_30:\n"
    "s_nop 1\n"
    "s_mov_b32 s57, 0\n"
    ".Lk_again_98:\n"
    "s_mov_b32 s6, 12\n"
    "s_call_b64 s[4:5], .Lk_x1_21\n"
    "s_waitcnt vmcnt(9)\n"
    "v_max3_u32 v250, v100, v102, v104\n"
    "v_max3_u32 v250, v250, v106, v108\n"
    "v_max3_u32 v250, v250, v110, v112\n"
    "v_max_u32 v250, v250, v114\n"
    "v_cmp_eq_u32_e64 vcc, -1, v250\n"
    "s_nop 0\n"
    "s_cbranch_vccz .Lk_there_99\n"
    "s_add_u32 s57, s57, 1\n"
    "s_cmp_lt_u32 s57, 65536\n"
    "s_cbranch_scc1 .Lk_again_98\n"
    ".Lk_there_99:\n"
    "s_nop 1\n"
    "s_branch .Lk_ret_31\n"
    ".Lk_ool_32:\n"
    "s_nop 1\n"
    "s_mov_b32 s57, 0\n"
    ".Lk_again_100:\n"
    "s_mov_b32 s6, 13\n"
    "s_call_b64 s[4:5], .Lk_x1_25\n"
    "s_waitcnt vmcnt(10)\n"
    "v_max3_u32 v250, v116, v118, v120\n"
    "v_max3_u32 v250, v250, v122, v124\n"
    "v_max3_u32 v250, v250, v126, v128\n"
    "v_max_u32 v250, v250, v130\n"
    "v_cmp_eq_u32_e64 vcc, -1, v250\n"
    "s_nop 0\n"
    "s_cbranch_vccz .Lk_there_101\n"
    "s_add_u32 s57, s57, 1\n"
    "s_cmp_lt_u32 s57, 65536\n"
    "s_cbranch_scc1 .Lk_again_100\n"
    ".Lk_there_101:\n"
    "s_nop 1\n"
    "s_branch .Lk_ret_33\n"
    ".Lk_ool_34:\n"
    "s_nop 1\n"
    "s_mov_b32 s57, 0\n"
    ".Lk_again_102:\n"
    "s_mov_b32 s6, 14\n"
    "s_call_b64 s[4:5], .Lk_x1_29\n"
    "s_waitcnt vmcnt(11)\n"
    "v_max3_u32 v250, v132, v134, v136\n"
    "v_max3_u32 v250, v250, v138, v140\n"
    "v_max3_u32 v250, v250, v142, v144\n"
    "v_max_u32 v250, v250, v146\n"
    "v_cmp_eq_u32_e64 vcc, -1, v250\n"
    "s_nop 0\n"
    "s_cbranch_vccz .Lk_there_103\n"
    "s_add_u32 s57, s57, 1\n"
    "s_cmp_lt_u32 s57, 65536\n"
    "s_cbranch_scc1 .Lk_again_102\n"
    ".Lk_there_103:\n"
    "s_nop 1\n"
    "s_branch .Lk_ret_35\n"
    ".Lk_ool_36:\n"
    "s_nop 1\n"
    "s_mov_b32 s57, 0\n"
    ".Lk_again_104:\n"
    "s_mov_b32 s6, 15\n"
    "s_call_b64 s[4:5], .Lk_x1_33\n"
    "s_waitcnt vmcnt(12)\n"
    "v_max3_u32 v250, v148, v150, v152\n"
    "v_max3_u32 v250, v250, v154, v156\n"
    "v_max3_u32 v250, v250, v158, v160\n"
    "v_max_u32 v250, v250, v162\n"
    "v_cmp_eq_u32_e64 vcc, -1, v250\n"
    "s_nop 0\n"
    "s_cbranch_vccz .Lk_there_105\n"
    "s_add_u32 s57, s57, 1\n"
    "s_cmp_lt_u32 s57, 65536\n"
    "s_cbranch_scc1 .Lk_again_104\n"
    ".Lk_there_105:\n"
    "s_nop 1\n"
    "s_branch .Lk_ret_37\n"
    ".Lk_ool_38:\n"
    "s_nop 1\n"
    "s_mov_b32 s57, 0\n"
    ".Lk_again_106:\n"
    "s_mov_b32 s6, 15\n"
    "s_call_b64 s[4:5], .Lk_x1_37\n"
    "s_waitcnt vmcnt(12)\n"
    "v_max3_u32 v250, v164, v166, v168\n"
    "v_max3_u32 v250, v250, v170, v172\n"
    "v_max3_u32 v250, v250, v174, v176\n"
    "v_max_u32 v250, v250, v178\n"
    "v_cmp_eq_u32_e64 vcc, -1, v250\n"
    "s_nop 0\n"
    "s_cbranch_vccz .Lk_there_107\n"
    "s_add_u32 s57, s57, 1\n"
    "s_cmp_lt_u32 s57, 65536\n"
    "s_cbranch_scc1 .Lk_again_106\n"
    ".Lk_there_107:\n"
    "s_nop 1\n"
    "s_branch .Lk_ret_39\n"
    ".Lk_x2c_0:\n"
    "v_xor_b32 v116, v20, v252\n"
    "v_xor_b32 v117, v21, v253\n"
    "v_xor_b32 v120, v22, v254\n"
    "v_xor_b32 v121, v23, v255\n"
    "v_xor_b32 v118, v24, v252\n"
    "v_xor_b32 v119, v25, v253\n"
    "v_xor_b32 v122, v26, v254\n"
    "v_xor_b32 v123, v27, v255\n"
    "v_xor_b32 v124, v28, v252\n"
    "v_xor_b32 v125, v29, v253\n"
    "v_xor_b32 v128, v30, v254\n"
    "v_xor_b32 v129, v31, v255\n"
    "v_xor_b32 v126, v32, v252\n"
    "v_xor_b32 v127, v33, v253\n"
    "v_xor_b32 v130, v34, v254\n"
    "v_xor_b32_e64 v131, v35, v255\n"
    "v_or3_b32 v250, v116, v117, v118\n"
    "v_or3_b32 v250, v119, v120, v250\n"
    "v_or3_b32 v250, v121, v122, v250\n"
    "v_or3_b32 v250, v123, v124, v250\n"
    "v_or3_b32 v250, v125, v126, v250\n"
    "v_or3_b32 v250, v127, v128, v250\n"
    "v_or3_b32 v250, v129, v130, v250\n"
    "v_bitop3_b32 v250, v131, v250, s56 bitop3:0xa8\n"
    "v_cmp_ne_u32 vcc, 0, v250\n"
    "s_setpc_b64 s[4:5]\n"
    ".Lk_x2c_1:\n"
    "v_xor_b32 v132, v36, v252\n"
    "v_xor_b32 v133, v37, v253\n"
    "v_xor_b32 v136, v38, v254\n"
    "v_xor_b32 v137, v39, v255\n"
    "v_xor_b32 v134, v40, v252\n"
    "v_xor_b32 v135, v41, v253\n"
    "v_xor_b32 v138, v42, v254\n"
    "v_xor_b32 v139, v43, v255\n"
    "v_xor_b32 v140, v44, v252\n"
    "v_xor_b32 v141, v45, v253\n"
    "v_xor_b32 v144, v46, v254\n"
    "v_xor_b32 v145, v47, v255\n"
    "v_xor_b32 v142, v48, v252\n"
    "v_xor_b32 v143, v49, v253\n"
    "v_xor_b32 v146, v50, v254\n"
    "v_xor_b32 v147, v51, v255\n"
    "v_or3_b32 v250, v132, v133, v134\n"
    "v_or3_b32 v250, v135, v136, v250\n"
    "v_or3_b32 v250, v137, v138, v250\n"
    "v_or3_b32 v250, v139, v140, v250\n"
    "v_or3_b32 v250, v141, v142, v250\n"
    "v_or3_b32 v250, v143, v144, v250\n"
    "v_or3_b32 v250, v145, v146, v250\n"
    "v_bitop3_b32 v250, v147, v250, s56 bitop3:0xa8\n"
    "v_cmp_ne_u32 vcc, 0, v250\n"
    "s_setpc_b64 s[4:5]\n"
    ".Lk_x2c_2:\n"
    "v_xor_b32 v116, v52, v252\n"
    "v_xor_b32 v117, v53, v253\n"
    "v_xor_b32 v120, v54, v254\n"
    "v_xor_b32 v121, v55, v255\n"
    "v_xor_b32 v118, v56, v252\n"
    "v_xor_b32 v119, v57, v253\n"
    "v_xor_b32 v122, v58, v254\n"
    "v_xor_b32 v123, v59, v255\n"
    "v_xor_b32 v124, v60, v252\n"
    "v_xor_b32 v125, v61, v253\n"
    "v_xor_b32 v128, v62, v254\n"
    "v_xor_b32 v129, v63, v255\n"
    "v_xor_b32 v126, v64, v252\n"
    "v_xor_b32 v127, v65, v253\n"
    "v_xor_b32 v130, v66, v254\n"
    "v_xor_b32 v131, v67, v255\n"
    "v_or3_b32 v250, v116, v117, v118\n"
    "v_or3_b32 v250, v119, v120, v250\n"
    "v_or3_b32 v250, v121, v122, v250\n"
    "v_or3_b32 v250, v123, v124, v250\n"
    "v_or3_b32 v250, v125, v126, v250\n"
    "v_or3_b32 v250, v127, v128, v250\n"
    "v_or3_b32 v250, v129, v130, v250\n"
    "v_bitop3_b32 v250, v131, v250, s56 bitop3:0xa8\n"
    "v_cmp_ne_u32 vcc, 0, v250\n"
    "s_setpc_b64 s[4:5]\n"
    ".Lk_x2c_3:\n"
    "v_xor_b32 v132, v68, v252\n"
    "v_xor_b32 v133, v69, v253\n"
    "v_xor_b32 v136, v70, v254\n"
    "v_xor_b32 v137, v71, v255\n"
    "v_xor_b32 v134, v72, v252\n"
    "v_xor_b32 v135, v73, v253\n"
    "v_xor_b32 v138, v74, v254\n"
    "v_xor_b32 v139, v75, v255\n"
    "v_xor_b32 v140, v76, v252\n"
    "v_xor_b32 v141, v77, v253\n"
    "v_xor_b32 v144, v78, v254\n"
    "v_xor_b32 v145, v79, v255\n"
    "v_xor_b32 v142, v80, v252\n"
    "v_xor_b32 v143, v81, v253\n"
    "v_xor_b32 v146, v82, v254\n"
    "v_xor_b32 v147, v83, v255\n"
    "v_or3_b32 v250, v132, v133, v134\n"
    "v_or3_b32 v250, v135, v136, v250\n"
    "v_or3_b32 v250, v137, v138, v250\n"
    "v_or3_b32 v250, v139, v140, v250\n"
    "v_or3_b32 v250, v141, v142, v250\n"
    "v_or3_b32 v250, v143, v144, v250\n"
    "v_or3_b32 v250, v145, v146, v250\n"
    "v_bitop3_b32 v250, v147, v250, s56 bitop3:0xa8\n"
    "v_cmp_ne_u32 vcc, 0, v250\n"
    "s_setpc_b64 s[4:5]\n"
    ".Lk_x2c_4:\n"
    "v_xor_b32 v116, v84, v252\n"
    "v_xor_b32 v117, v85, v253\n"
    "v_xor_b32 v120, v86, v254\n"
    "v_xor_b32 v121, v87, v255\n"
    "v_xor_b32 v118, v88, v252\n"
    "v_xor_b32 v119, v89, v253\n"
    "v_xor_b32 v122, v90, v254\n"
    "v_xor_b32 v123, v91, v255\n"
    "v_xor_b32 v124, v92, v252\n"
    "v_xor_b32 v125, v93, v253\n"
    "v_xor_b32 v128, v94, v254\n"
    "v_xor_b32 v129, v95, v255\n"
    "v_xor_b32 v126, v96, v252\n"
    "v_xor_b32 v127, v97, v253\n"
    "v_xor_b32 v130, v98, v254\n"
    "v_xor_b32 v131, v99, v255\n"
    "v_or3_b32 v250, v116, v117, v118\n"
    "v_or3_b32 v250, v119, v120, v250\n"
    "v_or3_b32 v250, v121, v122, v250\n"
    "v_or3_b32 v250, v123, v124, v250\n"
    "v_or3_b32 v250, v125, v126, v250\n"
    "v_or3_b32 v250, v127, v128, v250\n"
    "v_or3_b32 v250, v129, v130, v250\n"
    "v_bitop3_b32 v250, v131, v250, s56 bitop3:0xa8\n"
    "v_cmp_ne_u32 vcc, 0, v250\n"
    "s_setpc_b64 s[4:5]\n"
    ".Lk_x2c_5:\n"
    "v_xor_b32 v132, v100, v252\n"
    "v_xor_b32 v133, v101, v253\n"
    "v_xor_b32 v136, v102, v254\n"
    "v_xor_b32 v137, v103, v255\n"
    "v_xor_b32 v134, v104, v252\n"
    "v_xor_b32 v135, v105, v253\n"
    "v_xor_b32 v138, v106, v254\n"
    "v_xor_b32 v139, v107, v255\n"
    "v_xor_b32 v140, v108, v252\n"
    "v_xor_b32 v141, v109, v253\n"
    "v_xor_b32 v144, v110, v254\n"
    "v_xor_b32 v145, v111, v255\n"
    "v_xor_b32 v142, v112, v252\n"
    "v_xor_b32 v143, v113, v253\n"
    "v_xor_b32 v146, v114, v254\n"
    "v_xor_b32 v147, v115, v255\n"
    "v_or3_b32 v250, v132, v133, v134\n"
    "v_or3_b32 v250, v135, v136, v250\n"
    "v_or3_b32 v250, v137, v138, v250\n"
    "v_or3_b32 v250, v139, v140, v250\n"
    "v_or3_b32 v250, v141, v142, v250\n"
    "v_or3_b32 v250, v143, v144, v250\n"
    "v_or3_b32 v250, v145, v146, v250\n"
    "v_bitop3_b32 v250, v147, v250, s56 bitop3:0xa8\n"
    "v_cmp_ne_u32 vcc, 0, v250\n"
    "s_setpc_b64 s[4:5]\n"
    ".Lk_ool_43:\n"
    "s_nop 1\n"
    "s_mov_b32 s57, 0\n"
    ".Lk_again_108:\n"
    "s_mov_b32 s6, 11\n"
    "s_call_b64 s[4:5], .Lk_x2_0\n"
    "s_waitcnt vmcnt(8)\n"
    "s_call_b64 s[4:5], .Lk_x2c_0\n"
    "s_cbranch_vccz .Lk_there_109\n"
    "s_add_u32 s57, s57, 1\n"
    "s_cmp_lt_u32 s57, 65536\n"
    "s_cbranch_scc1 .Lk_again_108\n"
    ".Lk_there_109:\n"
    "s_waitcnt vmcnt(0)\n"
    "s_nop 1\n"
    "s_branch .Lk_ret_44\n"
    ".Lk_x2_0:\n"
    "buffer_load_dwordx4 v[20:23], v251, s[36:39], s99 offen offset:0 sc1\n"
    "s_sub_u32 s6, s6, 1\n"
    "s_cbranch_scc1 .Lk_x2_ret\n"
    ".Lk_x2_1:\n"
    "buffer_load_dwordx4 v[24:27], v251, s[36:39], s99 offen offset:1024 sc1\n"
    "s_sub_u32 s6, s6, 1\n"
    "s_cbranch_scc1 .Lk_x2_ret\n"
    ".Lk_x2_2:\n"
    "buffer_load_dwordx4 v[28:31], v251, s[44:47], s99 offen offset:0 sc1\n"
    "s_sub_u32 s6, s6, 1\n"
    "s_cbranch_scc1 .Lk_x2_ret\n"
    ".Lk_x2_3:\n"
    "buffer_load_dwordx4 v[32:35], v251, s[44:47], s99 offen offset:1024 sc1\n"
    "s_sub_u32 s6, s6, 1\n"
    "s_cbranch_scc1 .Lk_x2_ret\n"
    ".Lk_x2_4:\n"
    "buffer_load_dwordx4 v[36:39], v251, s[36:39], s22 offen offset:2048 sc1\n"
    "s_sub_u32 s6, s6, 1\n"
    "s_cbranch_scc1 .Lk_x2_ret\n"
    ".Lk_x2_5:\n"
    "buffer_load_dwordx4 v[40:43], v251, s[36:39], s22 offen offset:3072 sc1\n"
    "s_sub_u32 s6, s6, 1\n"
    "s_cbranch_scc1 .Lk_x2_ret\n"
    ".Lk_x2_6:\n"
    "buffer_load_dwordx4 v[44:47], v251, s[44:47], s22 offen offset:2048 sc1\n"
    "s_sub_u32 s6, s6, 1\n"
    "s_cbranch_scc1 .Lk_x2_ret\n"
    ".Lk_x2_7:\n"
    "buffer_load_dwordx4 v[48:51], v251, s[44:47], s22 offen offset:3072 sc1\n"
    "s_sub_u32 s6, s6, 1\n"
    "s_cbranch_scc1 .Lk_x2_ret\n"
    ".Lk_x2_8:\n"
    "buffer_load_dwordx4 v[52:55], v251, s[36:39], s23 offen offset:0 sc1\n"
    "s_sub_u32 s6, s6, 1\n"
    "s_cbranch_scc1 .Lk_x2_ret\n"
    ".Lk_x2_9:\n"
    "buffer_load_dwordx4 v[56:59], v251, s[36:39], s23 offen offset:1024 sc1\n"
    "s_sub_u32 s6, s6, 1\n"
    "s_cbranch_scc1 .Lk_x2_ret\n"
    ".Lk_x2_10:\n"
    "buffer_load_dwordx4 v[60:63], v251, s[36:39], s23 offen offset:2048 sc1\n"
    "s_sub_u32 s6, s6, 1\n"
    "s_cbranch_scc1 .Lk_x2_ret\n"
    ".Lk_x2_11:\n"
    "buffer_load_dwordx4 v[64:67], v251, s[36:39], s23 offen offset:3072 sc1\n"
    "s_sub_u32 s6, s6, 1\n"
    "s_cbranch_scc1 .Lk_x2_ret\n"
    ".Lk_x2_12:\n"
    "buffer_load_dwordx4 v[68:71], v251, s[44:47], s23 offen offset:0 sc1\n"
    "s_sub_u32 s6, s6, 1\n"
    "s_cbranch_scc1 .Lk_x2_ret\n"
    ".Lk_x2_13:\n"
    "buffer_load_dwordx4 v[72:75], v251, s[44:47], s23 offen offset:1024 sc1\n"
    "s_sub_u32 s6, s6, 1\n"
    "s_cbranch_scc1 .Lk_x2_ret\n"
    ".Lk_x2_14:\n"
    "buffer_load_dwordx4 v[76:79], v251, s[44:47], s23 offen offset:2048 sc1\n"
    "s_sub_u32 s6, s6, 1\n"
    "s_cbranch_scc1 .Lk_x2_ret\n"
    ".Lk_x2_15:\n"
    "buffer_load_dwordx4 v[80:83], v251, s[44:47], s23 offen offset:3072 sc1\n"
    "s_sub_u32 s6, s6, 1\n"
    "s_cbranch_scc1 .Lk_x2_ret\n"
    ".Lk_x2_16:\n"
    "buffer_load_dwordx4 v[84:87], v251, s[36:39], s24 offen offset:0 sc1\n"
    "s_sub_u32 s6, s6, 1\n"
    "s_cbranch_scc1 .Lk_x2_ret\n"
    ".Lk_x2_17:\n"
    "buffer_load_dwordx4 v[88:91], v251, s[36:39], s24 offen offset:1024 sc1\n"
    "s_sub_u32 s6, s6, 1\n"
    "s_cbranch_scc1 .Lk_x2_ret\n"
    ".Lk_x2_18:\n"
    "buffer_load_dwordx4 v[92:95], v251, s[36:39], s24 offen offset:2048 sc1\n"
    "s_sub_u32 s6, s6, 1\n"
    "s_cbranch_scc1 .Lk_x2_ret\n"
    ".Lk_x2_19:\n"
    "buffer_load_dwordx4 v[96:99], v251, s[36:39], s24 offen offset:3072 sc1\n"
    "s_sub_u32 s6, s6, 1\n"
    "s_cbranch_scc1 .Lk_x2_ret\n"
    ".Lk_x2_20:\n"
    "buffer_load_dwordx4 v[100:103], v251, s[44:47], s24 offen offset:0 sc1\n"
    "s_sub_u32 s6, s6, 1\n"
    "s_cbranch_scc1 .Lk_x2_ret\n"
    ".Lk_x2_21:\n"
    "buffer_load_dwordx4 v[104:107], v251, s[44:47], s24 offen offset:1024 sc1\n"
    "s_sub_u32 s6, s6, 1\n"
    "s_cbranch_scc1 .Lk_x2_ret\n"
    ".Lk_x2_22:\n"
    "buffer_load_dwordx4 v[108:111], v251, s[44:47], s24 offen offset:2048 sc1\n"
    "s_sub_u32 s6, s6, 1\n"
    "s_cbranch_scc1 .Lk_x2_ret\n"
    ".Lk_x2_23:\n"
    "buffer_load_dwordx4 v[112:115], v251, s[44:47], s24 offen offset:3072 sc1\n"
    "s_sub_u32 s6, s6, 1\n"
    "s_cbranch_scc1 .Lk_x2_ret\n"
    ".Lk_x2_24:\n"
    "buffer_load_dwordx4 v[20:23], v251, s[36:39], s25 offen offset:0 sc1\n"
    "s_sub_u32 s6, s6, 1\n"
    "s_cbranch_scc1 .Lk_x2_ret\n"
    ".Lk_x2_25:\n"
    "buffer_load_dwordx4 v[24:27], v251, s[36:39], s25 offen offset:1024 sc1\n"
    "s_sub_u32 s6, s6, 1\n"
    "s_cbranch_scc1 .Lk_x2_ret\n"
    ".Lk_x2_26:\n"
    "buffer_load_dwordx4 v[28:31], v251, s[36:39], s25 offen offset:2048 sc1\n"
    "s_sub_u32 s6, s6, 1\n"
    "s_cbranch_scc1 .Lk_x2_ret\n"
    ".Lk_x2_27:\n"
    "buffer_load_dwordx4 v[32:35], v251, s[36:39], s25 offen offset:3072 sc1\n"
    "s_sub_u32 s6, s6, 1\n"
    "s_cbranch_scc1 .Lk_x2_ret\n"
    ".Lk_x2_28:\n"
    "s_add_u32 s11, s62, 65536\n"
    "buffer_load_dwordx4 a[0:3], v251, s[32:35], s11 offen offset:0 sc1\n"
    "s_sub_u32 s6, s6, 1\n"
    "s_cbranch_scc1 .Lk_x2_ret\n"
    ".Lk_x2_29:\n"
    "buffer_load_dwordx4 v[36:39], v251, s[44:47], s25 offen offset:0 sc1\n"
    "s_sub_u32 s6, s6, 1\n"
    "s_cbranch_scc1 .Lk_x2_ret\n"
    ".Lk_x2_30:\n"
    "buffer_load_dwordx4 v[40:43], v251, s[44:47], s25 offen offset:1024 sc1\n"
    "s_sub_u32 s6, s6, 1\n"
    "s_cbranch_scc1 .Lk_x2_ret\n"
    ".Lk_x2_31:\n"
    "buffer_load_dwordx4 v[44:47], v251, s[44:47], s25 offen offset:2048 sc1\n"
    "s_sub_u32 s6, s6, 1\n"
    "s_cbranch_scc1 .Lk_x2_ret\n"
    ".Lk_x2_32:\n"
    "buffer_load_dwordx4 v[48:51], v251, s[44:47], s25 offen offset:3072 sc1\n"
    "s_sub_u32 s6, s6, 1\n"
    "s_cbranch_scc1 .Lk_x2_ret\n"
    ".Lk_x2_33:\n"
    "s_add_u32 s11, s62, 65536\n"
    "buffer_load_dwordx4 a[4:7], v251, s[32:35], s11 offen offset:1024 sc1\n"
    "s_sub_u32 s6, s6, 1\n"
    "s_cbranch_scc1 .Lk_x2_ret\n"
    ".Lk_x2_34:\n"
    "buffer_load_dwordx4 v[52:55], v251, s[36:39], s30 offen offset:0 sc1\n"
    "s_sub_u32 s6, s6, 1\n"
    "s_cbranch_scc1 .Lk_x2_ret\n"
    ".Lk_x2_35:\n"
    "buffer_load_dwordx4 v[56:59], v251, s[36:39], s30 offen offset:1024 sc1\n"
    "s_sub_u32 s6, s6, 1\n"
    "s_cbranch_scc1 .Lk_x2_ret\n"
    ".Lk_x2_36:\n"
    "buffer_load_dwordx4 v[60:63], v251, s[36:39], s30 offen offset:2048 sc1\n"
    "s_sub_u32 s6, s6, 1\n"
    "s_cbranch_scc1 .Lk_x2_ret\n"
    ".Lk_x2_37:\n"
    "buffer_load_dwordx4 v[64:67], v251, s[36:39], s30 offen offset:3072 sc1\n"
    "s_sub_u32 s6, s6, 1\n"
    "s_cbranch_scc1 .Lk_x2_ret\n"
    ".Lk_x2_38:\n"
    "s_add_u32 s11, s62, 65536\n"
    "buffer_load_dwordx4 a[8:11], v251, s[32:35], s11 offen offset:2048 sc1\n"
    "s_sub_u32 s6, s6, 1\n"
    "s_cbranch_scc1 .Lk_x2_ret\n"
    ".Lk_x2_39:\n"
    "buffer_load_dwordx4 v[68:71], v251, s[44:47], s30 offen offset:0 sc1\n"
    "s_sub_u32 s6, s6, 1\n"
    "s_cbranch_scc1 .Lk_x2_ret\n"
    ".Lk_x2_40:\n"
    "buffer_load_dwordx4 v[72:75], v251, s[44:47], s30 offen offset:1024 sc1\n"
    "s_sub_u32 s6, s6, 1\n"
    "s_cbranch_scc1 .Lk_x2_ret\n"
    ".Lk_x2_41:\n"
    "buffer_load_dwordx4 v[76:79], v251, s[44:47], s30 offen offset:2048 sc1\n"
    "s_sub_u32 s6, s6, 1\n"
    "s_cbranch_scc1 .Lk_x2_ret\n"
    ".Lk_x2_42:\n"
    "buffer_load_dwordx4 v[80:83], v251, s[44:47], s30 offen offset:3072 sc1\n"
    "s_sub_u32 s6, s6, 1\n"
    "s_cbranch_scc1 .Lk_x2_ret\n"
    ".Lk_x2_43:\n"
    "s_add_u32 s11, s62, 65536\n"
    "buffer_load_dwordx4 a[12:15], v251, s[32:35], s11 offen offset:3072 sc1\n"
    "s_sub_u32 s6, s6, 1\n"
    "s_cbranch_scc1 .Lk_x2_ret\n"
    ".Lk_x2_44:\n"
    "buffer_load_dwordx4 v[84:87], v251, s[36:39], s31 offen offset:0 sc1\n"
    "s_sub_u32 s6, s6, 1\n"
    "s_cbranch_scc1 .Lk_x2_ret\n"
    ".Lk_x2_45:\n"
    "buffer_load_dwordx4 v[88:91], v251, s[36:39], s31 offen offset:1024 sc1\n"
    "s_sub_u32 s6, s6, 1\n"
    "s_cbranch_scc1 .Lk_x2_ret\n"
    ".Lk_x2_46:\n"
    "buffer_load_dwordx4 v[92:95], v251, s[36:39], s31 offen offset:2048 sc1\n"
    "s_sub_u32 s6, s6, 1\n"
    "s_cbranch_scc1 .Lk_x2_ret\n"
    ".Lk_x2_47:\n"
    "buffer_load_dwordx4 v[96:99], v251, s[36:39], s31 offen offset:3072 sc1\n"
    "s_sub_u32 s6, s6, 1\n"
    "s_cbranch_scc1 .Lk_x2_ret\n"
    ".Lk_x2_48:\n"
    "s_add_u32 s11, s62, 69632\n"
    "buffer_load_dwordx4 a[16:19], v251, s[32:35], s11 offen offset:0 sc1\n"
    "s_sub_u32 s6, s6, 1\n"
    "s_cbranch_scc1 .Lk_x2_ret\n"
    ".Lk_x2_49:\n"
    "buffer_load_dwordx4 v[100:103], v251, s[44:47], s31 offen offset:0 sc1\n"
    "s_sub_u32 s6, s6, 1\n"
    "s_cbranch_scc1 .Lk_x2_ret\n"
    ".Lk_x2_50:\n"
    "buffer_load_dwordx4 v[104:107], v251, s[44:47], s31 offen offset:1024 sc1\n"
    "s_sub_u32 s6, s6, 1\n"
    "s_cbranch_scc1 .Lk_x2_ret\n"
    ".Lk_x2_51:\n"
    "buffer_load_dwordx4 v[108:111], v251, s[44:47], s31 offen offset:2048 sc1\n"
    "s_sub_u32 s6, s6, 1\n"
    "s_cbranch_scc1 .Lk_x2_ret\n"
    ".Lk_x2_52:\n"
    "buffer_load_dwordx4 v[112:115], v251, s[44:47], s31 offen offset:3072 sc1\n"
    "s_sub_u32 s6, s6, 1\n"
    "s_cbranch_scc1 .Lk_x2_ret\n"
    ".Lk_x2_53:\n"
    "s_add_u32 s11, s62, 69632\n"
    "buffer_load_dwordx4 a[20:23], v251, s[32:35], s11 offen offset:1024 sc1\n"
    "s_sub_u32 s6, s6, 1\n"
    "s_cbranch_scc1 .Lk_x2_ret\n"
    ".Lk_x2_54:\n"
    "buffer_load_dwordx4 v[20:23], v251, s[36:39], s98 offen offset:0 sc1\n"
    "s_sub_u32 s6, s6, 1\n"
    "s_cbranch_scc1 .Lk_x2_ret\n"
    ".Lk_x2_55:\n"
    "buffer_load_dwordx4 v[24:27], v251, s[36:39], s98 offen offset:1024 sc1\n"
    "s_sub_u32 s6, s6, 1\n"
    "s_cbranch_scc1 .Lk_x2_ret\n"
    ".Lk_x2_56:\n"
    "buffer_load_dwordx4 v[28:31], v251, s[36:39], s98 offen offset:2048 sc1\n"
    "s_sub_u32 s6, s6, 1\n"
    "s_cbranch_scc1 .Lk_x2_ret\n"
    ".Lk_x2_57:\n"
    "buffer_load_dwordx4 v[32:35], v251, s[36:39], s98 offen offset:3072 sc1\n"
    "s_sub_u32 s6, s6, 1\n"
    "s_cbranch_scc1 .Lk_x2_ret\n"
    ".Lk_x2_58:\n"
    "buffer_load_dwordx4 v[36:39], v251, s[44:47], s98 offen offset:0 sc1\n"
    "s_sub_u32 s6, s6, 1\n"
    "s_cbranch_scc1 .Lk_x2_ret\n"
    ".Lk_x2_59:\n"
    "buffer_load_dwordx4 v[40:43], v251, s[44:47], s98 offen offset:1024 sc1\n"
    "s_sub_u32 s6, s6, 1\n"
    "s_cbranch_scc1 .Lk_x2_ret\n"
    ".Lk_x2_60:\n"
    "buffer_load_dwordx4 v[44:47], v251, s[44:47], s98 offen offset:2048 sc1\n"
    "s_sub_u32 s6, s6, 1\n"
    "s_cbranch_scc1 .Lk_x2_ret\n"
    ".Lk_x2_61:\n"
    "buffer_load_dwordx4 v[48:51], v251, s[44:47], s98 offen offset:3072 sc1\n"
    "s_sub_u32 s6, s6, 1\n"
    "s_cbranch_scc1 .Lk_x2_ret\n"
    ".Lk_x2_62:\n"
    "buffer_load_dwordx2 v[234:235], v251, s[32:35], s10 offen sc1\n"
    "s_sub_u32 s6, s6, 1\n"
    "s_cbranch_scc1 .Lk_x2_ret\n"
    ".Lk_x2_63:\n"
    "buffer_load_dwordx2 v[4:5], v251, s[32:35], s54 offen offset:0 sc1\n"
    "s_sub_u32 s6, s6, 1\n"
    "s_cbranch_scc1 .Lk_x2_ret\n"
    ".Lk_x2_64:\n"
    "buffer_load_dwordx2 v[6:7], v251, s[32:35], s54 offen offset:1024 sc1\n"
    "s_sub_u32 s6, s6, 1\n"
    "s_cbranch_scc1 .Lk_x2_ret\n"
    ".Lk_x2_65:\n"
    "buffer_load_dwordx2 v[8:9], v251, s[32:35], s54 offen offset:2048 sc1\n"
    "s_sub_u32 s6, s6, 1\n"
    "s_cbranch_scc1 .Lk_x2_ret\n"
    ".Lk_x2_66:\n"
    "buffer_load_dwordx2 v[10:11], v251, s[32:35], s54 offen offset:3072 sc1\n"
    "s_sub_u32 s6, s6, 1\n"
    "s_cbranch_scc1 .Lk_x2_ret\n"
    ".Lk_x2_67:\n"
    "buffer_load_dwordx2 v[12:13], v236, s[32:35], s54 offen offset:0 sc1\n"
    "s_sub_u32 s6, s6, 1\n"
    "s_cbranch_scc1 .Lk_x2_ret\n"
    ".Lk_x2_ret:\n"
    "s_setpc_b64 s[4:5]\n"
    ".Lk_ool_45:\n"
    "s_nop 1\n"
    "s_mov_b32 s57, 0\n"
    ".Lk_again_110:\n"
    "s_mov_b32 s6, 12\n"
    "s_call_b64 s[4:5], .Lk_x2_4\n"
    "s_waitcnt vmcnt(9)\n"
    "s_call_b64 s[4:5], .Lk_x2c_1\n"
    "s_cbranch_vccz .Lk_there_111\n"
    "s_add_u32 s57, s57, 1\n"
    "s_cmp_lt_u32 s57, 65536\n"
    "s_cbranch_scc1 .Lk_again_110\n"
    ".Lk_there_111:\n"
    "s_waitcnt vmcnt(0)\n"
    "s_nop 1\n"
    "s_branch .Lk_ret_46\n"
    ".Lk_ool_47:\n"
    "s_nop 1\n"
    "s_mov_b32 s57, 0\n"
    ".Lk_again_112:\n"
    "s_mov_b32 s6, 13\n"
    "s_call_b64 s[4:5], .Lk_x2_8\n"
    "s_waitcnt vmcnt(10)\n"
    "s_call_b64 s[4:5], .Lk_x2c_2\n"
    "s_cbranch_vccz .Lk_there_113\n"
    "s_add_u32 s57, s57, 1\n"
    "s_cmp_lt_u32 s57, 65536\n"
    "s_cbranch_scc1 .Lk_again_112\n"
    ".Lk_there_113:\n"
    "s_waitcnt vmcnt(0)\n"
    "s_nop 1\n"
    "s_branch .Lk_ret_48\n"
    ".Lk_ool_49:\n"
    "s_nop 1\n"
    "s_mov_b32 s57, 0\n"
    ".Lk_again_114:\n"
    "s_mov_b32 s6, 14\n"
    "s_call_b64 s[4:5], .Lk_x2_12\n"
    "s_waitcnt vmcnt(11)\n"
    "s_call_b64 s[4:5], .Lk_x2c_3\n"
    "s_cbranch_vccz .Lk_there_115\n"
    "s_add_u32 s57, s57, 1\n"
    "s_cmp_lt_u32 s57, 65536\n"
    "s_cbranch_scc1 .Lk_again_114\n"
    ".Lk_there_115:\n"
    "s_nop 1\n"
    "s_branch .Lk_ret_50\n"
    ".Lk_ool_51:\n"
    "s_nop 1\n"
    "s_mov_b32 s57, 0\n"
    ".Lk_again_116:\n"
    "s_mov_b32 s6, 15\n"
    "s_call_b64 s[4:5], .Lk_x2_16\n"
    "s_waitcnt vmcnt(12)\n"
    "s_call_b64 s[4:5], .Lk_x2c_4\n"
    "s_cbranch_vccz .Lk_there_117\n"
    "s_add_u32 s57, s57, 1\n"
    "s_cmp_lt_u32 s57, 65536\n"
    "s_cbranch_scc1 .Lk_again_116\n"
    ".Lk_there_117:\n"
    "s_nop 1\n"
    "s_branch .Lk_ret_52\n"
    ".Lk_ool_53:\n"
    "s_nop 1\n"
    "s_mov_b32 s57, 0\n"
    ".Lk_again_118:\n"
    "s_mov_b32 s6, 16\n"
    "s_call_b64 s[4:5], .Lk_x2_20\n"
    "s_waitcnt vmcnt(13)\n"
    "s_call_b64 s[4:5], .Lk_x2c_5\n"
    "s_cbranch_vccz .Lk_there_119\n"
    "s_add_u32 s57, s57, 1\n"
    "s_cmp_lt_u32 s57, 65536\n"
    "s_cbranch_scc1 .Lk_again_118\n"
    ".Lk_there_119:\n"
    "s_nop 1\n"
    "s_branch .Lk_ret_54\n"
    ".Lk_ool_55:\n"
    "s_nop 1\n"
    "s_mov_b32 s57, 0\n"
    ".Lk_again_120:\n"
    "s_mov_b32 s6, 17\n"
    "s_call_b64 s[4:5], .Lk_x2_24\n"
    "s_waitcnt vmcnt(14)\n"
    "s_call_b64 s[4:5], .Lk_x2c_0\n"
    "s_cbranch_vccz .Lk_there_121\n"
    "s_add_u32 s57, s57, 1\n"
    "s_cmp_lt_u32 s57, 65536\n"
    "s_cbranch_scc1 .Lk_again_120\n"
    ".Lk_there_121:\n"
    "s_nop 1\n"
    "s_branch .Lk_ret_56\n"
    ".Lk_ool_57:\n"
    "s_nop 1\n"
    "s_mov_b32 s57, 0\n"
    ".Lk_again_122:\n"
    "s_mov_b32 s6, 17\n"
    "s_call_b64 s[4:5], .Lk_x2_29\n"
    "s_waitcnt vmcnt(14)\n"
    "s_call_b64 s[4:5], .Lk_x2c_1\n"
    "s_cbranch_vccz .Lk_there_123\n"
    "s_add_u32 s57, s57, 1\n"
    "s_cmp_lt_u32 s57, 65536\n"
    "s_cbranch_scc1 .Lk_again_122\n"
    ".Lk_there_123:\n"
    "s_nop 1\n"
    "s_branch .Lk_ret_58\n"
    ".Lk_ool_59:\n"
    "s_nop 1\n"
    "s_mov_b32 s57, 0\n"
    ".Lk_again_124:\n"
    "s_mov_b32 s6, 17\n"
    "s_call_b64 s[4:5], .Lk_x2_34\n"
    "s_waitcnt vmcnt(14)\n"
    "s_call_b64 s[4:5], .Lk_x2c_2\n"
    "s_cbranch_vccz .Lk_there_125\n"
    "s_add_u32 s57, s57, 1\n"
    "s_cmp_lt_u32 s57, 65536\n"
    "s_cbranch_scc1 .Lk_again_124\n"
    ".Lk_there_125:\n"
    "s_nop 1\n"
    "s_branch .Lk_ret_60\n"
    ".Lk_ool_61:\n"
    "s_nop 1\n"
    "s_mov_b32 s57, 0\n"
    ".Lk_again_126:\n"
    "s_mov_b32 s6, 17\n"
    "s_call_b64 s[4:5], .Lk_x2_39\n"
    "s_waitcnt vmcnt(14)\n"
    "s_call_b64 s[4:5], .Lk_x2c_3\n"
    "s_cbranch_vccz .Lk_there_127\n"
    "s_add_u32 s57, s57, 1\n"
    "s_cmp_lt_u32 s57, 65536\n"
    "s_cbranch_scc1 .Lk_again_126\n"
    ".Lk_there_127:\n"
    "s_nop 1\n"
    "s_branch .Lk_ret_62\n"
    ".Lk_ool_63:\n"
    "s_nop 1\n"
    "s_mov_b32 s57, 0\n"
    ".Lk_again_128:\n"
    "s_mov_b32 s6, 17\n"
    "s_call_b64 s[4:5], .Lk_x2_44\n"
    "s_waitcnt vmcnt(14)\n"
    "s_call_b64 s[4:5], .Lk_x2c_4\n"
    "s_cbranch_vccz .Lk_there_129\n"
    "s_add_u32 s57, s57, 1\n"
    "s_cmp_lt_u32 s57, 65536\n"
    "s_cbranch_scc1 .Lk_again_128\n"
    ".Lk_there_129:\n"
    "s_nop 1\n"
    "s_branch .Lk_ret_64\n"
    ".Lk_ool_65:\n"
    "s_nop 1\n"
    "s_mov_b32 s57, 0\n"
    ".Lk_again_130:\n"
    "s_mov_b32 s6, 17\n"
    "s_call_b64 s[4:5], .Lk_x2_49\n"
    "s_waitcnt vmcnt(14)\n"
    "s_call_b64 s[4:5], .Lk_x2c_5\n"
    "s_cbranch_vccz .Lk_there_131\n"
    "s_add_u32 s57, s57, 1\n"
    "s_cmp_lt_u32 s57, 65536\n"
    "s_cbranch_scc1 .Lk_again_130\n"
    ".Lk_there_131:\n"
    "s_nop 1\n"
    "s_branch .Lk_ret_66\n"
    ".Lk_ool_67:\n"
    "s_nop 1\n"
    "s_mov_b32 s57, 0\n"
    ".Lk_again_132:\n"
    "s_mov_b32 s6, 13\n"
    "s_call_b64 s[4:5], .Lk_x2_54\n"
    "s_waitcnt vmcnt(10)\n"
    "s_call_b64 s[4:5], .Lk_x2c_0\n"
    "s_cbranch_vccz .Lk_there_133\n"
    "s_add_u32 s57, s57, 1\n"
    "s_cmp_lt_u32 s57, 65536\n"
    "s_cbranch_scc1 .Lk_again_132\n"
    ".Lk_there_133:\n"
    "s_nop 1\n"
    "s_branch .Lk_ret_68\n"
    ".Lk_ool_69:\n"
    "s_nop 1\n"
    "s_mov_b32 s57, 0\n"
    ".Lk_again_134:\n"
    "s_mov_b32 s6, 9\n"
    "s_call_b64 s[4:5], .Lk_x2_58\n"
    "s_waitcnt vmcnt(6)\n"
    "s_call_b64 s[4:5], .Lk_x2c_1\n"
    "s_cbranch_vccz .Lk_there_135\n"
    "s_add_u32 s57, s57, 1\n"
    "s_cmp_lt_u32 s57, 65536\n"
    "s_cbranch_scc1 .Lk_again_134\n"
    ".Lk_there_135:\n"
    "s_nop 1\n"
    "s_branch .Lk_ret_70\n"
    ".Lk_ool_71:\n"
    "s_nop 1\n"
    "s_mov_b32 s57, 0\n"
    "s_waitcnt vmcnt(0) lgkmcnt(0)\n"
    ".Lk_spin_136:\n"
    "buffer_load_dwordx2 v[234:235], v251, s[32:35], s10 offen sc1\n"
    "s_waitcnt vmcnt(0)\n"
    "v_cmp_ne_u32_e64 vcc, s51, v235\n"
    "s_nop 0\n"
    "s_cbranch_vccz .Lk_done_137\n"
    "s_add_u32 s57, s57, 1\n"
    "s_sleep 4\n"
    "s_cmp_lt_u32 s57, 65536\n"
    "s_cbranch_scc1 .Lk_spin_136\n"
    ".Lk_done_137:\n"
    "s_nop 1\n"
    "s_branch .Lk_ret_72\n"
    ".Lk_end:\n"
    :: "{s[0:1]}"(__builtin_amdgcn_kernarg_segment_ptr()), "{s2}"(blockIdx.x), "{v0}"(threadIdx.x), "{s3}"((unsigned)(size_t)smem)
    : "v255", "a255", "s99", "vcc", "memory");
}

extern "C" void kernel_launch(void* const* d_in, const int* in_sizes, int n_in, void* d_out, int out_size, void* d_ws, size_t ws_size,
                              hipStream_t stream) {
  mega<<<256, 256, 0, stream>>>((const u16*)d_in[0], (const u16*)d_in[1], (const u16*)d_in[2], (const u16*)d_in[3], (const u16*)d_in[4],
                                (const u16*)d_in[5], (const u16*)d_in[6], (const u16*)d_in[7], (const u16*)d_in[8], (const u16*)d_in[10],
                                (const u16*)d_in[11], (u16*)d_out, (char*)d_ws);
}
```
